```python
import torch

def _softmax(x, axis=-1):
    return torch.softmax(x, dim=axis)
def _gelu(x):
    return torch.nn.functional.gelu(x, approximate="tanh")
def _silu(x):
    return torch.nn.functional.silu(x)
def _sigmoid(x):
    return torch.sigmoid(x)
def _layernorm(x, axis=-1, eps=1e-5):
    m = x.mean(dim=axis, keepdim=True)
    v = x.var(dim=axis, keepdim=True, unbiased=False)
    return (x - m) / torch.sqrt(v + eps)
def _scatter_add(tgt, idx, src, axis=0):
    return tgt.clone().index_add_(axis, idx, src)
def _segment_sum(src, seg, n):
    out = torch.zeros((n,) + src.shape[1:], dtype=src.dtype)
    return out.index_add_(0, seg, src)
def _pad(x, pw):
    flat = tuple(v for pair in reversed(pw) for v in pair)
    return torch.nn.functional.pad(x, flat)

kernel_name = 'gated_mix__spectral_fnet__geglu_ffn__prompt'

def setup_inputs():

    _w = (lambda a: (lambda t: t.to(torch.bfloat16) if t.is_floating_point() else t)(torch.as_tensor(a)))
    def _A_setup():
        B, S, D = 1, 2048, 1024
        ns = torch.arange(S)[:, None] * torch.arange(S)[None, :]
        nd = torch.arange(D)[:, None] * torch.arange(D)[None, :]
        Ws_re = torch.cos(-2.0 * torch.pi * ns / S).to(float)
        Ws_im = torch.sin(-2.0 * torch.pi * ns / S).to(float)
        Wd_re = torch.cos(-2.0 * torch.pi * nd / D).to(float)
        Wd_im = torch.sin(-2.0 * torch.pi * nd / D).to(float)
        return {
            "x": _w((lambda *s: torch.randn(*s, dtype=torch.bfloat16) * 0.05)(B, S, D)),
            "dft_seq_re": _w(Ws_re),
            "dft_seq_im": _w(Ws_im),
            "dft_hid_re": _w(Wd_re),
            "dft_hid_im": _w(Wd_im),
        }
    _A_in = _A_setup()

    def _B_setup():
        B, S, D, F = 1, 2048, 1024, 3584
        return {
            "x": _w((lambda *s: torch.randn(*s, dtype=torch.bfloat16) * 0.05)(B, S, D)),
            "w_gate": _w((lambda *s: torch.randn(*s, dtype=torch.bfloat16) * 0.4)(D, F)),
            "w_up": _w((lambda *s: torch.randn(*s, dtype=torch.bfloat16) * 0.4)(D, F)),
            "w_down": _w((lambda *s: torch.randn(*s, dtype=torch.bfloat16) * 0.4)(F, D)),
        }
    _B_in = _B_setup()

    return {
        "A_x": _A_in["x"],
        "A_dft_seq_re": _A_in["dft_seq_re"],
        "A_dft_seq_im": _A_in["dft_seq_im"],
        "A_dft_hid_re": _A_in["dft_hid_re"],
        "A_dft_hid_im": _A_in["dft_hid_im"],
        "B_x": _B_in["x"],
        "B_w_gate": _B_in["w_gate"],
        "B_w_up": _B_in["w_up"],
        "B_w_down": _B_in["w_down"],
        "gate_w": _w((lambda *s: torch.randn(*s, dtype=torch.bfloat16) * 0.05)(1024) * 16.0),
    }

def reference(A_x, A_dft_seq_re, A_dft_seq_im, A_dft_hid_re, A_dft_hid_im, B_x, B_w_gate, B_w_up, B_w_down, gate_w):
    def _A_ref(x, dft_seq_re, dft_seq_im, dft_hid_re, dft_hid_im):
        a = torch.einsum("bsd,ed->bse", x, dft_hid_re)
        b = torch.einsum("bsd,ed->bse", x, dft_hid_im)
        y = torch.einsum("ts,bse->bte", dft_seq_re, a) - torch.einsum("ts,bse->bte", dft_seq_im, b)
        return y

    def _B_ref(x, w_gate, w_up, w_down):
        return (_gelu(x @ w_gate) * (x @ w_up)) @ w_down

    x = A_x
    g = _sigmoid((x * gate_w).sum(-1, keepdims=True))
    a = _A_ref(x, A_dft_seq_re, A_dft_seq_im, A_dft_hid_re, A_dft_hid_im)
    b = _B_ref(x, B_w_gate, B_w_up, B_w_down)
    return g * a + (1 - g) * b
```

```cpp
#include <hip/hip_runtime.h>
struct Args { const void* p[12]; };
__global__ __launch_bounds__(256, 1) void k_fused(Args args) {
  __shared__ __attribute__((aligned(16))) char smem[163840];
  asm volatile(
"s_load_dwordx16 s[16:31], %0, 0x0\n"
"s_load_dwordx8 s[32:39], %0, 0x40\n"
"s_mov_b32 s12, %1\n"
"v_lshrrev_b32 v2, 10, %2\n"
"v_bfe_u32 v1, %2, 4, 6\n"
"v_readfirstlane_b32 s13, v2\n"
"s_getpc_b64 s[56:57]\n"
"START_%=:\n"
"s_and_b32 s57, s57, 0xffff\n"
"s_mov_b32 s58, END_%=-START_%=\n"
"s_mov_b32 s59, 0x20000\n"
"v_lshlrev_b32 v20, 3, %2\n"
"buffer_load_dword a255, v20, s[56:59], 0 offen\n"
"v_add_u32 v20, 0x8000, v20\n"
"buffer_load_dword a255, v20, s[56:59], 0 offen\n"
"v_add_u32 v20, 0x8000, v20\n"
"buffer_load_dword a255, v20, s[56:59], 0 offen\n"
"v_add_u32 v20, 0x8000, v20\n"
"s_bfe_u32 s83, s12, 0x20001\n"
"s_and_b32 s14, s12, 1\n"
"s_lshl_b32 s14, s14, 2\n"
"s_bfe_u32 s15, s12, 0x20003\n"
"s_add_u32 s4, s14, s15\n"
"s_lshr_b32 s5, s12, 5\n"
"s_mov_b32 s79, s4\n"
"s_mul_i32 s14, s83, 7\n"
"s_add_u32 s14, s14, s5\n"
"s_add_u32 s15, s83, 28\n"
"s_cmp_lt_u32 s5, 7\n"
"s_cselect_b32 s80, s14, s15\n"
"s_mov_b32 s64, 0\n"
"s_mov_b32 s65, 8192\n"
"s_mov_b32 s66, 262144\n"
"s_mov_b32 s67, 270336\n"
"v_lshrrev_b32 v20, 4, v1\n"
"v_and_b32 v2, 7, v1\n"
"v_xor_b32 v2, v2, v20\n"
"v_lshlrev_b32 v2, 4, v2\n"
"v_lshrrev_b32 v20, 5, v1\n"
"v_bfe_u32 v21, v1, 3, 2\n"
"v_lshl_add_u32 v21, v20, 3, v21\n"
"s_and_b32 s14, s13, 2\n"
"s_lshl_b32 s14, s14, 1\n"
"s_and_b32 s15, s13, 1\n"
"s_add_u32 s15, s14, s15\n"
"s_lshl_b32 s76, s15, 12\n"
"s_lshl_b32 s77, s13, 10\n"
"s_and_b32 s15, s13, 1\n"
"s_lshl_b32 s15, s15, 6\n"
"s_lshl_b32 s14, s14, 3\n"
"s_add_u32 s15, s14, s15\n"
"s_lshl_b32 s14, s79, 8\n"
"s_add_u32 s14, s14, s15\n"
"v_add_u32 v21, s14, v21\n"
"v_lshl_add_u32 v2, v21, 11, v2\n"
"v_xor_b32 v3, 64, v2\n"
"v_add_u32 v3, 32768, v3\n"
"s_cmp_lt_u32 s80, 28\n"
"s_cbranch_scc0 TYP2_%=\n"
"s_cmp_ge_u32 s80, 14\n"
"s_cselect_b32 s82, 1, 0\n"
"s_mul_i32 s14, s82, 14\n"
"s_sub_u32 s14, s80, s14\n"
"s_lshl_b32 s81, s14, 5\n"
"s_mov_b32 s68, 114688\n"
"s_mov_b32 s69, 229376\n"
"s_mov_b32 s70, 344064\n"
"v_lshrrev_b32 v20, 4, v1\n"
"s_lshr_b32 s14, s13, 1\n"
"s_lshl_b32 s14, s14, 2\n"
"v_or_b32 v21, s14, v20\n"
"v_lshlrev_b32 v21, 1, v21\n"
"v_and_b32 v22, 15, v1\n"
"v_xor_b32 v21, v21, v22\n"
"v_lshlrev_b32 v21, 4, v21\n"
"s_lshl_b32 s14, s13, 2\n"
"v_add_u32 v20, s14, v20\n"
"v_mul_u32_u24 v20, 7168, v20\n"
"s_lshl_b32 s14, s80, 8\n"
"v_add3_u32 v5, v20, v21, s14\n"
"v_lshrrev_b32 v20, 4, v1\n"
"v_and_b32 v4, 7, v1\n"
"v_xor_b32 v4, v4, v20\n"
"v_lshlrev_b32 v4, 4, v4\n"
"s_and_b32 s14, s13, 1\n"
"s_lshl_b32 s14, s14, 6\n"
"v_xor_b32 v4, s14, v4\n"
"s_lshl_b32 s14, s13, 3\n"
"s_add_u32 s14, s14, s81\n"
"v_lshrrev_b32 v20, 3, v1\n"
"v_add_u32 v20, s14, v20\n"
"v_lshl_add_u32 v4, v20, 11, v4\n"
"s_and_b32 s78, s12, 7\n"
"s_lshl_b32 s78, s78, 1\n"
"s_min_u32 s78, s78, 13\n"
"s_waitcnt lgkmcnt(0)\n"
"s_mov_b32 s44, s26\n"
"s_and_b32 s45, s27, 0xffff\n"
"s_mov_b32 s46, 7340032\n"
"s_mov_b32 s47, 0x20000\n"
"s_mov_b32 s48, s28\n"
"s_and_b32 s49, s29, 0xffff\n"
"s_mov_b32 s50, 7340032\n"
"s_mov_b32 s51, 0x20000\n"
"s_cmp_eq_u32 s82, 0\n"
"s_cselect_b32 s38, s22, s24\n"
"s_cselect_b32 s39, s23, s25\n"
"s_mov_b32 s52, s38\n"
"s_and_b32 s53, s39, 0xffff\n"
"s_mov_b32 s54, 2097152\n"
"s_mov_b32 s55, 0x20000\n"
"s_mov_b32 s40, s16\n"
"s_and_b32 s41, s17, 0xffff\n"
"s_mov_b32 s42, 4194304\n"
"s_mov_b32 s43, 0x20000\n"
"s_mov_b32 s60, s32\n"
"s_and_b32 s61, s33, 0xffff\n"
"s_mov_b32 s62, 2048\n"
"s_mov_b32 s63, 0x20000\n"
"s_mov_b32 s56, s36\n"
"s_and_b32 s57, s37, 0xffff\n"
"s_mov_b32 s58, 57016320\n"
"s_mov_b32 s59, 0x20000\n"
"s_mov_b32 s16, s78\n"
"s_movk_i32 s17, 128\n"
"s_mov_b32 s28, 458752\n"
"s_add_u32 m0, s77, 65536\n"
"s_nop 0\n"
"buffer_load_dwordx4 v5, s[44:47], 0 offen lds\n"
"s_add_u32 m0, s77, 81920\n"
"s_nop 0\n"
"buffer_load_dwordx4 v5, s[48:51], 0 offen lds\n"
"s_add_u32 m0, s77, 69632\n"
"s_nop 0\n"
"buffer_load_dwordx4 v5, s[44:47], s68 offen lds\n"
"s_add_u32 m0, s77, 86016\n"
"s_nop 0\n"
"buffer_load_dwordx4 v5, s[48:51], s68 offen lds\n"
"s_add_u32 m0, s77, 73728\n"
"s_nop 0\n"
"buffer_load_dwordx4 v5, s[44:47], s69 offen lds\n"
"s_add_u32 m0, s77, 90112\n"
"s_nop 0\n"
"buffer_load_dwordx4 v5, s[48:51], s69 offen lds\n"
"s_add_u32 m0, s77, 77824\n"
"s_nop 0\n"
"buffer_load_dwordx4 v5, s[44:47], s70 offen lds\n"
"s_add_u32 m0, s77, 94208\n"
"s_nop 0\n"
"buffer_load_dwordx4 v5, s[48:51], s70 offen lds\n"
"v_add_u32 v5, s28, v5\n"
"s_add_u32 m0, s77, 131072\n"
"s_nop 0\n"
"buffer_load_dwordx4 v4, s[52:55], 0 offen lds\n"
"v_add_u32 v4, s17, v4\n"
"s_add_u32 m0, s76, 0\n"
"s_nop 0\n"
"buffer_load_dwordx4 v2, s[40:43], s64 offen lds\n"
"s_add_u32 m0, s76, 1024\n"
"s_nop 0\n"
"buffer_load_dwordx4 v3, s[40:43], s64 offen lds\n"
"s_add_u32 m0, s76, 2048\n"
"s_nop 0\n"
"buffer_load_dwordx4 v2, s[40:43], s65 offen lds\n"
"s_add_u32 m0, s76, 3072\n"
"s_nop 0\n"
"buffer_load_dwordx4 v3, s[40:43], s65 offen lds\n"
"s_add_u32 m0, s76, 8192\n"
"s_nop 0\n"
"buffer_load_dwordx4 v2, s[40:43], s66 offen lds\n"
"s_add_u32 m0, s76, 9216\n"
"s_nop 0\n"
"buffer_load_dwordx4 v3, s[40:43], s66 offen lds\n"
"s_add_u32 m0, s76, 10240\n"
"s_nop 0\n"
"buffer_load_dwordx4 v2, s[40:43], s67 offen lds\n"
"v_add_u32 v2, s17, v2\n"
"s_add_u32 m0, s76, 11264\n"
"s_nop 0\n"
"buffer_load_dwordx4 v3, s[40:43], s67 offen lds\n"
"v_add_u32 v3, s17, v3\n"
"s_add_u32 s16, s16, 1\n"
"s_mov_b32 s28, s16\n"
"s_lshl_b32 s17, s28, 7\n"
"s_mul_i32 s28, s28, 458752\n"
"s_add_u32 m0, s77, 98304\n"
"s_nop 0\n"
"buffer_load_dwordx4 v5, s[44:47], 0 offen lds\n"
"v_lshrrev_b32 v20, 4, v1\n"
"v_bfe_u32 v21, v1, 1, 3\n"
"v_xor_b32 v20, v20, v21\n"
"v_lshlrev_b32 v20, 4, v20\n"
"v_and_b32 v21, 15, v1\n"
"v_lshl_add_u32 v6, v21, 7, v20\n"
"s_add_u32 m0, s77, 114688\n"
"s_nop 0\n"
"buffer_load_dwordx4 v5, s[48:51], 0 offen lds\n"
"s_and_b32 s15, s13, 1\n"
"s_mul_i32 s14, s15, 2048\n"
"s_add_u32 s14, s14, 131072\n"
"v_add_u32 v8, s14, v6\n"
"v_xor_b32 v9, 64, v8\n"
"s_add_u32 m0, s77, 102400\n"
"s_nop 0\n"
"buffer_load_dwordx4 v5, s[44:47], s68 offen lds\n"
"s_lshr_b32 s14, s13, 1\n"
"s_lshl_b32 s14, s14, 14\n"
"v_add_u32 v6, s14, v6\n"
"v_xor_b32 v7, 64, v6\n"
"s_lshl_b32 s14, s15, 2\n"
"v_lshrrev_b32 v20, 4, v1\n"
"s_add_u32 m0, s77, 118784\n"
"s_nop 0\n"
"buffer_load_dwordx4 v5, s[48:51], s68 offen lds\n"
"v_bfe_u32 v21, v1, 2, 2\n"
"v_lshl_add_u32 v20, v20, 3, v21\n"
"v_lshlrev_b32 v20, 8, v20\n"
"v_and_b32 v22, 3, v1\n"
"v_lshl_add_u32 v20, v22, 3, v20\n"
"s_add_u32 m0, s77, 106496\n"
"s_nop 0\n"
"buffer_load_dwordx4 v5, s[44:47], s69 offen lds\n"
"v_add_u32 v20, 65536, v20\n"
"v_bfe_u32 v21, v1, 2, 3\n"
"v_xor_b32 v21, s14, v21\n"
"v_xor_b32 v22, 0, v21\n"
"v_lshl_add_u32 v10, v22, 5, v20\n"
"v_xor_b32 v22, 1, v21\n"
"s_add_u32 m0, s77, 122880\n"
"s_nop 0\n"
"buffer_load_dwordx4 v5, s[48:51], s69 offen lds\n"
"v_lshl_add_u32 v11, v22, 5, v20\n"
"v_xor_b32 v22, 2, v21\n"
"v_lshl_add_u32 v12, v22, 5, v20\n"
"v_xor_b32 v22, 3, v21\n"
"v_lshl_add_u32 v13, v22, 5, v20\n"
"s_add_u32 m0, s77, 110592\n"
"s_nop 0\n"
"buffer_load_dwordx4 v5, s[44:47], s70 offen lds\n"
"s_lshr_b32 s14, s12, 3\n"
"s_lshl_b32 s14, s14, 2\n"
"s_add_u32 s14, s14, s13\n"
"s_mul_i32 s15, s14, 14336\n"
"s_mul_i32 s78, s83, 1835008\n"
"s_add_u32 s15, s15, s78\n"
"s_add_u32 m0, s77, 126976\n"
"s_nop 0\n"
"buffer_load_dwordx4 v5, s[48:51], s70 offen lds\n"
"v_add_u32 v5, s28, v5\n"
"v_lshlrev_b32 v14, 5, v1\n"
"v_add_u32 v15, s15, v14\n"
"s_lshl_b32 s14, s14, 15\n"
"s_and_b32 s15, s12, 1\n"
"s_lshl_b32 s15, s15, 22\n"
"s_add_u32 m0, s77, 135168\n"
"s_nop 0\n"
"buffer_load_dwordx4 v4, s[52:55], 0 offen lds\n"
"v_add_u32 v4, s17, v4\n"
"s_add_u32 s14, s14, s15\n"
"s_and_b32 s15, s83, 1\n"
"s_lshl_b32 s15, s15, 11\n"
"s_add_u32 s14, s14, s15\n"
"v_add_u32 v14, s14, v14\n"
"v_mov_b32 v21, 1073741824\n"
"v_mov_b32 v22, 1073741824\n"
"v_mov_b32 v29, 1073741824\n"
"v_mov_b32 v30, 1073741824\n"
"v_mov_b32 v31, 1073741824\n"
"v_mov_b32 v20, 0\n"
"s_lshl_b32 s14, s12, 2\n"
"s_add_u32 s14, s14, s13\n"
"s_cmp_eq_u32 s14, 1\n"
"s_cselect_b32 s6, 1, 2\n"
"s_cmp_eq_u32 s13, 0\n"
"s_cselect_b32 s6, 0, s6\n"
"s_cmp_eq_u32 s6, 0\n"
"s_cbranch_scc1 EV13_%=\n"
"EV13r_%=:\n"
"s_cmp_eq_u32 s6, 1\n"
"s_cbranch_scc1 EV14_%=\n"
"EV14r_%=:\n"
"buffer_store_dword v20, v31, s[56:59], 0 offen sc1\n"
"buffer_load_dword v23, v21, s[56:59], 0 offen sc1\n"
"buffer_load_dwordx4 v[24:27], v29, s[56:59], 0 offen sc1\n"
"buffer_load_dwordx4 v[16:19], v29, s[56:59], 0 offen offset:2048 sc1\n"
"s_cmp_lt_u32 s83, 2\n"
"s_cselect_b32 s38, s18, s20\n"
"s_cselect_b32 s39, s19, s21\n"
"s_mov_b32 s24, s38\n"
"s_and_b32 s25, s39, 0xffff\n"
"s_mov_b32 s26, 8388608\n"
"s_mov_b32 s27, 0x20000\n"
"s_waitcnt vmcnt(17)\n"
"s_barrier\n"
"ds_read_b128 a[212:215], v7 offset:0\n"
"ds_read_b128 a[208:211], v6 offset:0\n"
"ds_read_b64_tr_b16 a[64:65], v10 offset:0\n"
"ds_read_b64_tr_b16 a[66:67], v10 offset:1024\n"
"ds_read_b64_tr_b16 a[68:69], v10 offset:8192\n"
"ds_read_b64_tr_b16 a[70:71], v10 offset:9216\n"
"ds_read_b64_tr_b16 a[72:73], v11 offset:0\n"
"ds_read_b64_tr_b16 a[74:75], v11 offset:1024\n"
"ds_read_b64_tr_b16 a[76:77], v11 offset:8192\n"
"ds_read_b64_tr_b16 a[78:79], v11 offset:9216\n"
"ds_read_b64_tr_b16 a[80:81], v12 offset:0\n"
"ds_read_b64_tr_b16 a[82:83], v12 offset:1024\n"
"ds_read_b64_tr_b16 a[84:85], v12 offset:8192\n"
"ds_read_b64_tr_b16 a[86:87], v12 offset:9216\n"
"ds_read_b64_tr_b16 a[88:89], v13 offset:0\n"
"ds_read_b64_tr_b16 a[90:91], v13 offset:1024\n"
"ds_read_b64_tr_b16 a[92:93], v13 offset:8192\n"
"ds_read_b64_tr_b16 a[94:95], v13 offset:9216\n"
"ds_read_b64_tr_b16 a[96:97], v10 offset:16384\n"
"ds_read_b64_tr_b16 a[98:99], v10 offset:17408\n"
"ds_read_b64_tr_b16 a[100:101], v10 offset:24576\n"
"ds_read_b64_tr_b16 a[102:103], v10 offset:25600\n"
"ds_read_b64_tr_b16 a[104:105], v11 offset:16384\n"
"ds_read_b64_tr_b16 a[106:107], v11 offset:17408\n"
"ds_read_b64_tr_b16 a[108:109], v11 offset:24576\n"
"ds_read_b64_tr_b16 a[110:111], v11 offset:25600\n"
"ds_read_b64_tr_b16 a[112:113], v12 offset:16384\n"
"ds_read_b64_tr_b16 a[114:115], v12 offset:17408\n"
"ds_read_b64_tr_b16 a[116:117], v12 offset:24576\n"
"ds_read_b64_tr_b16 a[118:119], v12 offset:25600\n"
"ds_read_b64_tr_b16 a[120:121], v13 offset:16384\n"
"ds_read_b64_tr_b16 a[122:123], v13 offset:17408\n"
"ds_read_b64_tr_b16 a[124:125], v13 offset:24576\n"
"ds_read_b64_tr_b16 a[126:127], v13 offset:25600\n"
"ds_read_b128 a[128:131], v8 offset:0\n"
"ds_read_b128 a[132:135], v9 offset:0\n"
"ds_read_b128 a[220:223], v7 offset:2048\n"
"ds_read_b128 a[216:219], v6 offset:2048\n"
"v_lshlrev_b32 v250, 4, v1\n"
"s_lshl_b32 s14, s79, 5\n"
"s_add_u32 s14, s14, s80\n"
"s_lshl_b32 s14, s14, 2\n"
"s_add_u32 s78, s14, s13\n"
"s_lshl_b32 s14, s78, 12\n"
"buffer_load_dwordx4 v[224:227], v250, s[60:63], 0 offen offset:0\n"
"buffer_load_dwordx4 v[228:231], v250, s[60:63], 0 offen offset:1024\n"
"buffer_load_dwordx4 v[232:235], v250, s[40:43], s14 offen offset:0\n"
"buffer_load_dwordx4 v[236:239], v250, s[40:43], s14 offen offset:1024\n"
"buffer_load_dwordx4 v[240:243], v250, s[40:43], s14 offen offset:2048\n"
"buffer_load_dwordx4 v[244:247], v250, s[40:43], s14 offen offset:3072\n"
"s_mov_b32 s60, s30\n"
"s_and_b32 s61, s31, 0xffff\n"
"s_mov_b32 s62, 7340032\n"
"s_mov_b32 s63, 0x20000\n"
"s_waitcnt lgkmcnt(14)\n"
"v_mfma_f32_16x16x32_bf16 v[32:35], a[64:67], a[208:211], 0\n"
"ds_read_b128 a[228:231], v7 offset:4096\n"
"v_mfma_f32_16x16x32_bf16 v[32:35], a[68:71], a[212:215], v[32:35]\n"
"ds_read_b128 a[224:227], v6 offset:4096\n"
"v_mfma_f32_16x16x32_bf16 v[36:39], a[76:79], a[212:215], 0\n"
"s_add_u32 m0, s76, 32768\n"
"v_mfma_f32_16x16x32_bf16 v[36:39], a[72:75], a[208:211], v[36:39]\n"
"buffer_load_dwordx4 v2, s[40:43], s64 offen lds\n"
"v_mfma_f32_16x16x32_bf16 v[40:43], a[80:83], a[208:211], 0\n"
"v_mfma_f32_16x16x32_bf16 v[40:43], a[84:87], a[212:215], v[40:43]\n"
"v_mfma_f32_16x16x32_bf16 v[44:47], a[92:95], a[212:215], 0\n"
"s_add_u32 m0, s76, 33792\n"
"v_mfma_f32_16x16x32_bf16 v[44:47], a[88:91], a[208:211], v[44:47]\n"
"buffer_load_dwordx4 v3, s[40:43], s64 offen lds\n"
"v_mfma_f32_16x16x32_bf16 v[48:51], a[96:99], a[208:211], 0\n"
"v_mfma_f32_16x16x32_bf16 v[48:51], a[100:103], a[212:215], v[48:51]\n"
"s_waitcnt lgkmcnt(14)\n"
"v_mfma_f32_16x16x32_bf16 v[52:55], a[108:111], a[212:215], 0\n"
"s_add_u32 m0, s76, 34816\n"
"v_mfma_f32_16x16x32_bf16 v[52:55], a[104:107], a[208:211], v[52:55]\n"
"buffer_load_dwordx4 v2, s[40:43], s65 offen lds\n"
"s_waitcnt lgkmcnt(12)\n"
"v_mfma_f32_16x16x32_bf16 v[56:59], a[112:115], a[208:211], 0\n"
"s_waitcnt lgkmcnt(10)\n"
"v_mfma_f32_16x16x32_bf16 v[56:59], a[116:119], a[212:215], v[56:59]\n"
"s_waitcnt lgkmcnt(6)\n"
"v_mfma_f32_16x16x32_bf16 v[60:63], a[124:127], a[212:215], 0\n"
"s_add_u32 m0, s76, 35840\n"
"v_mfma_f32_16x16x32_bf16 v[60:63], a[120:123], a[208:211], v[60:63]\n"
"buffer_load_dwordx4 v3, s[40:43], s65 offen lds\n"
"s_waitcnt lgkmcnt(5)\n"
"v_mfma_f32_16x16x32_bf16 a[32:35], a[208:211], a[128:131], 0\n"
"s_waitcnt lgkmcnt(4)\n"
"v_mfma_f32_16x16x32_bf16 a[32:35], a[212:215], a[132:135], a[32:35]\n"
"s_waitcnt lgkmcnt(2)\n"
"v_mfma_f32_16x16x32_bf16 a[36:39], a[216:219], a[128:131], 0\n"
"ds_read_b128 a[236:239], v7 offset:6144\n"
"s_add_u32 m0, s76, 40960\n"
"v_mfma_f32_16x16x32_bf16 a[36:39], a[220:223], a[132:135], a[36:39]\n"
"ds_read_b128 a[232:235], v6 offset:6144\n"
"v_mfma_f32_16x16x32_bf16 v[92:95], a[124:127], a[220:223], 0\n"
"buffer_load_dwordx4 v2, s[40:43], s66 offen lds\n"
"v_mfma_f32_16x16x32_bf16 v[92:95], a[120:123], a[216:219], v[92:95]\n"
"v_mfma_f32_16x16x32_bf16 v[88:91], a[112:115], a[216:219], 0\n"
"s_add_u32 m0, s76, 41984\n"
"v_mfma_f32_16x16x32_bf16 v[88:91], a[116:119], a[220:223], v[88:91]\n"
"buffer_load_dwordx4 v3, s[40:43], s66 offen lds\n"
"v_mfma_f32_16x16x32_bf16 v[84:87], a[108:111], a[220:223], 0\n"
"v_mfma_f32_16x16x32_bf16 v[84:87], a[104:107], a[216:219], v[84:87]\n"
"v_mfma_f32_16x16x32_bf16 v[80:83], a[96:99], a[216:219], 0\n"
"s_add_u32 m0, s76, 43008\n"
"v_mfma_f32_16x16x32_bf16 v[80:83], a[100:103], a[220:223], v[80:83]\n"
"buffer_load_dwordx4 v2, s[40:43], s67 offen lds\n"
"v_mfma_f32_16x16x32_bf16 v[76:79], a[92:95], a[220:223], 0\n"
"v_add_u32 v2, s17, v2\n"
"v_mfma_f32_16x16x32_bf16 v[76:79], a[88:91], a[216:219], v[76:79]\n"
"v_mfma_f32_16x16x32_bf16 v[72:75], a[80:83], a[216:219], 0\n"
"s_add_u32 m0, s76, 44032\n"
"v_mfma_f32_16x16x32_bf16 v[72:75], a[84:87], a[220:223], v[72:75]\n"
"buffer_load_dwordx4 v3, s[40:43], s67 offen lds\n"
"v_mfma_f32_16x16x32_bf16 v[68:71], a[76:79], a[220:223], 0\n"
"v_add_u32 v3, s17, v3\n"
"s_add_u32 s16, s16, 1\n"
"v_mfma_f32_16x16x32_bf16 v[68:71], a[72:75], a[216:219], v[68:71]\n"
"s_cmp_eq_u32 s16, 15\n"
"s_cselect_b32 s28, -13, 1\n"
"s_lshl_b32 s17, s28, 7\n"
"v_mfma_f32_16x16x32_bf16 v[64:67], a[64:67], a[216:219], 0\n"
"s_mul_i32 s28, s28, 458752\n"
"v_mfma_f32_16x16x32_bf16 v[64:67], a[68:71], a[220:223], v[64:67]\n"
"s_waitcnt vmcnt(18) lgkmcnt(0)\n"
"s_barrier\n"
"v_mfma_f32_16x16x32_bf16 v[96:99], a[64:67], a[224:227], 0\n"
"ds_read_b128 a[212:215], v7 offset:8192\n"
"s_waitcnt vmcnt(8)\n"
"v_mfma_f32_16x16x32_bf16 v[96:99], a[68:71], a[228:231], v[96:99]\n"
"v_readlane_b32 s7, v23, 1\n"
"v_mfma_f32_16x16x32_bf16 v[100:103], a[76:79], a[228:231], 0\n"
"ds_read_b128 a[208:211], v6 offset:8192\n"
"v_add_u32 v16, 1, v16\n"
"v_mfma_f32_16x16x32_bf16 v[100:103], a[72:75], a[224:227], v[100:103]\n"
"s_add_u32 m0, s77, 65536\n"
"ds_read_b64_tr_b16 a[136:137], v10 offset:32768\n"
"v_mfma_f32_16x16x32_bf16 v[104:107], a[80:83], a[224:227], 0\n"
"v_cmp_eq_u32 vcc, v16, v24\n"
"v_mfma_f32_16x16x32_bf16 v[104:107], a[84:87], a[228:231], v[104:107]\n"
"buffer_load_dwordx4 v5, s[44:47], 0 offen lds\n"
"v_mfma_f32_16x16x32_bf16 v[108:111], a[92:95], a[228:231], 0\n"
"ds_read_b64_tr_b16 a[138:139], v10 offset:33792\n"
"s_nop 1\n"
"v_mfma_f32_16x16x32_bf16 v[108:111], a[88:91], a[224:227], v[108:111]\n"
"v_addc_co_u32 v16, vcc, 0, v16, vcc\n"
"v_add_u32 v17, 1, v17\n"
"v_mfma_f32_16x16x32_bf16 v[112:115], a[96:99], a[224:227], 0\n"
"ds_read_b64_tr_b16 a[140:141], v10 offset:40960\n"
"v_cmp_eq_u32 vcc, v17, v25\n"
"v_mfma_f32_16x16x32_bf16 v[112:115], a[100:103], a[228:231], v[112:115]\n"
"s_add_u32 m0, s77, 81920\n"
"s_nop 1\n"
"buffer_load_dwordx4 v5, s[48:51], 0 offen lds\n"
"v_mfma_f32_16x16x32_bf16 v[116:119], a[108:111], a[228:231], 0\n"
"ds_read_b64_tr_b16 a[142:143], v10 offset:41984\n"
"v_addc_co_u32 v17, vcc, 0, v17, vcc\n"
"v_mfma_f32_16x16x32_bf16 v[116:119], a[104:107], a[224:227], v[116:119]\n"
"v_add_u32 v18, 1, v18\n"
"v_cmp_eq_u32 vcc, v18, v26\n"
"v_mfma_f32_16x16x32_bf16 v[120:123], a[112:115], a[224:227], 0\n"
"ds_read_b64_tr_b16 a[144:145], v11 offset:32768\n"
"s_nop 1\n"
"v_mfma_f32_16x16x32_bf16 v[120:123], a[116:119], a[228:231], v[120:123]\n"
"s_add_u32 m0, s77, 69632\n"
"ds_read_b64_tr_b16 a[146:147], v11 offset:33792\n"
"v_mfma_f32_16x16x32_bf16 v[124:127], a[124:127], a[228:231], 0\n"
"v_addc_co_u32 v18, vcc, 0, v18, vcc\n"
"v_mfma_f32_16x16x32_bf16 v[124:127], a[120:123], a[224:227], v[124:127]\n"
"buffer_load_dwordx4 v5, s[44:47], s68 offen lds\n"
"v_add_u32 v19, 1, v19\n"
"v_mfma_f32_16x16x32_bf16 a[40:43], a[224:227], a[128:131], 0\n"
"ds_read_b64_tr_b16 a[148:149], v11 offset:40960\n"
"v_cmp_eq_u32 vcc, v19, v27\n"
"v_mfma_f32_16x16x32_bf16 a[40:43], a[228:231], a[132:135], a[40:43]\n"
"s_nop 1\n"
"v_addc_co_u32 v19, vcc, 0, v19, vcc\n"
"s_add_u32 m0, s77, 86016\n"
"v_mfma_f32_16x16x32_bf16 a[44:47], a[232:235], a[128:131], 0\n"
"ds_read_b64_tr_b16 a[150:151], v11 offset:41984\n"
"v_mov_b32 v28, s7\n"
"v_mfma_f32_16x16x32_bf16 a[44:47], a[236:239], a[132:135], a[44:47]\n"
"buffer_load_dwordx4 v5, s[48:51], s68 offen lds\n"
"v_add_u32 v28, 1, v28\n"
"v_mfma_f32_16x16x32_bf16 v[156:159], a[124:127], a[236:239], 0\n"
"ds_read_b64_tr_b16 a[152:153], v12 offset:32768\n"
"v_mfma_f32_16x16x32_bf16 v[156:159], a[120:123], a[232:235], v[156:159]\n"
"buffer_store_dwordx4 v[16:19], v29, s[56:59], 0 offen offset:2048 sc1\n"
"v_mfma_f32_16x16x32_bf16 v[152:155], a[112:115], a[232:235], 0\n"
"ds_read_b64_tr_b16 a[154:155], v12 offset:33792\n"
"v_mov_b32 v248, 0\n"
"v_mfma_f32_16x16x32_bf16 v[152:155], a[116:119], a[236:239], v[152:155]\n"
"v_mov_b32 v249, 0\n"
"s_add_u32 m0, s77, 73728\n"
"v_mfma_f32_16x16x32_bf16 v[148:151], a[108:111], a[236:239], 0\n"
"ds_read_b128 a[220:223], v7 offset:10240\n"
"v_dot2c_f32_bf16 v248, v224, v232\n"
"v_mfma_f32_16x16x32_bf16 v[148:151], a[104:107], a[232:235], v[148:151]\n"
"buffer_load_dwordx4 v5, s[44:47], s69 offen lds\n"
"v_mfma_f32_16x16x32_bf16 v[144:147], a[96:99], a[232:235], 0\n"
"ds_read_b128 a[216:219], v6 offset:10240\n"
"v_mfma_f32_16x16x32_bf16 v[144:147], a[100:103], a[236:239], v[144:147]\n"
"ds_read_b64_tr_b16 a[156:157], v12 offset:40960\n"
"v_dot2c_f32_bf16 v249, v224, v240\n"
"v_dot2c_f32_bf16 v248, v225, v233\n"
"v_mfma_f32_16x16x32_bf16 v[140:143], a[92:95], a[236:239], 0\n"
"ds_read_b64_tr_b16 a[158:159], v12 offset:41984\n"
"v_dot2c_f32_bf16 v249, v225, v241\n"
"s_add_u32 m0, s77, 90112\n"
"v_dot2c_f32_bf16 v248, v226, v234\n"
"v_dot2c_f32_bf16 v249, v226, v242\n"
"v_mfma_f32_16x16x32_bf16 v[140:143], a[88:91], a[232:235], v[140:143]\n"
"buffer_load_dwordx4 v5, s[48:51], s69 offen lds\n"
"v_mfma_f32_16x16x32_bf16 v[136:139], a[80:83], a[232:235], 0\n"
"ds_read_b64_tr_b16 a[160:161], v13 offset:32768\n"
"v_dot2c_f32_bf16 v248, v227, v235\n"
"v_mfma_f32_16x16x32_bf16 v[136:139], a[84:87], a[236:239], v[136:139]\n"
"ds_read_b64_tr_b16 a[162:163], v13 offset:33792\n"
"v_dot2c_f32_bf16 v249, v227, v243\n"
"v_dot2c_f32_bf16 v248, v228, v236\n"
"s_add_u32 m0, s77, 77824\n"
"v_mfma_f32_16x16x32_bf16 v[132:135], a[76:79], a[236:239], 0\n"
"ds_read_b64_tr_b16 a[164:165], v13 offset:40960\n"
"v_dot2c_f32_bf16 v249, v228, v244\n"
"v_dot2c_f32_bf16 v248, v229, v237\n"
"v_mfma_f32_16x16x32_bf16 v[132:135], a[72:75], a[232:235], v[132:135]\n"
"buffer_load_dwordx4 v5, s[44:47], s70 offen lds\n"
"v_dot2c_f32_bf16 v249, v229, v245\n"
"v_mfma_f32_16x16x32_bf16 v[128:131], a[64:67], a[232:235], 0\n"
"ds_read_b64_tr_b16 a[166:167], v13 offset:41984\n"
"v_dot2c_f32_bf16 v248, v230, v238\n"
"v_mfma_f32_16x16x32_bf16 v[128:131], a[68:71], a[236:239], v[128:131]\n"
"v_dot2c_f32_bf16 v249, v230, v246\n"
"s_add_u32 m0, s77, 94208\n"
"s_waitcnt lgkmcnt(14)\n"
"v_mfma_f32_16x16x32_bf16 v[160:163], a[64:67], a[208:211], 0\n"
"ds_read_b64_tr_b16 a[168:169], v10 offset:49152\n"
"v_dot2c_f32_bf16 v248, v231, v239\n"
"v_mfma_f32_16x16x32_bf16 v[160:163], a[68:71], a[212:215], v[160:163]\n"
"buffer_load_dwordx4 v5, s[48:51], s70 offen lds\n"
"v_dot2c_f32_bf16 v249, v231, v247\n"
"s_nop 1\n"
"v_add_u32 v5, s28, v5\n"
"v_mfma_f32_16x16x32_bf16 v[164:167], a[76:79], a[212:215], 0\n"
"ds_read_b64_tr_b16 a[170:171], v10 offset:50176\n"
"s_nop 1\n"
"v_mfma_f32_16x16x32_bf16 v[164:167], a[72:75], a[208:211], v[164:167]\n"
"ds_read_b64_tr_b16 a[172:173], v10 offset:57344\n"
"v_add_f32_dpp v248, v248, v248 row_ror:8 row_mask:0xf bank_mask:0xf\n"
"s_add_u32 m0, s77, 131072\n"
"v_mfma_f32_16x16x32_bf16 v[168:171], a[80:83], a[208:211], 0\n"
"v_add_f32_dpp v249, v249, v249 row_ror:8 row_mask:0xf bank_mask:0xf\n"
"v_mfma_f32_16x16x32_bf16 v[168:171], a[84:87], a[212:215], v[168:171]\n"
"buffer_load_dwordx4 v4, s[52:55], 0 offen lds\n"
"v_mfma_f32_16x16x32_bf16 v[172:175], a[92:95], a[212:215], 0\n"
"ds_read_b64_tr_b16 a[174:175], v10 offset:58368\n"
"s_nop 1\n"
"v_add_f32_dpp v248, v248, v248 row_ror:4 row_mask:0xf bank_mask:0xf\n"
"v_add_u32 v4, s17, v4\n"
"v_mfma_f32_16x16x32_bf16 v[172:175], a[88:91], a[208:211], v[172:175]\n"
"ds_read_b128 a[228:231], v7 offset:12288\n"
"v_add_f32_dpp v249, v249, v249 row_ror:4 row_mask:0xf bank_mask:0xf\n"
"v_mfma_f32_16x16x32_bf16 v[176:179], a[96:99], a[208:211], 0\n"
"ds_read_b128 a[224:227], v6 offset:12288\n"
"v_mfma_f32_16x16x32_bf16 v[176:179], a[100:103], a[212:215], v[176:179]\n"
"ds_read_b64_tr_b16 a[176:177], v11 offset:49152\n"
"s_nop 1\n"
"s_add_u32 m0, s76, 0\n"
"v_add_f32_dpp v248, v248, v248 row_ror:2 row_mask:0xf bank_mask:0xf\n"
"v_mfma_f32_16x16x32_bf16 v[180:183], a[108:111], a[212:215], 0\n"
"buffer_load_dwordx4 v2, s[40:43], s64 offen lds\n"
"v_mfma_f32_16x16x32_bf16 v[180:183], a[104:107], a[208:211], v[180:183]\n"
"ds_read_b64_tr_b16 a[178:179], v11 offset:50176\n"
"v_add_f32_dpp v249, v249, v249 row_ror:2 row_mask:0xf bank_mask:0xf\n"
"v_mfma_f32_16x16x32_bf16 v[184:187], a[112:115], a[208:211], 0\n"
"ds_read_b64_tr_b16 a[180:181], v11 offset:57344\n"
"s_nop 1\n"
"v_add_f32_dpp v248, v248, v248 row_ror:1 row_mask:0xf bank_mask:0xf\n"
"v_add_f32_dpp v249, v249, v249 row_ror:1 row_mask:0xf bank_mask:0xf\n"
"v_mfma_f32_16x16x32_bf16 v[184:187], a[116:119], a[212:215], v[184:187]\n"
"ds_read_b64_tr_b16 a[182:183], v11 offset:58368\n"
"s_nop 1\n"
"v_readlane_b32 s14, v248, 16\n"
"v_mfma_f32_16x16x32_bf16 v[188:191], a[124:127], a[212:215], 0\n"
"ds_read_b64_tr_b16 a[184:185], v12 offset:49152\n"
"v_readlane_b32 s15, v248, 32\n"
"v_readlane_b32 s38, v248, 48\n"
"v_mfma_f32_16x16x32_bf16 v[188:191], a[120:123], a[208:211], v[188:191]\n"
"s_nop 1\n"
"ds_read_b64_tr_b16 a[186:187], v12 offset:50176\n"
"v_mfma_f32_16x16x32_bf16 a[48:51], a[208:211], a[128:131], 0\n"
"v_add_f32 v248, s14, v248\n"
"s_add_u32 m0, s76, 1024\n"
"v_mfma_f32_16x16x32_bf16 a[48:51], a[212:215], a[132:135], a[48:51]\n"
"ds_read_b64_tr_b16 a[188:189], v12 offset:57344\n"
"v_add_f32 v248, s15, v248\n"
"s_waitcnt lgkmcnt(14)\n"
"v_mfma_f32_16x16x32_bf16 a[52:55], a[216:219], a[128:131], 0\n"
"buffer_load_dwordx4 v3, s[40:43], s64 offen lds\n"
"v_add_f32 v248, s38, v248\n"
"v_mfma_f32_16x16x32_bf16 a[52:55], a[220:223], a[132:135], a[52:55]\n"
"ds_read_b64_tr_b16 a[190:191], v12 offset:58368\n"
"v_mul_f32 v248, 0xbfb8aa3b, v248\n"
"v_exp_f32 v248, v248\n"
"v_mfma_f32_16x16x32_bf16 v[220:223], a[124:127], a[220:223], 0\n"
"s_nop 0\n"
"ds_read_b64_tr_b16 a[192:193], v13 offset:49152\n"
"v_mfma_f32_16x16x32_bf16 v[220:223], a[120:123], a[216:219], v[220:223]\n"
"v_add_f32 v248, 1.0, v248\n"
"v_mfma_f32_16x16x32_bf16 v[216:219], a[112:115], a[216:219], 0\n"
"v_rcp_f32 v252, v248\n"
"v_mfma_f32_16x16x32_bf16 v[216:219], a[116:119], a[220:223], v[216:219]\n"
"ds_read_b64_tr_b16 a[194:195], v13 offset:50176\n"
"s_nop 1\n"
"v_readlane_b32 s14, v249, 16\n"
"v_mfma_f32_16x16x32_bf16 v[212:215], a[108:111], a[220:223], 0\n"
"ds_read_b128 a[236:239], v7 offset:14336\n"
"v_mfma_f32_16x16x32_bf16 v[212:215], a[104:107], a[216:219], v[212:215]\n"
"ds_read_b64_tr_b16 a[196:197], v13 offset:57344\n"
"v_readlane_b32 s15, v249, 32\n"
"v_mfma_f32_16x16x32_bf16 v[208:211], a[96:99], a[216:219], 0\n"
"ds_read_b128 a[232:235], v6 offset:14336\n"
"v_readlane_b32 s38, v249, 48\n"
"s_add_u32 m0, s76, 2048\n"
"v_mfma_f32_16x16x32_bf16 v[208:211], a[100:103], a[220:223], v[208:211]\n"
"ds_read_b64_tr_b16 a[198:199], v13 offset:58368\n"
"s_nop 1\n"
"v_mfma_f32_16x16x32_bf16 v[204:207], a[92:95], a[220:223], 0\n"
"buffer_load_dwordx4 v2, s[40:43], s65 offen lds\n"
"v_add_f32 v249, s14, v249\n"
"v_mfma_f32_16x16x32_bf16 v[204:207], a[88:91], a[216:219], v[204:207]\n"
"ds_read_b128 a[200:203], v8 offset:4096\n"
"v_add_f32 v249, s15, v249\n"
"v_add_f32 v249, s38, v249\n"
"v_mul_f32 v249, 0xbfb8aa3b, v249\n"
"v_mfma_f32_16x16x32_bf16 v[200:203], a[80:83], a[216:219], 0\n"
"ds_read_b128 a[204:207], v9 offset:4096\n"
"v_exp_f32 v249, v249\n"
"v_mfma_f32_16x16x32_bf16 v[200:203], a[84:87], a[220:223], v[200:203]\n"
"s_nop 0\n"
"v_add_f32 v249, 1.0, v249\n"
"v_mfma_f32_16x16x32_bf16 v[196:199], a[76:79], a[220:223], 0\n"
"v_rcp_f32 v253, v249\n"
"v_mfma_f32_16x16x32_bf16 v[196:199], a[72:75], a[216:219], v[196:199]\n"
"s_lshl_b32 s14, s78, 3\n"
"s_add_u32 s14, s14, 65536\n"
"s_add_u32 m0, s76, 3072\n"
"v_mfma_f32_16x16x32_bf16 v[192:195], a[64:67], a[216:219], 0\n"
"s_mov_b64 exec, 1\n"
"buffer_store_dwordx2 v[252:253], v250, s[56:59], s14 offen sc1\n"
"s_mov_b64 exec, -1\n"
"v_mfma_f32_16x16x32_bf16 v[192:195], a[68:71], a[220:223], v[192:195]\n"
"buffer_load_dwordx4 v3, s[40:43], s65 offen lds\n"
"s_mov_b32 s78, 5\n"
"s_waitcnt lgkmcnt(14)\n"
"v_mfma_f32_16x16x32_bf16 v[224:227], a[64:67], a[224:227], 0\n"
"s_waitcnt vmcnt(19) lgkmcnt(0)\n"
"s_barrier\n"
"v_mfma_f32_16x16x32_bf16 v[224:227], a[68:71], a[228:231], v[224:227]\n"
"ds_read_b128 a[212:215], v7 offset:32768\n"
"v_mfma_f32_16x16x32_bf16 v[228:231], a[76:79], a[228:231], 0\n"
"ds_read_b128 a[208:211], v6 offset:32768\n"
"s_add_u32 m0, s76, 8192\n"
"v_mfma_f32_16x16x32_bf16 v[228:231], a[72:75], a[224:227], v[228:231]\n"
"buffer_load_dwordx4 v2, s[40:43], s66 offen lds\n"
"v_mfma_f32_16x16x32_bf16 v[232:235], a[80:83], a[224:227], 0\n"
"buffer_load_dword a255, v14, s[24:27], 0 offen\n"
"v_add_u32 v14, 4096, v14\n"
"v_mfma_f32_16x16x32_bf16 v[232:235], a[84:87], a[228:231], v[232:235]\n"
"v_mfma_f32_16x16x32_bf16 v[236:239], a[92:95], a[228:231], 0\n"
"v_mfma_f32_16x16x32_bf16 v[236:239], a[88:91], a[224:227], v[236:239]\n"
"v_mfma_f32_16x16x32_bf16 v[240:243], a[96:99], a[224:227], 0\n"
"v_mfma_f32_16x16x32_bf16 v[240:243], a[100:103], a[228:231], v[240:243]\n"
"v_mfma_f32_16x16x32_bf16 v[244:247], a[108:111], a[228:231], 0\n"
"s_add_u32 m0, s76, 9216\n"
"v_mfma_f32_16x16x32_bf16 v[244:247], a[104:107], a[224:227], v[244:247]\n"
"buffer_load_dwordx4 v3, s[40:43], s66 offen lds\n"
"v_mfma_f32_16x16x32_bf16 v[248:251], a[112:115], a[224:227], 0\n"
"v_mfma_f32_16x16x32_bf16 v[248:251], a[116:119], a[228:231], v[248:251]\n"
"v_mfma_f32_16x16x32_bf16 v[252:255], a[124:127], a[228:231], 0\n"
"v_mfma_f32_16x16x32_bf16 v[252:255], a[120:123], a[224:227], v[252:255]\n"
"v_mfma_f32_16x16x32_bf16 a[56:59], a[224:227], a[128:131], 0\n"
"v_mfma_f32_16x16x32_bf16 a[56:59], a[228:231], a[132:135], a[56:59]\n"
"v_mfma_f32_16x16x32_bf16 a[60:63], a[232:235], a[128:131], 0\n"
"ds_read_b128 a[220:223], v7 offset:34816\n"
"v_mfma_f32_16x16x32_bf16 a[60:63], a[236:239], a[132:135], a[60:63]\n"
"ds_read_b128 a[216:219], v6 offset:34816\n"
"s_add_u32 m0, s76, 10240\n"
"v_mfma_f32_16x16x32_bf16 a[28:31], a[124:127], a[236:239], 0\n"
"buffer_load_dwordx4 v2, s[40:43], s67 offen lds\n"
"v_mfma_f32_16x16x32_bf16 a[28:31], a[120:123], a[232:235], a[28:31]\n"
"v_add_u32 v2, s17, v2\n"
"v_mfma_f32_16x16x32_bf16 a[24:27], a[112:115], a[232:235], 0\n"
"v_mfma_f32_16x16x32_bf16 a[24:27], a[116:119], a[236:239], a[24:27]\n"
"v_mfma_f32_16x16x32_bf16 a[20:23], a[108:111], a[236:239], 0\n"
"v_mfma_f32_16x16x32_bf16 a[20:23], a[104:107], a[232:235], a[20:23]\n"
"v_mfma_f32_16x16x32_bf16 a[16:19], a[96:99], a[232:235], 0\n"
"v_mfma_f32_16x16x32_bf16 a[16:19], a[100:103], a[236:239], a[16:19]\n"
"v_mfma_f32_16x16x32_bf16 a[12:15], a[92:95], a[236:239], 0\n"
"s_add_u32 m0, s76, 11264\n"
"v_mfma_f32_16x16x32_bf16 a[12:15], a[88:91], a[232:235], a[12:15]\n"
"buffer_load_dwordx4 v3, s[40:43], s67 offen lds\n"
"v_mfma_f32_16x16x32_bf16 a[8:11], a[80:83], a[232:235], 0\n"
"v_add_u32 v3, s17, v3\n"
"s_add_u32 s16, s16, 1\n"
"v_mfma_f32_16x16x32_bf16 a[8:11], a[84:87], a[236:239], a[8:11]\n"
"s_cmp_eq_u32 s16, 15\n"
"s_cselect_b32 s28, -13, 1\n"
"s_lshl_b32 s17, s28, 7\n"
"v_mfma_f32_16x16x32_bf16 a[4:7], a[76:79], a[236:239], 0\n"
"s_mul_i32 s28, s28, 458752\n"
"v_mfma_f32_16x16x32_bf16 a[4:7], a[72:75], a[232:235], a[4:7]\n"
"v_mfma_f32_16x16x32_bf16 a[0:3], a[64:67], a[232:235], 0\n"
"v_mfma_f32_16x16x32_bf16 a[0:3], a[68:71], a[236:239], a[0:3]\n"
"s_waitcnt lgkmcnt(2)\n"
"v_mfma_f32_16x16x32_bf16 v[32:35], a[136:139], a[208:211], v[32:35]\n"
"ds_read_b128 a[228:231], v7 offset:36864\n"
"v_mfma_f32_16x16x32_bf16 v[32:35], a[140:143], a[212:215], v[32:35]\n"
"ds_read_b128 a[224:227], v6 offset:36864\n"
"s_add_u32 m0, s77, 98304\n"
"v_mfma_f32_16x16x32_bf16 v[36:39], a[148:151], a[212:215], v[36:39]\n"
"buffer_load_dwordx4 v5, s[44:47], 0 offen lds\n"
"v_mfma_f32_16x16x32_bf16 v[36:39], a[144:147], a[208:211], v[36:39]\n"
"v_mfma_f32_16x16x32_bf16 v[40:43], a[152:155], a[208:211], v[40:43]\n"
"v_mfma_f32_16x16x32_bf16 v[40:43], a[156:159], a[212:215], v[40:43]\n"
"v_mfma_f32_16x16x32_bf16 v[44:47], a[164:167], a[212:215], v[44:47]\n"
"v_mfma_f32_16x16x32_bf16 v[44:47], a[160:163], a[208:211], v[44:47]\n"
"v_mfma_f32_16x16x32_bf16 v[48:51], a[168:171], a[208:211], v[48:51]\n"
"s_add_u32 m0, s77, 114688\n"
"v_mfma_f32_16x16x32_bf16 v[48:51], a[172:175], a[212:215], v[48:51]\n"
"buffer_load_dwordx4 v5, s[48:51], 0 offen lds\n"
"v_mfma_f32_16x16x32_bf16 v[52:55], a[180:183], a[212:215], v[52:55]\n"
"v_mfma_f32_16x16x32_bf16 v[52:55], a[176:179], a[208:211], v[52:55]\n"
"v_mfma_f32_16x16x32_bf16 v[56:59], a[184:187], a[208:211], v[56:59]\n"
"v_mfma_f32_16x16x32_bf16 v[56:59], a[188:191], a[212:215], v[56:59]\n"
"v_mfma_f32_16x16x32_bf16 v[60:63], a[196:199], a[212:215], v[60:63]\n"
"v_mfma_f32_16x16x32_bf16 v[60:63], a[192:195], a[208:211], v[60:63]\n"
"s_add_u32 m0, s77, 102400\n"
"v_mfma_f32_16x16x32_bf16 a[32:35], a[208:211], a[200:203], a[32:35]\n"
"buffer_load_dwordx4 v5, s[44:47], s68 offen lds\n"
"v_mfma_f32_16x16x32_bf16 a[32:35], a[212:215], a[204:207], a[32:35]\n"
"s_waitcnt lgkmcnt(2)\n"
"v_mfma_f32_16x16x32_bf16 a[36:39], a[216:219], a[200:203], a[36:39]\n"
"ds_read_b128 a[236:239], v7 offset:38912\n"
"v_mfma_f32_16x16x32_bf16 a[36:39], a[220:223], a[204:207], a[36:39]\n"
"ds_read_b128 a[232:235], v6 offset:38912\n"
"v_mfma_f32_16x16x32_bf16 v[92:95], a[196:199], a[220:223], v[92:95]\n"
"v_mfma_f32_16x16x32_bf16 v[92:95], a[192:195], a[216:219], v[92:95]\n"
"v_mfma_f32_16x16x32_bf16 v[88:91], a[184:187], a[216:219], v[88:91]\n"
"s_add_u32 m0, s77, 118784\n"
"v_mfma_f32_16x16x32_bf16 v[88:91], a[188:191], a[220:223], v[88:91]\n"
"buffer_load_dwordx4 v5, s[48:51], s68 offen lds\n"
"v_mfma_f32_16x16x32_bf16 v[84:87], a[180:183], a[220:223], v[84:87]\n"
"v_mfma_f32_16x16x32_bf16 v[84:87], a[176:179], a[216:219], v[84:87]\n"
"v_mfma_f32_16x16x32_bf16 v[80:83], a[168:171], a[216:219], v[80:83]\n"
"v_mfma_f32_16x16x32_bf16 v[80:83], a[172:175], a[220:223], v[80:83]\n"
"v_mfma_f32_16x16x32_bf16 v[76:79], a[164:167], a[220:223], v[76:79]\n"
"v_mfma_f32_16x16x32_bf16 v[76:79], a[160:163], a[216:219], v[76:79]\n"
"s_add_u32 m0, s77, 106496\n"
"v_mfma_f32_16x16x32_bf16 v[72:75], a[152:155], a[216:219], v[72:75]\n"
"buffer_load_dwordx4 v5, s[44:47], s69 offen lds\n"
"v_mfma_f32_16x16x32_bf16 v[72:75], a[156:159], a[220:223], v[72:75]\n"
"v_mfma_f32_16x16x32_bf16 v[68:71], a[148:151], a[220:223], v[68:71]\n"
"v_mfma_f32_16x16x32_bf16 v[68:71], a[144:147], a[216:219], v[68:71]\n"
"v_mfma_f32_16x16x32_bf16 v[64:67], a[136:139], a[216:219], v[64:67]\n"
"v_mfma_f32_16x16x32_bf16 v[64:67], a[140:143], a[220:223], v[64:67]\n"
"s_waitcnt vmcnt(15) lgkmcnt(0)\n"
"s_barrier\n"
"v_mfma_f32_16x16x32_bf16 v[96:99], a[136:139], a[224:227], v[96:99]\n"
"ds_read_b128 a[212:215], v7 offset:40960\n"
"s_add_u32 m0, s77, 122880\n"
"v_mfma_f32_16x16x32_bf16 v[96:99], a[140:143], a[228:231], v[96:99]\n"
"ds_read_b128 a[208:211], v6 offset:40960\n"
"v_mfma_f32_16x16x32_bf16 v[100:103], a[148:151], a[228:231], v[100:103]\n"
"buffer_load_dwordx4 v5, s[48:51], s69 offen lds\n"
"v_mfma_f32_16x16x32_bf16 v[100:103], a[144:147], a[224:227], v[100:103]\n"
"ds_read_b64_tr_b16 a[64:65], v10 offset:0\n"
"v_mfma_f32_16x16x32_bf16 v[104:107], a[152:155], a[224:227], v[104:107]\n"
"ds_read_b64_tr_b16 a[66:67], v10 offset:1024\n"
"v_mfma_f32_16x16x32_bf16 v[104:107], a[156:159], a[228:231], v[104:107]\n"
"ds_read_b64_tr_b16 a[68:69], v10 offset:8192\n"
"v_mfma_f32_16x16x32_bf16 v[108:111], a[164:167], a[228:231], v[108:111]\n"
"v_mfma_f32_16x16x32_bf16 v[108:111], a[160:163], a[224:227], v[108:111]\n"
"ds_read_b64_tr_b16 a[70:71], v10 offset:9216\n"
"s_add_u32 m0, s77, 110592\n"
"v_mfma_f32_16x16x32_bf16 v[112:115], a[168:171], a[224:227], v[112:115]\n"
"buffer_load_dwordx4 v5, s[44:47], s70 offen lds\n"
"v_mfma_f32_16x16x32_bf16 v[112:115], a[172:175], a[228:231], v[112:115]\n"
"ds_read_b64_tr_b16 a[72:73], v11 offset:0\n"
"v_mfma_f32_16x16x32_bf16 v[116:119], a[180:183], a[228:231], v[116:119]\n"
"ds_read_b64_tr_b16 a[74:75], v11 offset:1024\n"
"v_mfma_f32_16x16x32_bf16 v[116:119], a[176:179], a[224:227], v[116:119]\n"
"v_mfma_f32_16x16x32_bf16 v[120:123], a[184:187], a[224:227], v[120:123]\n"
"ds_read_b64_tr_b16 a[76:77], v11 offset:8192\n"
"v_mfma_f32_16x16x32_bf16 v[120:123], a[188:191], a[228:231], v[120:123]\n"
"v_mfma_f32_16x16x32_bf16 v[124:127], a[196:199], a[228:231], v[124:127]\n"
"ds_read_b64_tr_b16 a[78:79], v11 offset:9216\n"
"s_add_u32 m0, s77, 126976\n"
"v_mfma_f32_16x16x32_bf16 v[124:127], a[192:195], a[224:227], v[124:127]\n"
"buffer_load_dwordx4 v5, s[48:51], s70 offen lds\n"
"v_mfma_f32_16x16x32_bf16 a[40:43], a[224:227], a[200:203], a[40:43]\n"
"ds_read_b64_tr_b16 a[80:81], v12 offset:0\n"
"v_add_u32 v5, s28, v5\n"
"v_mfma_f32_16x16x32_bf16 a[40:43], a[228:231], a[204:207], a[40:43]\n"
"ds_read_b64_tr_b16 a[82:83], v12 offset:1024\n"
"v_mfma_f32_16x16x32_bf16 a[44:47], a[232:235], a[200:203], a[44:47]\n"
"ds_read_b128 a[220:223], v7 offset:43008\n"
"v_mfma_f32_16x16x32_bf16 a[44:47], a[236:239], a[204:207], a[44:47]\n"
"ds_read_b128 a[216:219], v6 offset:43008\n"
"v_mfma_f32_16x16x32_bf16 v[156:159], a[196:199], a[236:239], v[156:159]\n"
"ds_read_b64_tr_b16 a[84:85], v12 offset:8192\n"
"v_mfma_f32_16x16x32_bf16 v[156:159], a[192:195], a[232:235], v[156:159]\n"
"ds_read_b64_tr_b16 a[86:87], v12 offset:9216\n"
"s_add_u32 m0, s77, 135168\n"
"v_mfma_f32_16x16x32_bf16 v[152:155], a[184:187], a[232:235], v[152:155]\n"
"buffer_load_dwordx4 v4, s[52:55], 0 offen lds\n"
"v_mfma_f32_16x16x32_bf16 v[152:155], a[188:191], a[236:239], v[152:155]\n"
"ds_read_b64_tr_b16 a[88:89], v13 offset:0\n"
"v_add_u32 v4, s17, v4\n"
"v_mfma_f32_16x16x32_bf16 v[148:151], a[180:183], a[236:239], v[148:151]\n"
"ds_read_b64_tr_b16 a[90:91], v13 offset:1024\n"
"v_mfma_f32_16x16x32_bf16 v[148:151], a[176:179], a[232:235], v[148:151]\n"
"v_mfma_f32_16x16x32_bf16 v[144:147], a[168:171], a[232:235], v[144:147]\n"
"ds_read_b64_tr_b16 a[92:93], v13 offset:8192\n"
"v_mfma_f32_16x16x32_bf16 v[144:147], a[172:175], a[236:239], v[144:147]\n"
"v_mfma_f32_16x16x32_bf16 v[140:143], a[164:167], a[236:239], v[140:143]\n"
"ds_read_b64_tr_b16 a[94:95], v13 offset:9216\n"
"v_mfma_f32_16x16x32_bf16 v[140:143], a[160:163], a[232:235], v[140:143]\n"
"v_mfma_f32_16x16x32_bf16 v[136:139], a[152:155], a[232:235], v[136:139]\n"
"ds_read_b64_tr_b16 a[96:97], v10 offset:16384\n"
"v_mfma_f32_16x16x32_bf16 v[136:139], a[156:159], a[236:239], v[136:139]\n"
"v_mfma_f32_16x16x32_bf16 v[132:135], a[148:151], a[236:239], v[132:135]\n"
"ds_read_b64_tr_b16 a[98:99], v10 offset:17408\n"
"v_mfma_f32_16x16x32_bf16 v[132:135], a[144:147], a[232:235], v[132:135]\n"
"ds_read_b64_tr_b16 a[100:101], v10 offset:24576\n"
"v_mfma_f32_16x16x32_bf16 v[128:131], a[136:139], a[232:235], v[128:131]\n"
"v_mfma_f32_16x16x32_bf16 v[128:131], a[140:143], a[236:239], v[128:131]\n"
"ds_read_b64_tr_b16 a[102:103], v10 offset:25600\n"
"s_waitcnt lgkmcnt(14)\n"
"v_mfma_f32_16x16x32_bf16 v[160:163], a[136:139], a[208:211], v[160:163]\n"
"ds_read_b128 a[228:231], v7 offset:45056\n"
"v_mfma_f32_16x16x32_bf16 v[160:163], a[140:143], a[212:215], v[160:163]\n"
"ds_read_b128 a[224:227], v6 offset:45056\n"
"v_mfma_f32_16x16x32_bf16 v[164:167], a[148:151], a[212:215], v[164:167]\n"
"ds_read_b64_tr_b16 a[104:105], v11 offset:16384\n"
"s_add_u32 m0, s76, 32768\n"
"v_mfma_f32_16x16x32_bf16 v[164:167], a[144:147], a[208:211], v[164:167]\n"
"buffer_load_dwordx4 v2, s[40:43], s64 offen lds\n"
"v_mfma_f32_16x16x32_bf16 v[168:171], a[152:155], a[208:211], v[168:171]\n"
"ds_read_b64_tr_b16 a[106:107], v11 offset:17408\n"
"v_mfma_f32_16x16x32_bf16 v[168:171], a[156:159], a[212:215], v[168:171]\n"
"ds_read_b64_tr_b16 a[108:109], v11 offset:24576\n"
"v_mfma_f32_16x16x32_bf16 v[172:175], a[164:167], a[212:215], v[172:175]\n"
"ds_read_b64_tr_b16 a[110:111], v11 offset:25600\n"
"v_mfma_f32_16x16x32_bf16 v[172:175], a[160:163], a[208:211], v[172:175]\n"
"v_mfma_f32_16x16x32_bf16 v[176:179], a[168:171], a[208:211], v[176:179]\n"
"ds_read_b64_tr_b16 a[112:113], v12 offset:16384\n"
"v_mfma_f32_16x16x32_bf16 v[176:179], a[172:175], a[212:215], v[176:179]\n"
"v_mfma_f32_16x16x32_bf16 v[180:183], a[180:183], a[212:215], v[180:183]\n"
"ds_read_b64_tr_b16 a[114:115], v12 offset:17408\n"
"s_add_u32 m0, s76, 33792\n"
"v_mfma_f32_16x16x32_bf16 v[180:183], a[176:179], a[208:211], v[180:183]\n"
"ds_read_b64_tr_b16 a[116:117], v12 offset:24576\n"
"v_mfma_f32_16x16x32_bf16 v[184:187], a[184:187], a[208:211], v[184:187]\n"
"buffer_load_dwordx4 v3, s[40:43], s64 offen lds\n"
"v_mfma_f32_16x16x32_bf16 v[184:187], a[188:191], a[212:215], v[184:187]\n"
"ds_read_b64_tr_b16 a[118:119], v12 offset:25600\n"
"v_mfma_f32_16x16x32_bf16 v[188:191], a[196:199], a[212:215], v[188:191]\n"
"v_mfma_f32_16x16x32_bf16 v[188:191], a[192:195], a[208:211], v[188:191]\n"
"ds_read_b64_tr_b16 a[120:121], v13 offset:16384\n"
"v_mfma_f32_16x16x32_bf16 a[48:51], a[208:211], a[200:203], a[48:51]\n"
"v_mfma_f32_16x16x32_bf16 a[48:51], a[212:215], a[204:207], a[48:51]\n"
"ds_read_b64_tr_b16 a[122:123], v13 offset:17408\n"
"s_waitcnt lgkmcnt(14)\n"
"v_mfma_f32_16x16x32_bf16 a[52:55], a[216:219], a[200:203], a[52:55]\n"
"ds_read_b128 a[236:239], v7 offset:47104\n"
"v_mfma_f32_16x16x32_bf16 a[52:55], a[220:223], a[204:207], a[52:55]\n"
"ds_read_b64_tr_b16 a[124:125], v13 offset:24576\n"
"v_mfma_f32_16x16x32_bf16 v[220:223], a[196:199], a[220:223], v[220:223]\n"
"ds_read_b128 a[232:235], v6 offset:47104\n"
"s_add_u32 m0, s76, 34816\n"
"v_mfma_f32_16x16x32_bf16 v[220:223], a[192:195], a[216:219], v[220:223]\n"
"ds_read_b64_tr_b16 a[126:127], v13 offset:25600\n"
"v_mfma_f32_16x16x32_bf16 v[216:219], a[184:187], a[216:219], v[216:219]\n"
"buffer_load_dwordx4 v2, s[40:43], s65 offen lds\n"
"v_mfma_f32_16x16x32_bf16 v[216:219], a[188:191], a[220:223], v[216:219]\n"
"ds_read_b128 a[128:131], v8 offset:0\n"
"v_mfma_f32_16x16x32_bf16 v[212:215], a[180:183], a[220:223], v[212:215]\n"
"ds_read_b128 a[132:135], v9 offset:0\n"
"v_mfma_f32_16x16x32_bf16 v[212:215], a[176:179], a[216:219], v[212:215]\n"
"v_mfma_f32_16x16x32_bf16 v[208:211], a[168:171], a[216:219], v[208:211]\n"
"v_mfma_f32_16x16x32_bf16 v[208:211], a[172:175], a[220:223], v[208:211]\n"
"v_mfma_f32_16x16x32_bf16 v[204:207], a[164:167], a[220:223], v[204:207]\n"
"s_add_u32 m0, s76, 35840\n"
"v_mfma_f32_16x16x32_bf16 v[204:207], a[160:163], a[216:219], v[204:207]\n"
"buffer_load_dwordx4 v3, s[40:43], s65 offen lds\n"
"v_mfma_f32_16x16x32_bf16 v[200:203], a[152:155], a[216:219], v[200:203]\n"
"v_mfma_f32_16x16x32_bf16 v[200:203], a[156:159], a[220:223], v[200:203]\n"
"v_mfma_f32_16x16x32_bf16 v[196:199], a[148:151], a[220:223], v[196:199]\n"
"v_mfma_f32_16x16x32_bf16 v[196:199], a[144:147], a[216:219], v[196:199]\n"
"v_mfma_f32_16x16x32_bf16 v[192:195], a[136:139], a[216:219], v[192:195]\n"
"v_mfma_f32_16x16x32_bf16 v[192:195], a[140:143], a[220:223], v[192:195]\n"
"s_waitcnt vmcnt(18) lgkmcnt(0)\n"
"s_barrier\n"
"v_mfma_f32_16x16x32_bf16 v[224:227], a[136:139], a[224:227], v[224:227]\n"
"ds_read_b128 a[212:215], v7 offset:0\n"
"v_mfma_f32_16x16x32_bf16 v[224:227], a[140:143], a[228:231], v[224:227]\n"
"ds_read_b128 a[208:211], v6 offset:0\n"
"s_add_u32 m0, s76, 40960\n"
"v_mfma_f32_16x16x32_bf16 v[228:231], a[148:151], a[228:231], v[228:231]\n"
"buffer_load_dwordx4 v2, s[40:43], s66 offen lds\n"
"v_mfma_f32_16x16x32_bf16 v[228:231], a[144:147], a[224:227], v[228:231]\n"
"v_mfma_f32_16x16x32_bf16 v[232:235], a[152:155], a[224:227], v[232:235]\n"
"buffer_load_dword a255, v15, s[60:63], 0 offen\n"
"v_add_u32 v15, 2048, v15\n"
"v_mfma_f32_16x16x32_bf16 v[232:235], a[156:159], a[228:231], v[232:235]\n"
"v_mfma_f32_16x16x32_bf16 v[236:239], a[164:167], a[228:231], v[236:239]\n"
"v_mfma_f32_16x16x32_bf16 v[236:239], a[160:163], a[224:227], v[236:239]\n"
"v_mfma_f32_16x16x32_bf16 v[240:243], a[168:171], a[224:227], v[240:243]\n"
"v_mfma_f32_16x16x32_bf16 v[240:243], a[172:175], a[228:231], v[240:243]\n"
"v_mfma_f32_16x16x32_bf16 v[244:247], a[180:183], a[228:231], v[244:247]\n"
"s_add_u32 m0, s76, 41984\n"
"v_mfma_f32_16x16x32_bf16 v[244:247], a[176:179], a[224:227], v[244:247]\n"
"buffer_store_dword v23, v22, s[56:59], 0 offen sc1\n"
"v_mfma_f32_16x16x32_bf16 v[248:251], a[184:187], a[224:227], v[248:251]\n"
"buffer_load_dwordx4 v3, s[40:43], s66 offen lds\n"
"v_mfma_f32_16x16x32_bf16 v[248:251], a[188:191], a[228:231], v[248:251]\n"
"v_mfma_f32_16x16x32_bf16 v[252:255], a[196:199], a[228:231], v[252:255]\n"
"v_mfma_f32_16x16x32_bf16 v[252:255], a[192:195], a[224:227], v[252:255]\n"
"v_mfma_f32_16x16x32_bf16 a[56:59], a[224:227], a[200:203], a[56:59]\n"
"v_mfma_f32_16x16x32_bf16 a[56:59], a[228:231], a[204:207], a[56:59]\n"
"v_mfma_f32_16x16x32_bf16 a[60:63], a[232:235], a[200:203], a[60:63]\n"
"ds_read_b128 a[220:223], v7 offset:2048\n"
"v_mfma_f32_16x16x32_bf16 a[60:63], a[236:239], a[204:207], a[60:63]\n"
"buffer_load_dword v23, v21, s[56:59], 0 offen sc1\n"
"v_mfma_f32_16x16x32_bf16 a[28:31], a[196:199], a[236:239], a[28:31]\n"
"ds_read_b128 a[216:219], v6 offset:2048\n"
"s_add_u32 m0, s76, 43008\n"
"v_mfma_f32_16x16x32_bf16 a[28:31], a[192:195], a[232:235], a[28:31]\n"
"buffer_load_dwordx4 v2, s[40:43], s67 offen lds\n"
"v_add_u32 v2, s17, v2\n"
"v_mfma_f32_16x16x32_bf16 a[24:27], a[184:187], a[232:235], a[24:27]\n"
"v_mfma_f32_16x16x32_bf16 a[24:27], a[188:191], a[236:239], a[24:27]\n"
"v_mfma_f32_16x16x32_bf16 a[20:23], a[180:183], a[236:239], a[20:23]\n"
"v_mfma_f32_16x16x32_bf16 a[20:23], a[176:179], a[232:235], a[20:23]\n"
"v_mfma_f32_16x16x32_bf16 a[16:19], a[168:171], a[232:235], a[16:19]\n"
"v_mfma_f32_16x16x32_bf16 a[16:19], a[172:175], a[236:239], a[16:19]\n"
"v_mfma_f32_16x16x32_bf16 a[12:15], a[164:167], a[236:239], a[12:15]\n"
"s_add_u32 m0, s76, 44032\n"
"v_mfma_f32_16x16x32_bf16 a[12:15], a[160:163], a[232:235], a[12:15]\n"
"buffer_load_dwordx4 v3, s[40:43], s67 offen lds\n"
"v_mfma_f32_16x16x32_bf16 a[8:11], a[152:155], a[232:235], a[8:11]\n"
"v_add_u32 v3, s17, v3\n"
"s_add_u32 s16, s16, 1\n"
"v_mfma_f32_16x16x32_bf16 a[8:11], a[156:159], a[236:239], a[8:11]\n"
"s_cmp_eq_u32 s16, 15\n"
"s_cselect_b32 s28, -13, 1\n"
"s_lshl_b32 s17, s28, 7\n"
"v_mfma_f32_16x16x32_bf16 a[4:7], a[148:151], a[236:239], a[4:7]\n"
"s_mul_i32 s28, s28, 458752\n"
"v_mfma_f32_16x16x32_bf16 a[4:7], a[144:147], a[232:235], a[4:7]\n"
"v_mfma_f32_16x16x32_bf16 a[0:3], a[136:139], a[232:235], a[0:3]\n"
"v_mfma_f32_16x16x32_bf16 a[0:3], a[140:143], a[236:239], a[0:3]\n"
"T1loop_%=:\n"
"s_waitcnt lgkmcnt(2)\n"
"v_mfma_f32_16x16x32_bf16 v[32:35], a[64:67], a[208:211], v[32:35]\n"
"ds_read_b128 a[228:231], v7 offset:4096\n"
"v_mfma_f32_16x16x32_bf16 v[32:35], a[68:71], a[212:215], v[32:35]\n"
"ds_read_b128 a[224:227], v6 offset:4096\n"
"s_add_u32 m0, s77, 65536\n"
"v_mfma_f32_16x16x32_bf16 v[36:39], a[76:79], a[212:215], v[36:39]\n"
"buffer_load_dwordx4 v5, s[44:47], 0 offen lds\n"
"v_mfma_f32_16x16x32_bf16 v[36:39], a[72:75], a[208:211], v[36:39]\n"
"v_mfma_f32_16x16x32_bf16 v[40:43], a[80:83], a[208:211], v[40:43]\n"
"v_mfma_f32_16x16x32_bf16 v[40:43], a[84:87], a[212:215], v[40:43]\n"
"v_mfma_f32_16x16x32_bf16 v[44:47], a[92:95], a[212:215], v[44:47]\n"
"v_mfma_f32_16x16x32_bf16 v[44:47], a[88:91], a[208:211], v[44:47]\n"
"v_mfma_f32_16x16x32_bf16 v[48:51], a[96:99], a[208:211], v[48:51]\n"
"s_add_u32 m0, s77, 81920\n"
"v_mfma_f32_16x16x32_bf16 v[48:51], a[100:103], a[212:215], v[48:51]\n"
"buffer_load_dwordx4 v5, s[48:51], 0 offen lds\n"
"v_mfma_f32_16x16x32_bf16 v[52:55], a[108:111], a[212:215], v[52:55]\n"
"v_mfma_f32_16x16x32_bf16 v[52:55], a[104:107], a[208:211], v[52:55]\n"
"v_mfma_f32_16x16x32_bf16 v[56:59], a[112:115], a[208:211], v[56:59]\n"
"v_mfma_f32_16x16x32_bf16 v[56:59], a[116:119], a[212:215], v[56:59]\n"
"v_mfma_f32_16x16x32_bf16 v[60:63], a[124:127], a[212:215], v[60:63]\n"
"v_mfma_f32_16x16x32_bf16 v[60:63], a[120:123], a[208:211], v[60:63]\n"
"s_add_u32 m0, s77, 69632\n"
"v_mfma_f32_16x16x32_bf16 a[32:35], a[208:211], a[128:131], a[32:35]\n"
"buffer_load_dwordx4 v5, s[44:47], s68 offen lds\n"
"v_mfma_f32_16x16x32_bf16 a[32:35], a[212:215], a[132:135], a[32:35]\n"
"s_waitcnt lgkmcnt(2)\n"
"v_mfma_f32_16x16x32_bf16 a[36:39], a[216:219], a[128:131], a[36:39]\n"
"ds_read_b128 a[236:239], v7 offset:6144\n"
"v_mfma_f32_16x16x32_bf16 a[36:39], a[220:223], a[132:135], a[36:39]\n"
"ds_read_b128 a[232:235], v6 offset:6144\n"
"v_mfma_f32_16x16x32_bf16 v[92:95], a[124:127], a[220:223], v[92:95]\n"
"v_mfma_f32_16x16x32_bf16 v[92:95], a[120:123], a[216:219], v[92:95]\n"
"v_mfma_f32_16x16x32_bf16 v[88:91], a[112:115], a[216:219], v[88:91]\n"
"s_add_u32 m0, s77, 86016\n"
"v_mfma_f32_16x16x32_bf16 v[88:91], a[116:119], a[220:223], v[88:91]\n"
"buffer_load_dwordx4 v5, s[48:51], s68 offen lds\n"
"v_mfma_f32_16x16x32_bf16 v[84:87], a[108:111], a[220:223], v[84:87]\n"
"v_mfma_f32_16x16x32_bf16 v[84:87], a[104:107], a[216:219], v[84:87]\n"
"v_mfma_f32_16x16x32_bf16 v[80:83], a[96:99], a[216:219], v[80:83]\n"
"v_mfma_f32_16x16x32_bf16 v[80:83], a[100:103], a[220:223], v[80:83]\n"
"v_mfma_f32_16x16x32_bf16 v[76:79], a[92:95], a[220:223], v[76:79]\n"
"v_mfma_f32_16x16x32_bf16 v[76:79], a[88:91], a[216:219], v[76:79]\n"
"s_add_u32 m0, s77, 73728\n"
"v_mfma_f32_16x16x32_bf16 v[72:75], a[80:83], a[216:219], v[72:75]\n"
"buffer_load_dwordx4 v5, s[44:47], s69 offen lds\n"
"v_mfma_f32_16x16x32_bf16 v[72:75], a[84:87], a[220:223], v[72:75]\n"
"v_mfma_f32_16x16x32_bf16 v[68:71], a[76:79], a[220:223], v[68:71]\n"
"v_mfma_f32_16x16x32_bf16 v[68:71], a[72:75], a[216:219], v[68:71]\n"
"v_mfma_f32_16x16x32_bf16 v[64:67], a[64:67], a[216:219], v[64:67]\n"
"v_mfma_f32_16x16x32_bf16 v[64:67], a[68:71], a[220:223], v[64:67]\n"
"s_waitcnt vmcnt(16) lgkmcnt(0)\n"
"s_barrier\n"
"v_mfma_f32_16x16x32_bf16 v[96:99], a[64:67], a[224:227], v[96:99]\n"
"ds_read_b128 a[212:215], v7 offset:8192\n"
"s_add_u32 m0, s77, 90112\n"
"v_mfma_f32_16x16x32_bf16 v[96:99], a[68:71], a[228:231], v[96:99]\n"
"ds_read_b128 a[208:211], v6 offset:8192\n"
"v_mfma_f32_16x16x32_bf16 v[100:103], a[76:79], a[228:231], v[100:103]\n"
"buffer_load_dwordx4 v5, s[48:51], s69 offen lds\n"
"v_mfma_f32_16x16x32_bf16 v[100:103], a[72:75], a[224:227], v[100:103]\n"
"ds_read_b64_tr_b16 a[136:137], v10 offset:32768\n"
"v_mfma_f32_16x16x32_bf16 v[104:107], a[80:83], a[224:227], v[104:107]\n"
"ds_read_b64_tr_b16 a[138:139], v10 offset:33792\n"
"v_mfma_f32_16x16x32_bf16 v[104:107], a[84:87], a[228:231], v[104:107]\n"
"ds_read_b64_tr_b16 a[140:141], v10 offset:40960\n"
"v_mfma_f32_16x16x32_bf16 v[108:111], a[92:95], a[228:231], v[108:111]\n"
"v_mfma_f32_16x16x32_bf16 v[108:111], a[88:91], a[224:227], v[108:111]\n"
"ds_read_b64_tr_b16 a[142:143], v10 offset:41984\n"
"s_add_u32 m0, s77, 77824\n"
"v_mfma_f32_16x16x32_bf16 v[112:115], a[96:99], a[224:227], v[112:115]\n"
"buffer_load_dwordx4 v5, s[44:47], s70 offen lds\n"
"v_mfma_f32_16x16x32_bf16 v[112:115], a[100:103], a[228:231], v[112:115]\n"
"ds_read_b64_tr_b16 a[144:145], v11 offset:32768\n"
"v_mfma_f32_16x16x32_bf16 v[116:119], a[108:111], a[228:231], v[116:119]\n"
"ds_read_b64_tr_b16 a[146:147], v11 offset:33792\n"
"v_mfma_f32_16x16x32_bf16 v[116:119], a[104:107], a[224:227], v[116:119]\n"
"v_mfma_f32_16x16x32_bf16 v[120:123], a[112:115], a[224:227], v[120:123]\n"
"ds_read_b64_tr_b16 a[148:149], v11 offset:40960\n"
"v_mfma_f32_16x16x32_bf16 v[120:123], a[116:119], a[228:231], v[120:123]\n"
"v_mfma_f32_16x16x32_bf16 v[124:127], a[124:127], a[228:231], v[124:127]\n"
"ds_read_b64_tr_b16 a[150:151], v11 offset:41984\n"
"s_add_u32 m0, s77, 94208\n"
"v_mfma_f32_16x16x32_bf16 v[124:127], a[120:123], a[224:227], v[124:127]\n"
"buffer_load_dwordx4 v5, s[48:51], s70 offen lds\n"
"v_mfma_f32_16x16x32_bf16 a[40:43], a[224:227], a[128:131], a[40:43]\n"
"ds_read_b64_tr_b16 a[152:153], v12 offset:32768\n"
"v_add_u32 v5, s28, v5\n"
"v_mfma_f32_16x16x32_bf16 a[40:43], a[228:231], a[132:135], a[40:43]\n"
"ds_read_b64_tr_b16 a[154:155], v12 offset:33792\n"
"v_mfma_f32_16x16x32_bf16 a[44:47], a[232:235], a[128:131], a[44:47]\n"
"ds_read_b128 a[220:223], v7 offset:10240\n"
"v_mfma_f32_16x16x32_bf16 a[44:47], a[236:239], a[132:135], a[44:47]\n"
"ds_read_b128 a[216:219], v6 offset:10240\n"
"v_mfma_f32_16x16x32_bf16 v[156:159], a[124:127], a[236:239], v[156:159]\n"
"ds_read_b64_tr_b16 a[156:157], v12 offset:40960\n"
"v_mfma_f32_16x16x32_bf16 v[156:159], a[120:123], a[232:235], v[156:159]\n"
"ds_read_b64_tr_b16 a[158:159], v12 offset:41984\n"
"s_add_u32 m0, s77, 131072\n"
"v_mfma_f32_16x16x32_bf16 v[152:155], a[112:115], a[232:235], v[152:155]\n"
"buffer_load_dwordx4 v4, s[52:55], 0 offen lds\n"
"v_mfma_f32_16x16x32_bf16 v[152:155], a[116:119], a[236:239], v[152:155]\n"
"ds_read_b64_tr_b16 a[160:161], v13 offset:32768\n"
"v_add_u32 v4, s17, v4\n"
"v_mfma_f32_16x16x32_bf16 v[148:151], a[108:111], a[236:239], v[148:151]\n"
"ds_read_b64_tr_b16 a[162:163], v13 offset:33792\n"
"v_mfma_f32_16x16x32_bf16 v[148:151], a[104:107], a[232:235], v[148:151]\n"
"v_mfma_f32_16x16x32_bf16 v[144:147], a[96:99], a[232:235], v[144:147]\n"
"ds_read_b64_tr_b16 a[164:165], v13 offset:40960\n"
"v_mfma_f32_16x16x32_bf16 v[144:147], a[100:103], a[236:239], v[144:147]\n"
"v_mfma_f32_16x16x32_bf16 v[140:143], a[92:95], a[236:239], v[140:143]\n"
"ds_read_b64_tr_b16 a[166:167], v13 offset:41984\n"
"v_mfma_f32_16x16x32_bf16 v[140:143], a[88:91], a[232:235], v[140:143]\n"
"v_mfma_f32_16x16x32_bf16 v[136:139], a[80:83], a[232:235], v[136:139]\n"
"ds_read_b64_tr_b16 a[168:169], v10 offset:49152\n"
"v_mfma_f32_16x16x32_bf16 v[136:139], a[84:87], a[236:239], v[136:139]\n"
"v_mfma_f32_16x16x32_bf16 v[132:135], a[76:79], a[236:239], v[132:135]\n"
"ds_read_b64_tr_b16 a[170:171], v10 offset:50176\n"
"v_mfma_f32_16x16x32_bf16 v[132:135], a[72:75], a[232:235], v[132:135]\n"
"ds_read_b64_tr_b16 a[172:173], v10 offset:57344\n"
"v_mfma_f32_16x16x32_bf16 v[128:131], a[64:67], a[232:235], v[128:131]\n"
"v_mfma_f32_16x16x32_bf16 v[128:131], a[68:71], a[236:239], v[128:131]\n"
"ds_read_b64_tr_b16 a[174:175], v10 offset:58368\n"
"s_waitcnt lgkmcnt(14)\n"
"v_mfma_f32_16x16x32_bf16 v[160:163], a[64:67], a[208:211], v[160:163]\n"
"ds_read_b128 a[228:231], v7 offset:12288\n"
"v_mfma_f32_16x16x32_bf16 v[160:163], a[68:71], a[212:215], v[160:163]\n"
"ds_read_b128 a[224:227], v6 offset:12288\n"
"v_mfma_f32_16x16x32_bf16 v[164:167], a[76:79], a[212:215], v[164:167]\n"
"ds_read_b64_tr_b16 a[176:177], v11 offset:49152\n"
"s_add_u32 m0, s76, 0\n"
"v_mfma_f32_16x16x32_bf16 v[164:167], a[72:75], a[208:211], v[164:167]\n"
"buffer_load_dwordx4 v2, s[40:43], s64 offen lds\n"
"v_mfma_f32_16x16x32_bf16 v[168:171], a[80:83], a[208:211], v[168:171]\n"
"ds_read_b64_tr_b16 a[178:179], v11 offset:50176\n"
"v_mfma_f32_16x16x32_bf16 v[168:171], a[84:87], a[212:215], v[168:171]\n"
"ds_read_b64_tr_b16 a[180:181], v11 offset:57344\n"
"v_mfma_f32_16x16x32_bf16 v[172:175], a[92:95], a[212:215], v[172:175]\n"
"ds_read_b64_tr_b16 a[182:183], v11 offset:58368\n"
"v_mfma_f32_16x16x32_bf16 v[172:175], a[88:91], a[208:211], v[172:175]\n"
"v_mfma_f32_16x16x32_bf16 v[176:179], a[96:99], a[208:211], v[176:179]\n"
"ds_read_b64_tr_b16 a[184:185], v12 offset:49152\n"
"v_mfma_f32_16x16x32_bf16 v[176:179], a[100:103], a[212:215], v[176:179]\n"
"v_mfma_f32_16x16x32_bf16 v[180:183], a[108:111], a[212:215], v[180:183]\n"
"ds_read_b64_tr_b16 a[186:187], v12 offset:50176\n"
"s_add_u32 m0, s76, 1024\n"
"v_mfma_f32_16x16x32_bf16 v[180:183], a[104:107], a[208:211], v[180:183]\n"
"ds_read_b64_tr_b16 a[188:189], v12 offset:57344\n"
"v_mfma_f32_16x16x32_bf16 v[184:187], a[112:115], a[208:211], v[184:187]\n"
"buffer_load_dwordx4 v3, s[40:43], s64 offen lds\n"
"v_mfma_f32_16x16x32_bf16 v[184:187], a[116:119], a[212:215], v[184:187]\n"
"ds_read_b64_tr_b16 a[190:191], v12 offset:58368\n"
"v_mfma_f32_16x16x32_bf16 v[188:191], a[124:127], a[212:215], v[188:191]\n"
"v_mfma_f32_16x16x32_bf16 v[188:191], a[120:123], a[208:211], v[188:191]\n"
"ds_read_b64_tr_b16 a[192:193], v13 offset:49152\n"
"v_mfma_f32_16x16x32_bf16 a[48:51], a[208:211], a[128:131], a[48:51]\n"
"v_mfma_f32_16x16x32_bf16 a[48:51], a[212:215], a[132:135], a[48:51]\n"
"ds_read_b64_tr_b16 a[194:195], v13 offset:50176\n"
"s_waitcnt lgkmcnt(14)\n"
"v_mfma_f32_16x16x32_bf16 a[52:55], a[216:219], a[128:131], a[52:55]\n"
"ds_read_b128 a[236:239], v7 offset:14336\n"
"v_mfma_f32_16x16x32_bf16 a[52:55], a[220:223], a[132:135], a[52:55]\n"
"ds_read_b64_tr_b16 a[196:197], v13 offset:57344\n"
"v_mfma_f32_16x16x32_bf16 v[220:223], a[124:127], a[220:223], v[220:223]\n"
"ds_read_b128 a[232:235], v6 offset:14336\n"
"s_add_u32 m0, s76, 2048\n"
"v_mfma_f32_16x16x32_bf16 v[220:223], a[120:123], a[216:219], v[220:223]\n"
"ds_read_b64_tr_b16 a[198:199], v13 offset:58368\n"
"v_mfma_f32_16x16x32_bf16 v[216:219], a[112:115], a[216:219], v[216:219]\n"
"buffer_load_dwordx4 v2, s[40:43], s65 offen lds\n"
"v_mfma_f32_16x16x32_bf16 v[216:219], a[116:119], a[220:223], v[216:219]\n"
"ds_read_b128 a[200:203], v8 offset:4096\n"
"v_mfma_f32_16x16x32_bf16 v[212:215], a[108:111], a[220:223], v[212:215]\n"
"ds_read_b128 a[204:207], v9 offset:4096\n"
"v_mfma_f32_16x16x32_bf16 v[212:215], a[104:107], a[216:219], v[212:215]\n"
"v_mfma_f32_16x16x32_bf16 v[208:211], a[96:99], a[216:219], v[208:211]\n"
"v_mfma_f32_16x16x32_bf16 v[208:211], a[100:103], a[220:223], v[208:211]\n"
"v_mfma_f32_16x16x32_bf16 v[204:207], a[92:95], a[220:223], v[204:207]\n"
"s_add_u32 m0, s76, 3072\n"
"v_mfma_f32_16x16x32_bf16 v[204:207], a[88:91], a[216:219], v[204:207]\n"
"buffer_load_dwordx4 v3, s[40:43], s65 offen lds\n"
"v_mfma_f32_16x16x32_bf16 v[200:203], a[80:83], a[216:219], v[200:203]\n"
"v_mfma_f32_16x16x32_bf16 v[200:203], a[84:87], a[220:223], v[200:203]\n"
"v_mfma_f32_16x16x32_bf16 v[196:199], a[76:79], a[220:223], v[196:199]\n"
"v_mfma_f32_16x16x32_bf16 v[196:199], a[72:75], a[216:219], v[196:199]\n"
"v_mfma_f32_16x16x32_bf16 v[192:195], a[64:67], a[216:219], v[192:195]\n"
"v_mfma_f32_16x16x32_bf16 v[192:195], a[68:71], a[220:223], v[192:195]\n"
"s_waitcnt vmcnt(20) lgkmcnt(0)\n"
"s_barrier\n"
"v_mfma_f32_16x16x32_bf16 v[224:227], a[64:67], a[224:227], v[224:227]\n"
"ds_read_b128 a[212:215], v7 offset:32768\n"
"v_mfma_f32_16x16x32_bf16 v[224:227], a[68:71], a[228:231], v[224:227]\n"
"ds_read_b128 a[208:211], v6 offset:32768\n"
"s_add_u32 m0, s76, 8192\n"
"v_mfma_f32_16x16x32_bf16 v[228:231], a[76:79], a[228:231], v[228:231]\n"
"buffer_load_dwordx4 v2, s[40:43], s66 offen lds\n"
"v_mfma_f32_16x16x32_bf16 v[228:231], a[72:75], a[224:227], v[228:231]\n"
"v_mfma_f32_16x16x32_bf16 v[232:235], a[80:83], a[224:227], v[232:235]\n"
"buffer_load_dword a255, v14, s[24:27], 0 offen\n"
"v_add_u32 v14, 4096, v14\n"
"v_mfma_f32_16x16x32_bf16 v[232:235], a[84:87], a[228:231], v[232:235]\n"
"v_mfma_f32_16x16x32_bf16 v[236:239], a[92:95], a[228:231], v[236:239]\n"
"v_xor_b32 v24, v24, v16\n"
"v_mfma_f32_16x16x32_bf16 v[236:239], a[88:91], a[224:227], v[236:239]\n"
"v_mfma_f32_16x16x32_bf16 v[240:243], a[96:99], a[224:227], v[240:243]\n"
"v_xor_b32 v25, v25, v17\n"
"v_mfma_f32_16x16x32_bf16 v[240:243], a[100:103], a[228:231], v[240:243]\n"
"v_mfma_f32_16x16x32_bf16 v[244:247], a[108:111], a[228:231], v[244:247]\n"
"v_xor_b32 v26, v26, v18\n"
"s_add_u32 m0, s76, 9216\n"
"v_mfma_f32_16x16x32_bf16 v[244:247], a[104:107], a[224:227], v[244:247]\n"
"v_xor_b32 v27, v27, v19\n"
"v_mfma_f32_16x16x32_bf16 v[248:251], a[112:115], a[224:227], v[248:251]\n"
"buffer_load_dwordx4 v3, s[40:43], s66 offen lds\n"
"v_mfma_f32_16x16x32_bf16 v[248:251], a[116:119], a[228:231], v[248:251]\n"
"v_or3_b32 v24, v24, v25, v26\n"
"v_mfma_f32_16x16x32_bf16 v[252:255], a[124:127], a[228:231], v[252:255]\n"
"v_mfma_f32_16x16x32_bf16 v[252:255], a[120:123], a[224:227], v[252:255]\n"
"v_or_b32 v24, v24, v27\n"
"v_mfma_f32_16x16x32_bf16 a[56:59], a[224:227], a[128:131], a[56:59]\n"
"v_cmp_ne_u32 vcc, 0, v24\n"
"v_mfma_f32_16x16x32_bf16 a[56:59], a[228:231], a[132:135], a[56:59]\n"
"v_mfma_f32_16x16x32_bf16 a[60:63], a[232:235], a[128:131], a[60:63]\n"
"ds_read_b128 a[220:223], v7 offset:34816\n"
"s_nop 3\n"
"v_mfma_f32_16x16x32_bf16 a[60:63], a[236:239], a[132:135], a[60:63]\n"
"ds_read_b128 a[216:219], v6 offset:34816\n"
"s_add_u32 m0, s76, 10240\n"
"v_mfma_f32_16x16x32_bf16 a[28:31], a[124:127], a[236:239], a[28:31]\n"
"s_cmp_eq_u64 vcc, 0\n"
"s_cselect_b32 s14, 0, 1073741824\n"
"buffer_load_dwordx4 v2, s[40:43], s67 offen lds\n"
"v_mfma_f32_16x16x32_bf16 a[28:31], a[120:123], a[232:235], a[28:31]\n"
"buffer_store_dword v28, v30, s[56:59], s14 offen sc1\n"
"v_add_u32 v2, s17, v2\n"
"v_mfma_f32_16x16x32_bf16 a[24:27], a[112:115], a[232:235], a[24:27]\n"
"v_mfma_f32_16x16x32_bf16 a[24:27], a[116:119], a[236:239], a[24:27]\n"
"buffer_load_dwordx4 v[24:27], v29, s[56:59], 0 offen sc1\n"
"v_mfma_f32_16x16x32_bf16 a[20:23], a[108:111], a[236:239], a[20:23]\n"
"v_mfma_f32_16x16x32_bf16 a[20:23], a[104:107], a[232:235], a[20:23]\n"
"v_mfma_f32_16x16x32_bf16 a[16:19], a[96:99], a[232:235], a[16:19]\n"
"v_mfma_f32_16x16x32_bf16 a[16:19], a[100:103], a[236:239], a[16:19]\n"
"v_mfma_f32_16x16x32_bf16 a[12:15], a[92:95], a[236:239], a[12:15]\n"
"s_add_u32 m0, s76, 11264\n"
"v_mfma_f32_16x16x32_bf16 a[12:15], a[88:91], a[232:235], a[12:15]\n"
"buffer_load_dwordx4 v3, s[40:43], s67 offen lds\n"
"v_mfma_f32_16x16x32_bf16 a[8:11], a[80:83], a[232:235], a[8:11]\n"
"v_add_u32 v3, s17, v3\n"
"s_add_u32 s16, s16, 1\n"
"v_mfma_f32_16x16x32_bf16 a[8:11], a[84:87], a[236:239], a[8:11]\n"
"s_cmp_eq_u32 s16, 15\n"
"s_cselect_b32 s28, -13, 1\n"
"s_lshl_b32 s17, s28, 7\n"
"v_mfma_f32_16x16x32_bf16 a[4:7], a[76:79], a[236:239], a[4:7]\n"
"s_mul_i32 s28, s28, 458752\n"
"v_mfma_f32_16x16x32_bf16 a[4:7], a[72:75], a[232:235], a[4:7]\n"
"v_mfma_f32_16x16x32_bf16 a[0:3], a[64:67], a[232:235], a[0:3]\n"
"v_mfma_f32_16x16x32_bf16 a[0:3], a[68:71], a[236:239], a[0:3]\n"
"s_waitcnt lgkmcnt(2)\n"
"v_mfma_f32_16x16x32_bf16 v[32:35], a[136:139], a[208:211], v[32:35]\n"
"ds_read_b128 a[228:231], v7 offset:36864\n"
"v_mfma_f32_16x16x32_bf16 v[32:35], a[140:143], a[212:215], v[32:35]\n"
"ds_read_b128 a[224:227], v6 offset:36864\n"
"s_add_u32 m0, s77, 98304\n"
"v_mfma_f32_16x16x32_bf16 v[36:39], a[148:151], a[212:215], v[36:39]\n"
"buffer_load_dwordx4 v5, s[44:47], 0 offen lds\n"
"v_mfma_f32_16x16x32_bf16 v[36:39], a[144:147], a[208:211], v[36:39]\n"
"v_mfma_f32_16x16x32_bf16 v[40:43], a[152:155], a[208:211], v[40:43]\n"
"v_mfma_f32_16x16x32_bf16 v[40:43], a[156:159], a[212:215], v[40:43]\n"
"v_mfma_f32_16x16x32_bf16 v[44:47], a[164:167], a[212:215], v[44:47]\n"
"v_mfma_f32_16x16x32_bf16 v[44:47], a[160:163], a[208:211], v[44:47]\n"
"v_mfma_f32_16x16x32_bf16 v[48:51], a[168:171], a[208:211], v[48:51]\n"
"s_add_u32 m0, s77, 114688\n"
"v_mfma_f32_16x16x32_bf16 v[48:51], a[172:175], a[212:215], v[48:51]\n"
"buffer_load_dwordx4 v5, s[48:51], 0 offen lds\n"
"v_mfma_f32_16x16x32_bf16 v[52:55], a[180:183], a[212:215], v[52:55]\n"
"v_mfma_f32_16x16x32_bf16 v[52:55], a[176:179], a[208:211], v[52:55]\n"
"v_mfma_f32_16x16x32_bf16 v[56:59], a[184:187], a[208:211], v[56:59]\n"
"v_mfma_f32_16x16x32_bf16 v[56:59], a[188:191], a[212:215], v[56:59]\n"
"v_mfma_f32_16x16x32_bf16 v[60:63], a[196:199], a[212:215], v[60:63]\n"
"v_mfma_f32_16x16x32_bf16 v[60:63], a[192:195], a[208:211], v[60:63]\n"
"s_add_u32 m0, s77, 102400\n"
"v_mfma_f32_16x16x32_bf16 a[32:35], a[208:211], a[200:203], a[32:35]\n"
"buffer_load_dwordx4 v5, s[44:47], s68 offen lds\n"
"v_mfma_f32_16x16x32_bf16 a[32:35], a[212:215], a[204:207], a[32:35]\n"
"s_waitcnt lgkmcnt(2)\n"
"v_mfma_f32_16x16x32_bf16 a[36:39], a[216:219], a[200:203], a[36:39]\n"
"ds_read_b128 a[236:239], v7 offset:38912\n"
"v_mfma_f32_16x16x32_bf16 a[36:39], a[220:223], a[204:207], a[36:39]\n"
"ds_read_b128 a[232:235], v6 offset:38912\n"
"v_mfma_f32_16x16x32_bf16 v[92:95], a[196:199], a[220:223], v[92:95]\n"
"v_mfma_f32_16x16x32_bf16 v[92:95], a[192:195], a[216:219], v[92:95]\n"
"v_mfma_f32_16x16x32_bf16 v[88:91], a[184:187], a[216:219], v[88:91]\n"
"s_add_u32 m0, s77, 118784\n"
"v_mfma_f32_16x16x32_bf16 v[88:91], a[188:191], a[220:223], v[88:91]\n"
"buffer_load_dwordx4 v5, s[48:51], s68 offen lds\n"
"v_mfma_f32_16x16x32_bf16 v[84:87], a[180:183], a[220:223], v[84:87]\n"
"v_mfma_f32_16x16x32_bf16 v[84:87], a[176:179], a[216:219], v[84:87]\n"
"v_mfma_f32_16x16x32_bf16 v[80:83], a[168:171], a[216:219], v[80:83]\n"
"v_mfma_f32_16x16x32_bf16 v[80:83], a[172:175], a[220:223], v[80:83]\n"
"v_mfma_f32_16x16x32_bf16 v[76:79], a[164:167], a[220:223], v[76:79]\n"
"v_mfma_f32_16x16x32_bf16 v[76:79], a[160:163], a[216:219], v[76:79]\n"
"s_add_u32 m0, s77, 106496\n"
"v_mfma_f32_16x16x32_bf16 v[72:75], a[152:155], a[216:219], v[72:75]\n"
"buffer_load_dwordx4 v5, s[44:47], s69 offen lds\n"
"v_mfma_f32_16x16x32_bf16 v[72:75], a[156:159], a[220:223], v[72:75]\n"
"v_mfma_f32_16x16x32_bf16 v[68:71], a[148:151], a[220:223], v[68:71]\n"
"v_mfma_f32_16x16x32_bf16 v[68:71], a[144:147], a[216:219], v[68:71]\n"
"v_mfma_f32_16x16x32_bf16 v[64:67], a[136:139], a[216:219], v[64:67]\n"
"v_mfma_f32_16x16x32_bf16 v[64:67], a[140:143], a[220:223], v[64:67]\n"
"s_waitcnt vmcnt(16) lgkmcnt(0)\n"
"s_barrier\n"
"v_mfma_f32_16x16x32_bf16 v[96:99], a[136:139], a[224:227], v[96:99]\n"
"ds_read_b128 a[212:215], v7 offset:40960\n"
"s_add_u32 m0, s77, 122880\n"
"v_mfma_f32_16x16x32_bf16 v[96:99], a[140:143], a[228:231], v[96:99]\n"
"ds_read_b128 a[208:211], v6 offset:40960\n"
"v_mfma_f32_16x16x32_bf16 v[100:103], a[148:151], a[228:231], v[100:103]\n"
"buffer_load_dwordx4 v5, s[48:51], s69 offen lds\n"
"v_mfma_f32_16x16x32_bf16 v[100:103], a[144:147], a[224:227], v[100:103]\n"
"ds_read_b64_tr_b16 a[64:65], v10 offset:0\n"
"v_mfma_f32_16x16x32_bf16 v[104:107], a[152:155], a[224:227], v[104:107]\n"
"ds_read_b64_tr_b16 a[66:67], v10 offset:1024\n"
"v_mfma_f32_16x16x32_bf16 v[104:107], a[156:159], a[228:231], v[104:107]\n"
"ds_read_b64_tr_b16 a[68:69], v10 offset:8192\n"
"v_mfma_f32_16x16x32_bf16 v[108:111], a[164:167], a[228:231], v[108:111]\n"
"v_mfma_f32_16x16x32_bf16 v[108:111], a[160:163], a[224:227], v[108:111]\n"
"ds_read_b64_tr_b16 a[70:71], v10 offset:9216\n"
"s_add_u32 m0, s77, 110592\n"
"v_mfma_f32_16x16x32_bf16 v[112:115], a[168:171], a[224:227], v[112:115]\n"
"buffer_load_dwordx4 v5, s[44:47], s70 offen lds\n"
"v_mfma_f32_16x16x32_bf16 v[112:115], a[172:175], a[228:231], v[112:115]\n"
"ds_read_b64_tr_b16 a[72:73], v11 offset:0\n"
"v_mfma_f32_16x16x32_bf16 v[116:119], a[180:183], a[228:231], v[116:119]\n"
"ds_read_b64_tr_b16 a[74:75], v11 offset:1024\n"
"v_mfma_f32_16x16x32_bf16 v[116:119], a[176:179], a[224:227], v[116:119]\n"
"v_mfma_f32_16x16x32_bf16 v[120:123], a[184:187], a[224:227], v[120:123]\n"
"ds_read_b64_tr_b16 a[76:77], v11 offset:8192\n"
"v_mfma_f32_16x16x32_bf16 v[120:123], a[188:191], a[228:231], v[120:123]\n"
"v_mfma_f32_16x16x32_bf16 v[124:127], a[196:199], a[228:231], v[124:127]\n"
"ds_read_b64_tr_b16 a[78:79], v11 offset:9216\n"
"s_add_u32 m0, s77, 126976\n"
"v_mfma_f32_16x16x32_bf16 v[124:127], a[192:195], a[224:227], v[124:127]\n"
"buffer_load_dwordx4 v5, s[48:51], s70 offen lds\n"
"v_mfma_f32_16x16x32_bf16 a[40:43], a[224:227], a[200:203], a[40:43]\n"
"ds_read_b64_tr_b16 a[80:81], v12 offset:0\n"
"v_add_u32 v5, s28, v5\n"
"v_mfma_f32_16x16x32_bf16 a[40:43], a[228:231], a[204:207], a[40:43]\n"
"ds_read_b64_tr_b16 a[82:83], v12 offset:1024\n"
"v_mfma_f32_16x16x32_bf16 a[44:47], a[232:235], a[200:203], a[44:47]\n"
"ds_read_b128 a[220:223], v7 offset:43008\n"
"v_mfma_f32_16x16x32_bf16 a[44:47], a[236:239], a[204:207], a[44:47]\n"
"ds_read_b128 a[216:219], v6 offset:43008\n"
"v_mfma_f32_16x16x32_bf16 v[156:159], a[196:199], a[236:239], v[156:159]\n"
"ds_read_b64_tr_b16 a[84:85], v12 offset:8192\n"
"v_mfma_f32_16x16x32_bf16 v[156:159], a[192:195], a[232:235], v[156:159]\n"
"ds_read_b64_tr_b16 a[86:87], v12 offset:9216\n"
"s_add_u32 m0, s77, 135168\n"
"v_mfma_f32_16x16x32_bf16 v[152:155], a[184:187], a[232:235], v[152:155]\n"
"buffer_load_dwordx4 v4, s[52:55], 0 offen lds\n"
"v_mfma_f32_16x16x32_bf16 v[152:155], a[188:191], a[236:239], v[152:155]\n"
"ds_read_b64_tr_b16 a[88:89], v13 offset:0\n"
"v_add_u32 v4, s17, v4\n"
"v_mfma_f32_16x16x32_bf16 v[148:151], a[180:183], a[236:239], v[148:151]\n"
"ds_read_b64_tr_b16 a[90:91], v13 offset:1024\n"
"v_mfma_f32_16x16x32_bf16 v[148:151], a[176:179], a[232:235], v[148:151]\n"
"v_mfma_f32_16x16x32_bf16 v[144:147], a[168:171], a[232:235], v[144:147]\n"
"ds_read_b64_tr_b16 a[92:93], v13 offset:8192\n"
"v_mfma_f32_16x16x32_bf16 v[144:147], a[172:175], a[236:239], v[144:147]\n"
"v_mfma_f32_16x16x32_bf16 v[140:143], a[164:167], a[236:239], v[140:143]\n"
"ds_read_b64_tr_b16 a[94:95], v13 offset:9216\n"
"v_mfma_f32_16x16x32_bf16 v[140:143], a[160:163], a[232:235], v[140:143]\n"
"v_mfma_f32_16x16x32_bf16 v[136:139], a[152:155], a[232:235], v[136:139]\n"
"ds_read_b64_tr_b16 a[96:97], v10 offset:16384\n"
"v_mfma_f32_16x16x32_bf16 v[136:139], a[156:159], a[236:239], v[136:139]\n"
"v_mfma_f32_16x16x32_bf16 v[132:135], a[148:151], a[236:239], v[132:135]\n"
"ds_read_b64_tr_b16 a[98:99], v10 offset:17408\n"
"v_mfma_f32_16x16x32_bf16 v[132:135], a[144:147], a[232:235], v[132:135]\n"
"ds_read_b64_tr_b16 a[100:101], v10 offset:24576\n"
"v_mfma_f32_16x16x32_bf16 v[128:131], a[136:139], a[232:235], v[128:131]\n"
"v_mfma_f32_16x16x32_bf16 v[128:131], a[140:143], a[236:239], v[128:131]\n"
"ds_read_b64_tr_b16 a[102:103], v10 offset:25600\n"
"s_waitcnt lgkmcnt(14)\n"
"v_mfma_f32_16x16x32_bf16 v[160:163], a[136:139], a[208:211], v[160:163]\n"
"ds_read_b128 a[228:231], v7 offset:45056\n"
"v_mfma_f32_16x16x32_bf16 v[160:163], a[140:143], a[212:215], v[160:163]\n"
"ds_read_b128 a[224:227], v6 offset:45056\n"
"v_mfma_f32_16x16x32_bf16 v[164:167], a[148:151], a[212:215], v[164:167]\n"
"ds_read_b64_tr_b16 a[104:105], v11 offset:16384\n"
"s_add_u32 m0, s76, 32768\n"
"v_mfma_f32_16x16x32_bf16 v[164:167], a[144:147], a[208:211], v[164:167]\n"
"buffer_load_dwordx4 v2, s[40:43], s64 offen lds\n"
"v_mfma_f32_16x16x32_bf16 v[168:171], a[152:155], a[208:211], v[168:171]\n"
"ds_read_b64_tr_b16 a[106:107], v11 offset:17408\n"
"v_mfma_f32_16x16x32_bf16 v[168:171], a[156:159], a[212:215], v[168:171]\n"
"ds_read_b64_tr_b16 a[108:109], v11 offset:24576\n"
"v_mfma_f32_16x16x32_bf16 v[172:175], a[164:167], a[212:215], v[172:175]\n"
"ds_read_b64_tr_b16 a[110:111], v11 offset:25600\n"
"v_mfma_f32_16x16x32_bf16 v[172:175], a[160:163], a[208:211], v[172:175]\n"
"v_mfma_f32_16x16x32_bf16 v[176:179], a[168:171], a[208:211], v[176:179]\n"
"ds_read_b64_tr_b16 a[112:113], v12 offset:16384\n"
"v_mfma_f32_16x16x32_bf16 v[176:179], a[172:175], a[212:215], v[176:179]\n"
"v_mfma_f32_16x16x32_bf16 v[180:183], a[180:183], a[212:215], v[180:183]\n"
"ds_read_b64_tr_b16 a[114:115], v12 offset:17408\n"
"s_add_u32 m0, s76, 33792\n"
"v_mfma_f32_16x16x32_bf16 v[180:183], a[176:179], a[208:211], v[180:183]\n"
"ds_read_b64_tr_b16 a[116:117], v12 offset:24576\n"
"v_mfma_f32_16x16x32_bf16 v[184:187], a[184:187], a[208:211], v[184:187]\n"
"buffer_load_dwordx4 v3, s[40:43], s64 offen lds\n"
"v_mfma_f32_16x16x32_bf16 v[184:187], a[188:191], a[212:215], v[184:187]\n"
"ds_read_b64_tr_b16 a[118:119], v12 offset:25600\n"
"v_mfma_f32_16x16x32_bf16 v[188:191], a[196:199], a[212:215], v[188:191]\n"
"v_mfma_f32_16x16x32_bf16 v[188:191], a[192:195], a[208:211], v[188:191]\n"
"ds_read_b64_tr_b16 a[120:121], v13 offset:16384\n"
"v_mfma_f32_16x16x32_bf16 a[48:51], a[208:211], a[200:203], a[48:51]\n"
"v_mfma_f32_16x16x32_bf16 a[48:51], a[212:215], a[204:207], a[48:51]\n"
"ds_read_b64_tr_b16 a[122:123], v13 offset:17408\n"
"s_waitcnt lgkmcnt(14)\n"
"v_mfma_f32_16x16x32_bf16 a[52:55], a[216:219], a[200:203], a[52:55]\n"
"ds_read_b128 a[236:239], v7 offset:47104\n"
"v_mfma_f32_16x16x32_bf16 a[52:55], a[220:223], a[204:207], a[52:55]\n"
"ds_read_b64_tr_b16 a[124:125], v13 offset:24576\n"
"v_mfma_f32_16x16x32_bf16 v[220:223], a[196:199], a[220:223], v[220:223]\n"
"ds_read_b128 a[232:235], v6 offset:47104\n"
"s_add_u32 m0, s76, 34816\n"
"v_mfma_f32_16x16x32_bf16 v[220:223], a[192:195], a[216:219], v[220:223]\n"
"ds_read_b64_tr_b16 a[126:127], v13 offset:25600\n"
"v_mfma_f32_16x16x32_bf16 v[216:219], a[184:187], a[216:219], v[216:219]\n"
"buffer_load_dwordx4 v2, s[40:43], s65 offen lds\n"
"v_mfma_f32_16x16x32_bf16 v[216:219], a[188:191], a[220:223], v[216:219]\n"
"ds_read_b128 a[128:131], v8 offset:0\n"
"v_mfma_f32_16x16x32_bf16 v[212:215], a[180:183], a[220:223], v[212:215]\n"
"ds_read_b128 a[132:135], v9 offset:0\n"
"v_mfma_f32_16x16x32_bf16 v[212:215], a[176:179], a[216:219], v[212:215]\n"
"v_mfma_f32_16x16x32_bf16 v[208:211], a[168:171], a[216:219], v[208:211]\n"
"v_mfma_f32_16x16x32_bf16 v[208:211], a[172:175], a[220:223], v[208:211]\n"
"v_mfma_f32_16x16x32_bf16 v[204:207], a[164:167], a[220:223], v[204:207]\n"
"s_add_u32 m0, s76, 35840\n"
"v_mfma_f32_16x16x32_bf16 v[204:207], a[160:163], a[216:219], v[204:207]\n"
"buffer_load_dwordx4 v3, s[40:43], s65 offen lds\n"
"v_mfma_f32_16x16x32_bf16 v[200:203], a[152:155], a[216:219], v[200:203]\n"
"v_mfma_f32_16x16x32_bf16 v[200:203], a[156:159], a[220:223], v[200:203]\n"
"v_mfma_f32_16x16x32_bf16 v[196:199], a[148:151], a[220:223], v[196:199]\n"
"v_mfma_f32_16x16x32_bf16 v[196:199], a[144:147], a[216:219], v[196:199]\n"
"v_mfma_f32_16x16x32_bf16 v[192:195], a[136:139], a[216:219], v[192:195]\n"
"v_mfma_f32_16x16x32_bf16 v[192:195], a[140:143], a[220:223], v[192:195]\n"
"s_waitcnt vmcnt(20) lgkmcnt(0)\n"
"s_barrier\n"
"v_mfma_f32_16x16x32_bf16 v[224:227], a[136:139], a[224:227], v[224:227]\n"
"ds_read_b128 a[212:215], v7 offset:0\n"
"v_mfma_f32_16x16x32_bf16 v[224:227], a[140:143], a[228:231], v[224:227]\n"
"ds_read_b128 a[208:211], v6 offset:0\n"
"s_add_u32 m0, s76, 40960\n"
"v_mfma_f32_16x16x32_bf16 v[228:231], a[148:151], a[228:231], v[228:231]\n"
"buffer_load_dwordx4 v2, s[40:43], s66 offen lds\n"
"v_mfma_f32_16x16x32_bf16 v[228:231], a[144:147], a[224:227], v[228:231]\n"
"v_mfma_f32_16x16x32_bf16 v[232:235], a[152:155], a[224:227], v[232:235]\n"
"buffer_load_dword a255, v15, s[60:63], 0 offen\n"
"v_add_u32 v15, 2048, v15\n"
"v_mfma_f32_16x16x32_bf16 v[232:235], a[156:159], a[228:231], v[232:235]\n"
"v_mfma_f32_16x16x32_bf16 v[236:239], a[164:167], a[228:231], v[236:239]\n"
"v_mfma_f32_16x16x32_bf16 v[236:239], a[160:163], a[224:227], v[236:239]\n"
"v_mfma_f32_16x16x32_bf16 v[240:243], a[168:171], a[224:227], v[240:243]\n"
"v_mfma_f32_16x16x32_bf16 v[240:243], a[172:175], a[228:231], v[240:243]\n"
"v_mfma_f32_16x16x32_bf16 v[244:247], a[180:183], a[228:231], v[244:247]\n"
"s_add_u32 m0, s76, 41984\n"
"v_mfma_f32_16x16x32_bf16 v[244:247], a[176:179], a[224:227], v[244:247]\n"
"buffer_store_dword v23, v22, s[56:59], 0 offen sc1\n"
"v_mfma_f32_16x16x32_bf16 v[248:251], a[184:187], a[224:227], v[248:251]\n"
"buffer_load_dwordx4 v3, s[40:43], s66 offen lds\n"
"v_mfma_f32_16x16x32_bf16 v[248:251], a[188:191], a[228:231], v[248:251]\n"
"v_mfma_f32_16x16x32_bf16 v[252:255], a[196:199], a[228:231], v[252:255]\n"
"v_mfma_f32_16x16x32_bf16 v[252:255], a[192:195], a[224:227], v[252:255]\n"
"v_mfma_f32_16x16x32_bf16 a[56:59], a[224:227], a[200:203], a[56:59]\n"
"v_mfma_f32_16x16x32_bf16 a[56:59], a[228:231], a[204:207], a[56:59]\n"
"v_mfma_f32_16x16x32_bf16 a[60:63], a[232:235], a[200:203], a[60:63]\n"
"ds_read_b128 a[220:223], v7 offset:2048\n"
"v_mfma_f32_16x16x32_bf16 a[60:63], a[236:239], a[204:207], a[60:63]\n"
"buffer_load_dword v23, v21, s[56:59], 0 offen sc1\n"
"v_mfma_f32_16x16x32_bf16 a[28:31], a[196:199], a[236:239], a[28:31]\n"
"ds_read_b128 a[216:219], v6 offset:2048\n"
"s_add_u32 m0, s76, 43008\n"
"v_mfma_f32_16x16x32_bf16 a[28:31], a[192:195], a[232:235], a[28:31]\n"
"buffer_load_dwordx4 v2, s[40:43], s67 offen lds\n"
"v_add_u32 v2, s17, v2\n"
"v_mfma_f32_16x16x32_bf16 a[24:27], a[184:187], a[232:235], a[24:27]\n"
"v_mfma_f32_16x16x32_bf16 a[24:27], a[188:191], a[236:239], a[24:27]\n"
"v_mfma_f32_16x16x32_bf16 a[20:23], a[180:183], a[236:239], a[20:23]\n"
"v_mfma_f32_16x16x32_bf16 a[20:23], a[176:179], a[232:235], a[20:23]\n"
"v_mfma_f32_16x16x32_bf16 a[16:19], a[168:171], a[232:235], a[16:19]\n"
"v_mfma_f32_16x16x32_bf16 a[16:19], a[172:175], a[236:239], a[16:19]\n"
"v_mfma_f32_16x16x32_bf16 a[12:15], a[164:167], a[236:239], a[12:15]\n"
"s_add_u32 m0, s76, 44032\n"
"v_mfma_f32_16x16x32_bf16 a[12:15], a[160:163], a[232:235], a[12:15]\n"
"buffer_load_dwordx4 v3, s[40:43], s67 offen lds\n"
"v_mfma_f32_16x16x32_bf16 a[8:11], a[152:155], a[232:235], a[8:11]\n"
"v_add_u32 v3, s17, v3\n"
"s_add_u32 s16, s16, 1\n"
"v_mfma_f32_16x16x32_bf16 a[8:11], a[156:159], a[236:239], a[8:11]\n"
"s_cmp_eq_u32 s16, 15\n"
"s_cselect_b32 s28, -13, 1\n"
"s_lshl_b32 s17, s28, 7\n"
"v_mfma_f32_16x16x32_bf16 a[4:7], a[148:151], a[236:239], a[4:7]\n"
"s_mul_i32 s28, s28, 458752\n"
"v_mfma_f32_16x16x32_bf16 a[4:7], a[144:147], a[232:235], a[4:7]\n"
"s_sub_u32 s78, s78, 1\n"
"v_mfma_f32_16x16x32_bf16 a[0:3], a[136:139], a[232:235], a[0:3]\n"
"v_mfma_f32_16x16x32_bf16 a[0:3], a[140:143], a[236:239], a[0:3]\n"
"s_cbranch_scc0 T1loop_%=\n"
"s_waitcnt lgkmcnt(2)\n"
"v_mfma_f32_16x16x32_bf16 v[32:35], a[64:67], a[208:211], v[32:35]\n"
"ds_read_b128 a[228:231], v7 offset:4096\n"
"v_mfma_f32_16x16x32_bf16 v[32:35], a[68:71], a[212:215], v[32:35]\n"
"ds_read_b128 a[224:227], v6 offset:4096\n"
"v_mfma_f32_16x16x32_bf16 v[36:39], a[76:79], a[212:215], v[36:39]\n"
"v_mfma_f32_16x16x32_bf16 v[36:39], a[72:75], a[208:211], v[36:39]\n"
"v_mfma_f32_16x16x32_bf16 v[40:43], a[80:83], a[208:211], v[40:43]\n"
"v_mfma_f32_16x16x32_bf16 v[40:43], a[84:87], a[212:215], v[40:43]\n"
"v_mfma_f32_16x16x32_bf16 v[44:47], a[92:95], a[212:215], v[44:47]\n"
"v_mfma_f32_16x16x32_bf16 v[44:47], a[88:91], a[208:211], v[44:47]\n"
"v_mfma_f32_16x16x32_bf16 v[48:51], a[96:99], a[208:211], v[48:51]\n"
"v_mfma_f32_16x16x32_bf16 v[48:51], a[100:103], a[212:215], v[48:51]\n"
"v_mfma_f32_16x16x32_bf16 v[52:55], a[108:111], a[212:215], v[52:55]\n"
"v_mfma_f32_16x16x32_bf16 v[52:55], a[104:107], a[208:211], v[52:55]\n"
"v_mfma_f32_16x16x32_bf16 v[56:59], a[112:115], a[208:211], v[56:59]\n"
"v_mfma_f32_16x16x32_bf16 v[56:59], a[116:119], a[212:215], v[56:59]\n"
"v_mfma_f32_16x16x32_bf16 v[60:63], a[124:127], a[212:215], v[60:63]\n"
"v_mfma_f32_16x16x32_bf16 v[60:63], a[120:123], a[208:211], v[60:63]\n"
"v_mfma_f32_16x16x32_bf16 a[32:35], a[208:211], a[128:131], a[32:35]\n"
"v_mfma_f32_16x16x32_bf16 a[32:35], a[212:215], a[132:135], a[32:35]\n"
"s_waitcnt lgkmcnt(2)\n"
"v_mfma_f32_16x16x32_bf16 a[36:39], a[216:219], a[128:131], a[36:39]\n"
"ds_read_b128 a[236:239], v7 offset:6144\n"
"v_mfma_f32_16x16x32_bf16 a[36:39], a[220:223], a[132:135], a[36:39]\n"
"ds_read_b128 a[232:235], v6 offset:6144\n"
"v_mfma_f32_16x16x32_bf16 v[92:95], a[124:127], a[220:223], v[92:95]\n"
"v_mfma_f32_16x16x32_bf16 v[92:95], a[120:123], a[216:219], v[92:95]\n"
"v_mfma_f32_16x16x32_bf16 v[88:91], a[112:115], a[216:219], v[88:91]\n"
"v_mfma_f32_16x16x32_bf16 v[88:91], a[116:119], a[220:223], v[88:91]\n"
"v_mfma_f32_16x16x32_bf16 v[84:87], a[108:111], a[220:223], v[84:87]\n"
"v_mfma_f32_16x16x32_bf16 v[84:87], a[104:107], a[216:219], v[84:87]\n"
"v_mfma_f32_16x16x32_bf16 v[80:83], a[96:99], a[216:219], v[80:83]\n"
"v_mfma_f32_16x16x32_bf16 v[80:83], a[100:103], a[220:223], v[80:83]\n"
"v_mfma_f32_16x16x32_bf16 v[76:79], a[92:95], a[220:223], v[76:79]\n"
"v_mfma_f32_16x16x32_bf16 v[76:79], a[88:91], a[216:219], v[76:79]\n"
"v_mfma_f32_16x16x32_bf16 v[72:75], a[80:83], a[216:219], v[72:75]\n"
"v_mfma_f32_16x16x32_bf16 v[72:75], a[84:87], a[220:223], v[72:75]\n"
"v_mfma_f32_16x16x32_bf16 v[68:71], a[76:79], a[220:223], v[68:71]\n"
"v_mfma_f32_16x16x32_bf16 v[68:71], a[72:75], a[216:219], v[68:71]\n"
"v_mfma_f32_16x16x32_bf16 v[64:67], a[64:67], a[216:219], v[64:67]\n"
"v_mfma_f32_16x16x32_bf16 v[64:67], a[68:71], a[220:223], v[64:67]\n"
"s_waitcnt vmcnt(11) lgkmcnt(0)\n"
"s_barrier\n"
"v_mfma_f32_16x16x32_bf16 v[96:99], a[64:67], a[224:227], v[96:99]\n"
"ds_read_b128 a[212:215], v7 offset:8192\n"
"v_mfma_f32_16x16x32_bf16 v[96:99], a[68:71], a[228:231], v[96:99]\n"
"ds_read_b128 a[208:211], v6 offset:8192\n"
"v_mfma_f32_16x16x32_bf16 v[100:103], a[76:79], a[228:231], v[100:103]\n"
"ds_read_b64_tr_b16 a[136:137], v10 offset:32768\n"
"v_mfma_f32_16x16x32_bf16 v[100:103], a[72:75], a[224:227], v[100:103]\n"
"ds_read_b64_tr_b16 a[138:139], v10 offset:33792\n"
"v_mfma_f32_16x16x32_bf16 v[104:107], a[80:83], a[224:227], v[104:107]\n"
"v_mfma_f32_16x16x32_bf16 v[104:107], a[84:87], a[228:231], v[104:107]\n"
"ds_read_b64_tr_b16 a[140:141], v10 offset:40960\n"
"v_mfma_f32_16x16x32_bf16 v[108:111], a[92:95], a[228:231], v[108:111]\n"
"v_mfma_f32_16x16x32_bf16 v[108:111], a[88:91], a[224:227], v[108:111]\n"
"ds_read_b64_tr_b16 a[142:143], v10 offset:41984\n"
"v_mfma_f32_16x16x32_bf16 v[112:115], a[96:99], a[224:227], v[112:115]\n"
"v_mfma_f32_16x16x32_bf16 v[112:115], a[100:103], a[228:231], v[112:115]\n"
"ds_read_b64_tr_b16 a[144:145], v11 offset:32768\n"
"v_mfma_f32_16x16x32_bf16 v[116:119], a[108:111], a[228:231], v[116:119]\n"
"ds_read_b64_tr_b16 a[146:147], v11 offset:33792\n"
"v_mfma_f32_16x16x32_bf16 v[116:119], a[104:107], a[224:227], v[116:119]\n"
"v_mfma_f32_16x16x32_bf16 v[120:123], a[112:115], a[224:227], v[120:123]\n"
"ds_read_b64_tr_b16 a[148:149], v11 offset:40960\n"
"v_mfma_f32_16x16x32_bf16 v[120:123], a[116:119], a[228:231], v[120:123]\n"
"v_mfma_f32_16x16x32_bf16 v[124:127], a[124:127], a[228:231], v[124:127]\n"
"ds_read_b64_tr_b16 a[150:151], v11 offset:41984\n"
"v_mfma_f32_16x16x32_bf16 v[124:127], a[120:123], a[224:227], v[124:127]\n"
"v_mfma_f32_16x16x32_bf16 a[40:43], a[224:227], a[128:131], a[40:43]\n"
"ds_read_b64_tr_b16 a[152:153], v12 offset:32768\n"
"v_mfma_f32_16x16x32_bf16 a[40:43], a[228:231], a[132:135], a[40:43]\n"
"ds_read_b64_tr_b16 a[154:155], v12 offset:33792\n"
"v_mfma_f32_16x16x32_bf16 a[44:47], a[232:235], a[128:131], a[44:47]\n"
"ds_read_b128 a[220:223], v7 offset:10240\n"
"v_mfma_f32_16x16x32_bf16 a[44:47], a[236:239], a[132:135], a[44:47]\n"
"ds_read_b128 a[216:219], v6 offset:10240\n"
"v_mfma_f32_16x16x32_bf16 v[156:159], a[124:127], a[236:239], v[156:159]\n"
"ds_read_b64_tr_b16 a[156:157], v12 offset:40960\n"
"v_mfma_f32_16x16x32_bf16 v[156:159], a[120:123], a[232:235], v[156:159]\n"
"ds_read_b64_tr_b16 a[158:159], v12 offset:41984\n"
"v_mfma_f32_16x16x32_bf16 v[152:155], a[112:115], a[232:235], v[152:155]\n"
"v_mfma_f32_16x16x32_bf16 v[152:155], a[116:119], a[236:239], v[152:155]\n"
"ds_read_b64_tr_b16 a[160:161], v13 offset:32768\n"
"v_mfma_f32_16x16x32_bf16 v[148:151], a[108:111], a[236:239], v[148:151]\n"
"ds_read_b64_tr_b16 a[162:163], v13 offset:33792\n"
"v_mfma_f32_16x16x32_bf16 v[148:151], a[104:107], a[232:235], v[148:151]\n"
"v_mfma_f32_16x16x32_bf16 v[144:147], a[96:99], a[232:235], v[144:147]\n"
"ds_read_b64_tr_b16 a[164:165], v13 offset:40960\n"
"v_mfma_f32_16x16x32_bf16 v[144:147], a[100:103], a[236:239], v[144:147]\n"
"v_mfma_f32_16x16x32_bf16 v[140:143], a[92:95], a[236:239], v[140:143]\n"
"ds_read_b64_tr_b16 a[166:167], v13 offset:41984\n"
"v_mfma_f32_16x16x32_bf16 v[140:143], a[88:91], a[232:235], v[140:143]\n"
"v_mfma_f32_16x16x32_bf16 v[136:139], a[80:83], a[232:235], v[136:139]\n"
"ds_read_b64_tr_b16 a[168:169], v10 offset:49152\n"
"v_mfma_f32_16x16x32_bf16 v[136:139], a[84:87], a[236:239], v[136:139]\n"
"v_mfma_f32_16x16x32_bf16 v[132:135], a[76:79], a[236:239], v[132:135]\n"
"ds_read_b64_tr_b16 a[170:171], v10 offset:50176\n"
"v_mfma_f32_16x16x32_bf16 v[132:135], a[72:75], a[232:235], v[132:135]\n"
"ds_read_b64_tr_b16 a[172:173], v10 offset:57344\n"
"v_mfma_f32_16x16x32_bf16 v[128:131], a[64:67], a[232:235], v[128:131]\n"
"v_mfma_f32_16x16x32_bf16 v[128:131], a[68:71], a[236:239], v[128:131]\n"
"ds_read_b64_tr_b16 a[174:175], v10 offset:58368\n"
"s_waitcnt lgkmcnt(14)\n"
"v_mfma_f32_16x16x32_bf16 v[160:163], a[64:67], a[208:211], v[160:163]\n"
"ds_read_b128 a[228:231], v7 offset:12288\n"
"v_mfma_f32_16x16x32_bf16 v[160:163], a[68:71], a[212:215], v[160:163]\n"
"ds_read_b128 a[224:227], v6 offset:12288\n"
"v_mfma_f32_16x16x32_bf16 v[164:167], a[76:79], a[212:215], v[164:167]\n"
"ds_read_b64_tr_b16 a[176:177], v11 offset:49152\n"
"v_mfma_f32_16x16x32_bf16 v[164:167], a[72:75], a[208:211], v[164:167]\n"
"ds_read_b64_tr_b16 a[178:179], v11 offset:50176\n"
"v_mfma_f32_16x16x32_bf16 v[168:171], a[80:83], a[208:211], v[168:171]\n"
"ds_read_b64_tr_b16 a[180:181], v11 offset:57344\n"
"v_mfma_f32_16x16x32_bf16 v[168:171], a[84:87], a[212:215], v[168:171]\n"
"v_mfma_f32_16x16x32_bf16 v[172:175], a[92:95], a[212:215], v[172:175]\n"
"ds_read_b64_tr_b16 a[182:183], v11 offset:58368\n"
"v_mfma_f32_16x16x32_bf16 v[172:175], a[88:91], a[208:211], v[172:175]\n"
"v_mfma_f32_16x16x32_bf16 v[176:179], a[96:99], a[208:211], v[176:179]\n"
"ds_read_b64_tr_b16 a[184:185], v12 offset:49152\n"
"v_mfma_f32_16x16x32_bf16 v[176:179], a[100:103], a[212:215], v[176:179]\n"
"v_mfma_f32_16x16x32_bf16 v[180:183], a[108:111], a[212:215], v[180:183]\n"
"ds_read_b64_tr_b16 a[186:187], v12 offset:50176\n"
"v_mfma_f32_16x16x32_bf16 v[180:183], a[104:107], a[208:211], v[180:183]\n"
"ds_read_b64_tr_b16 a[188:189], v12 offset:57344\n"
"v_mfma_f32_16x16x32_bf16 v[184:187], a[112:115], a[208:211], v[184:187]\n"
"v_mfma_f32_16x16x32_bf16 v[184:187], a[116:119], a[212:215], v[184:187]\n"
"ds_read_b64_tr_b16 a[190:191], v12 offset:58368\n"
"v_mfma_f32_16x16x32_bf16 v[188:191], a[124:127], a[212:215], v[188:191]\n"
"v_mfma_f32_16x16x32_bf16 v[188:191], a[120:123], a[208:211], v[188:191]\n"
"ds_read_b64_tr_b16 a[192:193], v13 offset:49152\n"
"v_mfma_f32_16x16x32_bf16 a[48:51], a[208:211], a[128:131], a[48:51]\n"
"v_mfma_f32_16x16x32_bf16 a[48:51], a[212:215], a[132:135], a[48:51]\n"
"ds_read_b64_tr_b16 a[194:195], v13 offset:50176\n"
"s_waitcnt lgkmcnt(14)\n"
"v_mfma_f32_16x16x32_bf16 a[52:55], a[216:219], a[128:131], a[52:55]\n"
"s_waitcnt vmcnt(2)\n"
"ds_read_b128 a[236:239], v7 offset:14336\n"
"v_mfma_f32_16x16x32_bf16 a[52:55], a[220:223], a[132:135], a[52:55]\n"
"ds_read_b64_tr_b16 a[196:197], v13 offset:57344\n"
"v_readlane_b32 s14, v23, 1\n"
"v_mfma_f32_16x16x32_bf16 v[220:223], a[124:127], a[220:223], v[220:223]\n"
"ds_read_b128 a[232:235], v6 offset:14336\n"
"v_mfma_f32_16x16x32_bf16 v[220:223], a[120:123], a[216:219], v[220:223]\n"
"ds_read_b64_tr_b16 a[198:199], v13 offset:58368\n"
"s_nop 3\n"
"v_mfma_f32_16x16x32_bf16 v[216:219], a[112:115], a[216:219], v[216:219]\n"
"s_cmp_lg_u32 s14, s7\n"
"s_cselect_b32 s6, 2, s6\n"
"ds_read_b128 a[200:203], v8 offset:4096\n"
"v_mfma_f32_16x16x32_bf16 v[216:219], a[116:119], a[220:223], v[216:219]\n"
"v_mfma_f32_16x16x32_bf16 v[212:215], a[108:111], a[220:223], v[212:215]\n"
"ds_read_b128 a[204:207], v9 offset:4096\n"
"v_mfma_f32_16x16x32_bf16 v[212:215], a[104:107], a[216:219], v[212:215]\n"
"s_cmp_lt_u32 s6, 2\n"
"s_cbranch_scc1 EV16_%=\n"
"EV16r_%=:\n"
"v_mfma_f32_16x16x32_bf16 v[208:211], a[96:99], a[216:219], v[208:211]\n"
"v_mfma_f32_16x16x32_bf16 v[208:211], a[100:103], a[220:223], v[208:211]\n"
"s_lshr_b32 s69, s83, 1\n"
"s_xor_b32 s69, s69, s13\n"
"v_mfma_f32_16x16x32_bf16 v[204:207], a[92:95], a[220:223], v[204:207]\n"
"s_and_b32 s69, s69, 1\n"
"v_mfma_f32_16x16x32_bf16 v[204:207], a[88:91], a[216:219], v[204:207]\n"
"s_and_b32 s15, s13, 2\n"
"v_mfma_f32_16x16x32_bf16 v[200:203], a[80:83], a[216:219], v[200:203]\n"
"s_add_u32 s69, s69, s15\n"
"v_mfma_f32_16x16x32_bf16 v[200:203], a[84:87], a[220:223], v[200:203]\n"
"v_lshrrev_b32 v5, 4, v1\n"
"v_mfma_f32_16x16x32_bf16 v[196:199], a[76:79], a[220:223], v[196:199]\n"
"v_and_b32 v2, 7, v1\n"
"v_mfma_f32_16x16x32_bf16 v[196:199], a[72:75], a[216:219], v[196:199]\n"
"v_xor_b32 v2, v2, v5\n"
"v_mfma_f32_16x16x32_bf16 v[192:195], a[64:67], a[216:219], v[192:195]\n"
"v_lshlrev_b32 v2, 4, v2\n"
"v_mfma_f32_16x16x32_bf16 v[192:195], a[68:71], a[220:223], v[192:195]\n"
"s_waitcnt vmcnt(0) lgkmcnt(0)\n"
"s_barrier\n"
"v_lshrrev_b32 v5, 5, v1\n"
"v_mfma_f32_16x16x32_bf16 v[224:227], a[64:67], a[224:227], v[224:227]\n"
"ds_read_b128 a[212:215], v7 offset:32768\n"
"v_bfe_u32 v15, v1, 3, 2\n"
"v_mfma_f32_16x16x32_bf16 v[224:227], a[68:71], a[228:231], v[224:227]\n"
"ds_read_b128 a[208:211], v6 offset:32768\n"
"v_lshl_add_u32 v15, v5, 3, v15\n"
"v_mfma_f32_16x16x32_bf16 v[228:231], a[76:79], a[228:231], v[228:231]\n"
"s_lshl_b32 s14, s4, 8\n"
"v_mfma_f32_16x16x32_bf16 v[228:231], a[72:75], a[224:227], v[228:231]\n"
"s_lshl_b32 s15, s69, 6\n"
"v_mfma_f32_16x16x32_bf16 v[232:235], a[80:83], a[224:227], v[232:235]\n"
"buffer_load_dword a255, v14, s[24:27], 0 offen\n"
"v_add_u32 v14, 4096, v14\n"
"v_mfma_f32_16x16x32_bf16 v[232:235], a[84:87], a[228:231], v[232:235]\n"
"s_add_u32 s14, s14, s15\n"
"v_add_u32 v15, s14, v15\n"
"v_lshl_add_u32 v2, v15, 12, v2\n"
"v_mfma_f32_16x16x32_bf16 v[236:239], a[92:95], a[228:231], v[236:239]\n"
"s_and_b32 s14, s83, 1\n"
"v_mfma_f32_16x16x32_bf16 v[236:239], a[88:91], a[224:227], v[236:239]\n"
"s_lshl_b32 s15, s14, 11\n"
"v_mfma_f32_16x16x32_bf16 v[240:243], a[96:99], a[224:227], v[240:243]\n"
"v_add_u32 v2, s15, v2\n"
"v_mfma_f32_16x16x32_bf16 v[240:243], a[100:103], a[228:231], v[240:243]\n"
"v_xor_b32 v3, 64, v2\n"
"v_mfma_f32_16x16x32_bf16 v[244:247], a[108:111], a[228:231], v[244:247]\n"
"v_add_u32 v3, 64512, v3\n"
"v_mfma_f32_16x16x32_bf16 v[244:247], a[104:107], a[224:227], v[244:247]\n"
"s_lshl_b32 s64, s14, 17\n"
"v_mfma_f32_16x16x32_bf16 v[248:251], a[112:115], a[224:227], v[248:251]\n"
"s_add_u32 s65, s64, 14336\n"
"v_mfma_f32_16x16x32_bf16 v[248:251], a[116:119], a[228:231], v[248:251]\n"
"s_xor_b32 s66, s64, 131072\n"
"v_mfma_f32_16x16x32_bf16 v[252:255], a[124:127], a[228:231], v[252:255]\n"
"s_add_u32 s67, s66, 14336\n"
"v_mfma_f32_16x16x32_bf16 v[252:255], a[120:123], a[224:227], v[252:255]\n"
"s_lshl_b32 s10, s14, 12\n"
"v_mfma_f32_16x16x32_bf16 a[56:59], a[224:227], a[128:131], a[56:59]\n"
"s_xor_b32 s11, s10, 4096\n"
"v_mfma_f32_16x16x32_bf16 a[56:59], a[228:231], a[132:135], a[56:59]\n"
"s_mov_b32 s68, 14336\n"
"s_lshl_b32 s76, s13, 13\n"
"v_mfma_f32_16x16x32_bf16 a[60:63], a[232:235], a[128:131], a[60:63]\n"
"ds_read_b128 a[220:223], v7 offset:34816\n"
"s_lshl_b32 s77, s13, 12\n"
"v_mfma_f32_16x16x32_bf16 a[60:63], a[236:239], a[132:135], a[60:63]\n"
"ds_read_b128 a[216:219], v6 offset:34816\n"
"s_lshl_b32 s14, s83, 21\n"
"v_mfma_f32_16x16x32_bf16 a[28:31], a[124:127], a[236:239], a[28:31]\n"
"s_lshl_b32 s15, s5, 14\n"
"v_mfma_f32_16x16x32_bf16 a[28:31], a[120:123], a[232:235], a[28:31]\n"
"s_add_u32 s14, s14, s15\n"
"v_mfma_f32_16x16x32_bf16 a[24:27], a[112:115], a[232:235], a[24:27]\n"
"s_add_u32 s14, s14, s77\n"
"v_mfma_f32_16x16x32_bf16 a[24:27], a[116:119], a[236:239], a[24:27]\n"
"s_add_u32 s14, s14, 15073280\n"
"v_mfma_f32_16x16x32_bf16 a[20:23], a[108:111], a[236:239], a[20:23]\n"
"v_lshlrev_b32 v5, 4, v1\n"
"v_mfma_f32_16x16x32_bf16 a[20:23], a[104:107], a[232:235], a[20:23]\n"
"v_add_u32 v4, s14, v5\n"
"v_mfma_f32_16x16x32_bf16 a[16:19], a[96:99], a[232:235], a[16:19]\n"
"s_add_u32 s77, s77, 32768\n"
"v_mfma_f32_16x16x32_bf16 a[16:19], a[100:103], a[236:239], a[16:19]\n"
"s_mov_b32 s7, 0\n"
"v_mfma_f32_16x16x32_bf16 a[12:15], a[92:95], a[236:239], a[12:15]\n"
"s_mov_b32 s8, 0\n"
"v_mfma_f32_16x16x32_bf16 a[12:15], a[88:91], a[232:235], a[12:15]\n"
"s_mov_b32 s9, 49152\n"
"v_mfma_f32_16x16x32_bf16 a[8:11], a[80:83], a[232:235], a[8:11]\n"
"s_mov_b32 s6, s13\n"
"v_mfma_f32_16x16x32_bf16 a[8:11], a[84:87], a[236:239], a[8:11]\n"
"v_mfma_f32_16x16x32_bf16 a[4:7], a[76:79], a[236:239], a[4:7]\n"
"v_mfma_f32_16x16x32_bf16 a[4:7], a[72:75], a[232:235], a[4:7]\n"
"v_mfma_f32_16x16x32_bf16 a[0:3], a[64:67], a[232:235], a[0:3]\n"
"v_mfma_f32_16x16x32_bf16 a[0:3], a[68:71], a[236:239], a[0:3]\n"
"ds_read_b128 a[84:87], v7 offset:36864\n"
"s_waitcnt lgkmcnt(3)\n"
"v_mfma_f32_16x16x32_bf16 v[32:35], a[136:139], a[208:211], v[32:35]\n"
"v_mfma_f32_16x16x32_bf16 v[32:35], a[140:143], a[212:215], v[32:35]\n"
"ds_read_b128 a[80:83], v6 offset:36864\n"
"v_mfma_f32_16x16x32_bf16 v[48:51], a[172:175], a[212:215], v[48:51]\n"
"ds_read_b128 a[92:95], v7 offset:38912\n"
"v_mfma_f32_16x16x32_bf16 v[48:51], a[168:171], a[208:211], v[48:51]\n"
"ds_read_b128 a[88:91], v6 offset:38912\n"
"v_mfma_f32_16x16x32_bf16 v[36:39], a[144:147], a[208:211], v[36:39]\n"
"v_mfma_f32_16x16x32_bf16 v[36:39], a[148:151], a[212:215], v[36:39]\n"
"ds_read_b128 a[100:103], v7 offset:40960\n"
"v_mfma_f32_16x16x32_bf16 v[52:55], a[180:183], a[212:215], v[52:55]\n"
"ds_read_b128 a[96:99], v6 offset:40960\n"
"v_mfma_f32_16x16x32_bf16 v[52:55], a[176:179], a[208:211], v[52:55]\n"
"ds_read_b128 a[108:111], v7 offset:43008\n"
"v_mfma_f32_16x16x32_bf16 v[40:43], a[152:155], a[208:211], v[40:43]\n"
"v_mfma_f32_16x16x32_bf16 v[40:43], a[156:159], a[212:215], v[40:43]\n"
"ds_read_b128 a[104:107], v6 offset:43008\n"
"v_mfma_f32_16x16x32_bf16 v[56:59], a[188:191], a[212:215], v[56:59]\n"
"ds_read_b128 a[116:119], v7 offset:45056\n"
"v_mfma_f32_16x16x32_bf16 v[56:59], a[184:187], a[208:211], v[56:59]\n"
"ds_read_b128 a[112:115], v6 offset:45056\n"
"v_mfma_f32_16x16x32_bf16 v[44:47], a[160:163], a[208:211], v[44:47]\n"
"v_mfma_f32_16x16x32_bf16 v[44:47], a[164:167], a[212:215], v[44:47]\n"
"ds_read_b128 a[124:127], v7 offset:47104\n"
"v_mfma_f32_16x16x32_bf16 v[60:63], a[196:199], a[212:215], v[60:63]\n"
"ds_read_b128 a[120:123], v6 offset:47104\n"
"v_mfma_f32_16x16x32_bf16 v[60:63], a[192:195], a[208:211], v[60:63]\n"
"s_waitcnt lgkmcnt(0)\n"
"s_barrier\n"
"s_lshl_b32 s14, s82, 5\n"
"v_mfma_f32_16x16x32_bf16 a[32:35], a[208:211], a[200:203], a[32:35]\n"
"s_lshl_b32 s15, s79, 2\n"
"s_add_u32 s14, s14, s15\n"
"s_lshl_b32 s14, s14, 6\n"
"v_mfma_f32_16x16x32_bf16 a[32:35], a[212:215], a[204:207], a[32:35]\n"
"s_lshr_b32 s15, s81, 4\n"
"s_add_u32 s14, s14, s15\n"
"s_and_b32 s15, s13, 1\n"
"v_mfma_f32_16x16x32_bf16 a[36:39], a[216:219], a[200:203], a[36:39]\n"
"s_mul_i32 s15, s15, 1\n"
"s_add_u32 s14, s14, s15\n"
"s_lshl_b32 s14, s14, 11\n"
"v_mfma_f32_16x16x32_bf16 a[36:39], a[220:223], a[204:207], a[36:39]\n"
"s_and_b32 s15, s13, 2\n"
"s_lshl_b32 s15, s15, 9\n"
"s_add_u32 s14, s14, s15\n"
"s_add_u32 s14, s14, 15073280\n"
"v_mfma_f32_16x16x32_bf16 a[40:43], a[80:83], a[200:203], a[40:43]\n"
"v_lshlrev_b32 v7, 4, v1\n"
"v_add_u32 v6, s14, v7\n"
"s_mul_i32 s14, s79, 56\n"
"v_mfma_f32_16x16x32_bf16 a[40:43], a[84:87], a[204:207], a[40:43]\n"
"s_lshl_b32 s15, s80, 1\n"
"s_add_u32 s14, s14, s15\n"
"s_and_b32 s15, s13, 1\n"
"v_mfma_f32_16x16x32_bf16 a[44:47], a[88:91], a[200:203], a[44:47]\n"
"s_add_u32 s14, s14, s15\n"
"s_lshl_b32 s14, s14, 15\n"
"s_and_b32 s15, s13, 2\n"
"v_mfma_f32_16x16x32_bf16 a[44:47], a[92:95], a[204:207], a[44:47]\n"
"s_lshl_b32 s15, s15, 11\n"
"s_add_u32 s14, s14, s15\n"
"s_add_u32 s14, s14, 393216\n"
"v_add_u32 v7, s14, v7\n"
"v_mfma_f32_16x16x32_bf16 a[48:51], a[96:99], a[200:203], a[48:51]\n"
"s_mov_b32 s38, 0xbdd2d3e8\n"
"s_mov_b32 s39, 0xbdd2d3e8\n"
"v_mov_b32 v8, 0xc0135761\n"
"v_mfma_f32_16x16x32_bf16 a[48:51], a[100:103], a[204:207], a[48:51]\n"
"v_mov_b32 v9, 0xc0135761\n"
"s_lshl_b32 s14, s5, 2\n"
"v_and_b32 v12, 3, v1\n"
"v_mfma_f32_16x16x32_bf16 a[52:55], a[104:107], a[200:203], a[52:55]\n"
"v_add_u32 v12, s14, v12\n"
"s_lshr_b32 s15, s83, 1\n"
"s_mul_i32 s14, s15, 14\n"
"v_mfma_f32_16x16x32_bf16 a[52:55], a[108:111], a[204:207], a[52:55]\n"
"v_add_u32 v13, s14, v12\n"
"s_lshl_b32 s15, s15, 1\n"
"s_add_u32 s15, s15, 28\n"
"v_mov_b32 v10, s15\n"
"v_mfma_f32_16x16x32_bf16 a[56:59], a[112:115], a[200:203], a[56:59]\n"
"v_cmp_lt_u32 vcc, 22, v12\n"
"s_nop 1\n"
"v_addc_co_u32 v10, vcc, 0, v10, vcc\n"
"v_mfma_f32_16x16x32_bf16 a[56:59], a[116:119], a[204:207], a[56:59]\n"
"v_cmp_gt_u32 vcc, 14, v12\n"
"s_nop 1\n"
"v_cndmask_b32 v10, v10, v13, vcc\n"
"v_mfma_f32_16x16x32_bf16 a[60:63], a[120:123], a[200:203], a[60:63]\n"
"s_and_b32 s14, s83, 1\n"
"s_lshl_b32 s14, s14, 2\n"
"v_bfe_u32 v12, v1, 2, 2\n"
"v_mfma_f32_16x16x32_bf16 a[60:63], a[124:127], a[204:207], a[60:63]\n"
"v_add_u32 v12, s14, v12\n"
"v_lshl_add_u32 v10, v12, 5, v10\n"
"v_lshlrev_b32 v10, 7, v10\n"
"v_accvgpr_read_b32 v16, a32\n"
"v_accvgpr_read_b32 v17, a33\n"
"v_accvgpr_read_b32 v18, a34\n"
"v_accvgpr_read_b32 v19, a35\n"
"v_accvgpr_read_b32 v20, a36\n"
"v_accvgpr_read_b32 v21, a37\n"
"v_mfma_f32_16x16x32_bf16 v[64:67], a[136:139], a[216:219], v[64:67]\n"
"v_accvgpr_read_b32 v22, a38\n"
"v_accvgpr_read_b32 v23, a39\n"
"v_cvt_pk_bf16_f32 v16, v16, v17\n"
"v_cvt_pk_bf16_f32 v17, v18, v19\n"
"v_cvt_pk_bf16_f32 v18, v20, v21\n"
"v_cvt_pk_bf16_f32 v19, v22, v23\n"
"s_mov_b32 s82, 0\n"
"v_mfma_f32_16x16x32_bf16 v[64:67], a[140:143], a[220:223], v[64:67]\n"
"buffer_store_dwordx4 v[16:19], v6, s[56:59], s82 offen sc1\n"
"v_accvgpr_read_b32 v24, a40\n"
"v_accvgpr_read_b32 v25, a41\n"
"v_accvgpr_read_b32 v26, a42\n"
"v_accvgpr_read_b32 v27, a43\n"
"v_accvgpr_read_b32 v28, a44\n"
"v_accvgpr_read_b32 v29, a45\n"
"v_mfma_f32_16x16x32_bf16 v[80:83], a[172:175], a[220:223], v[80:83]\n"
"v_accvgpr_read_b32 v30, a46\n"
"v_accvgpr_read_b32 v31, a47\n"
"v_cvt_pk_bf16_f32 v24, v24, v25\n"
"v_cvt_pk_bf16_f32 v25, v26, v27\n"
"v_cvt_pk_bf16_f32 v26, v28, v29\n"
"v_cvt_pk_bf16_f32 v27, v30, v31\n"
"s_mov_b32 s82, 131072\n"
"v_mfma_f32_16x16x32_bf16 v[80:83], a[168:171], a[216:219], v[80:83]\n"
"buffer_store_dwordx4 v[24:27], v6, s[56:59], s82 offen sc1\n"
"v_accvgpr_read_b32 v16, a48\n"
"v_accvgpr_read_b32 v17, a49\n"
"v_accvgpr_read_b32 v18, a50\n"
"v_accvgpr_read_b32 v19, a51\n"
"v_accvgpr_read_b32 v20, a52\n"
"v_accvgpr_read_b32 v21, a53\n"
"v_mfma_f32_16x16x32_bf16 v[68:71], a[144:147], a[216:219], v[68:71]\n"
"v_accvgpr_read_b32 v22, a54\n"
"v_accvgpr_read_b32 v23, a55\n"
"v_cvt_pk_bf16_f32 v16, v16, v17\n"
"v_cvt_pk_bf16_f32 v17, v18, v19\n"
"v_cvt_pk_bf16_f32 v18, v20, v21\n"
"v_cvt_pk_bf16_f32 v19, v22, v23\n"
"s_mov_b32 s82, 262144\n"
"v_mfma_f32_16x16x32_bf16 v[68:71], a[148:151], a[220:223], v[68:71]\n"
"buffer_store_dwordx4 v[16:19], v6, s[56:59], s82 offen sc1\n"
"v_accvgpr_read_b32 v24, a56\n"
"v_accvgpr_read_b32 v25, a57\n"
"v_accvgpr_read_b32 v26, a58\n"
"v_accvgpr_read_b32 v27, a59\n"
"v_accvgpr_read_b32 v28, a60\n"
"v_accvgpr_read_b32 v29, a61\n"
"v_mfma_f32_16x16x32_bf16 v[84:87], a[180:183], a[220:223], v[84:87]\n"
"v_accvgpr_read_b32 v30, a62\n"
"v_accvgpr_read_b32 v31, a63\n"
"v_cvt_pk_bf16_f32 v24, v24, v25\n"
"v_cvt_pk_bf16_f32 v25, v26, v27\n"
"v_cvt_pk_bf16_f32 v26, v28, v29\n"
"v_cvt_pk_bf16_f32 v27, v30, v31\n"
"s_mov_b32 s82, 393216\n"
"v_mfma_f32_16x16x32_bf16 v[84:87], a[176:179], a[216:219], v[84:87]\n"
"buffer_store_dwordx4 v[24:27], v6, s[56:59], s82 offen sc1\n"
"s_add_u32 m0, s76, 0\n"
"v_mfma_f32_16x16x32_bf16 v[72:75], a[152:155], a[216:219], v[72:75]\n"
"buffer_load_dwordx4 v2, s[24:27], s64 offen offset:0 lds\n"
"v_mul_f32 v16, v32, v32\n"
"v_mul_f32 v17, v33, v33\n"
"v_mul_f32 v18, v34, v34\n"
"v_mul_f32 v19, v35, v35\n"
"v_mul_f32 v20, v36, v36\n"
"v_mul_f32 v21, v37, v37\n"
"v_mfma_f32_16x16x32_bf16 v[72:75], a[156:159], a[220:223], v[72:75]\n"
"v_mul_f32 v22, v38, v38\n"
"v_mul_f32 v23, v39, v39\n"
"v_fma_f32 v16, v16, s38, v8\n"
"v_fma_f32 v17, v17, s38, v8\n"
"v_fma_f32 v18, v18, s38, v8\n"
"v_fma_f32 v19, v19, s38, v8\n"
"v_mfma_f32_16x16x32_bf16 v[88:91], a[188:191], a[220:223], v[88:91]\n"
"buffer_load_dwordx4 v3, s[24:27], s64 offen offset:1024 lds\n"
"v_fma_f32 v20, v20, s38, v8\n"
"v_fma_f32 v21, v21, s38, v8\n"
"v_fma_f32 v22, v22, s38, v8\n"
"v_fma_f32 v23, v23, s38, v8\n"
"v_mul_f32 v16, v16, v32\n"
"v_mfma_f32_16x16x32_bf16 v[88:91], a[184:187], a[216:219], v[88:91]\n"
"v_mul_f32 v17, v17, v33\n"
"v_mul_f32 v18, v18, v34\n"
"v_mul_f32 v19, v19, v35\n"
"v_mul_f32 v20, v20, v36\n"
"v_mul_f32 v21, v21, v37\n"
"v_mul_f32 v22, v22, v38\n"
"v_mfma_f32_16x16x32_bf16 v[76:79], a[160:163], a[216:219], v[76:79]\n"
"buffer_load_dwordx4 v2, s[24:27], s65 offen offset:2048 lds\n"
"v_mul_f32 v23, v23, v39\n"
"v_exp_f32 v16, v16\n"
"v_exp_f32 v17, v17\n"
"v_exp_f32 v18, v18\n"
"v_exp_f32 v19, v19\n"
"v_exp_f32 v20, v20\n"
"v_mfma_f32_16x16x32_bf16 v[76:79], a[164:167], a[220:223], v[76:79]\n"
"v_exp_f32 v21, v21\n"
"v_exp_f32 v22, v22\n"
"v_exp_f32 v23, v23\n"
"v_mul_f32 v48, v48, v32\n"
"v_mul_f32 v49, v49, v33\n"
"v_mfma_f32_16x16x32_bf16 v[92:95], a[196:199], a[220:223], v[92:95]\n"
"buffer_load_dwordx4 v3, s[24:27], s65 offen offset:3072 lds\n"
"v_mul_f32 v50, v50, v34\n"
"v_mul_f32 v51, v51, v35\n"
"v_mul_f32 v52, v52, v36\n"
"v_mul_f32 v53, v53, v37\n"
"v_mul_f32 v54, v54, v38\n"
"v_mul_f32 v55, v55, v39\n"
"v_mfma_f32_16x16x32_bf16 v[92:95], a[192:195], a[216:219], v[92:95]\n"
"v_add_f32 v16, 1.0, v16\n"
"v_add_f32 v17, 1.0, v17\n"
"v_add_f32 v18, 1.0, v18\n"
"v_add_f32 v19, 1.0, v19\n"
"v_add_f32 v20, 1.0, v20\n"
"v_add_f32 v21, 1.0, v21\n"
"s_add_u32 m0, s76, 4096\n"
"v_mfma_f32_16x16x32_bf16 v[96:99], a[136:139], a[80:83], v[96:99]\n"
"buffer_load_dwordx4 v2, s[24:27], s66 offen offset:0 lds\n"
"v_add_f32 v22, 1.0, v22\n"
"v_add_f32 v23, 1.0, v23\n"
"v_rcp_f32 v16, v16\n"
"v_rcp_f32 v17, v17\n"
"v_rcp_f32 v18, v18\n"
"v_mfma_f32_16x16x32_bf16 v[96:99], a[140:143], a[84:87], v[96:99]\n"
"v_rcp_f32 v19, v19\n"
"v_rcp_f32 v20, v20\n"
"v_rcp_f32 v21, v21\n"
"v_rcp_f32 v22, v22\n"
"v_rcp_f32 v23, v23\n"
"v_mul_f32 v48, v48, v16\n"
"v_mfma_f32_16x16x32_bf16 v[112:115], a[172:175], a[84:87], v[112:115]\n"
"buffer_load_dwordx4 v3, s[24:27], s66 offen offset:1024 lds\n"
"v_mul_f32 v49, v49, v17\n"
"v_mul_f32 v50, v50, v18\n"
"v_mul_f32 v51, v51, v19\n"
"v_mul_f32 v52, v52, v20\n"
"v_mul_f32 v53, v53, v21\n"
"v_mul_f32 v54, v54, v22\n"
"v_mfma_f32_16x16x32_bf16 v[112:115], a[168:171], a[80:83], v[112:115]\n"
"v_mul_f32 v55, v55, v23\n"
"v_cvt_pk_bf16_f32 v32, v48, v49\n"
"v_cvt_pk_bf16_f32 v33, v50, v51\n"
"v_cvt_pk_bf16_f32 v34, v52, v53\n"
"v_cvt_pk_bf16_f32 v35, v54, v55\n"
"v_mfma_f32_16x16x32_bf16 v[100:103], a[144:147], a[80:83], v[100:103]\n"
"buffer_load_dwordx4 v2, s[24:27], s67 offen offset:2048 lds\n"
"v_add_u32 v2, 512, v2\n"
"v_mul_f32 v16, v40, v40\n"
"v_mul_f32 v17, v41, v41\n"
"v_mul_f32 v18, v42, v42\n"
"v_mul_f32 v19, v43, v43\n"
"v_mul_f32 v20, v44, v44\n"
"v_mul_f32 v21, v45, v45\n"
"v_mfma_f32_16x16x32_bf16 v[100:103], a[148:151], a[84:87], v[100:103]\n"
"v_mul_f32 v22, v46, v46\n"
"v_mul_f32 v23, v47, v47\n"
"v_fma_f32 v16, v16, s38, v8\n"
"v_fma_f32 v17, v17, s38, v8\n"
"v_fma_f32 v18, v18, s38, v8\n"
"v_fma_f32 v19, v19, s38, v8\n"
"v_mfma_f32_16x16x32_bf16 v[116:119], a[180:183], a[84:87], v[116:119]\n"
"buffer_load_dwordx4 v3, s[24:27], s67 offen offset:3072 lds\n"
"v_add_u32 v3, 512, v3\n"
"v_fma_f32 v20, v20, s38, v8\n"
"v_fma_f32 v21, v21, s38, v8\n"
"v_fma_f32 v22, v22, s38, v8\n"
"v_fma_f32 v23, v23, s38, v8\n"
"v_mul_f32 v16, v16, v40\n"
"v_mfma_f32_16x16x32_bf16 v[116:119], a[176:179], a[80:83], v[116:119]\n"
"v_mul_f32 v17, v17, v41\n"
"v_mul_f32 v18, v18, v42\n"
"v_mul_f32 v19, v19, v43\n"
"v_mul_f32 v20, v20, v44\n"
"v_mul_f32 v21, v21, v45\n"
"v_mul_f32 v22, v22, v46\n"
"s_add_u32 m0, s76, 49152\n"
"v_mfma_f32_16x16x32_bf16 v[104:107], a[152:155], a[80:83], v[104:107]\n"
"buffer_load_dwordx4 v2, s[24:27], s64 offen offset:0 lds\n"
"v_mul_f32 v23, v23, v47\n"
"v_exp_f32 v16, v16\n"
"v_exp_f32 v17, v17\n"
"v_exp_f32 v18, v18\n"
"v_exp_f32 v19, v19\n"
"v_exp_f32 v20, v20\n"
"v_mfma_f32_16x16x32_bf16 v[104:107], a[156:159], a[84:87], v[104:107]\n"
"v_exp_f32 v21, v21\n"
"v_exp_f32 v22, v22\n"
"v_exp_f32 v23, v23\n"
"v_mul_f32 v56, v56, v40\n"
"v_mul_f32 v57, v57, v41\n"
"v_mfma_f32_16x16x32_bf16 v[120:123], a[188:191], a[84:87], v[120:123]\n"
"buffer_load_dwordx4 v3, s[24:27], s64 offen offset:1024 lds\n"
"v_mul_f32 v58, v58, v42\n"
"v_mul_f32 v59, v59, v43\n"
"v_mul_f32 v60, v60, v44\n"
"v_mul_f32 v61, v61, v45\n"
"v_mul_f32 v62, v62, v46\n"
"v_mul_f32 v63, v63, v47\n"
"v_mfma_f32_16x16x32_bf16 v[120:123], a[184:187], a[80:83], v[120:123]\n"
"v_add_f32 v16, 1.0, v16\n"
"v_add_f32 v17, 1.0, v17\n"
"v_add_f32 v18, 1.0, v18\n"
"v_add_f32 v19, 1.0, v19\n"
"v_add_f32 v20, 1.0, v20\n"
"v_add_f32 v21, 1.0, v21\n"
"v_mfma_f32_16x16x32_bf16 v[108:111], a[160:163], a[80:83], v[108:111]\n"
"buffer_load_dwordx4 v2, s[24:27], s65 offen offset:2048 lds\n"
"v_add_f32 v22, 1.0, v22\n"
"v_add_f32 v23, 1.0, v23\n"
"v_rcp_f32 v16, v16\n"
"v_rcp_f32 v17, v17\n"
"v_rcp_f32 v18, v18\n"
"v_mfma_f32_16x16x32_bf16 v[108:111], a[164:167], a[84:87], v[108:111]\n"
"v_rcp_f32 v19, v19\n"
"v_rcp_f32 v20, v20\n"
"v_rcp_f32 v21, v21\n"
"v_rcp_f32 v22, v22\n"
"v_rcp_f32 v23, v23\n"
"v_mul_f32 v56, v56, v16\n"
"v_mfma_f32_16x16x32_bf16 v[124:127], a[196:199], a[84:87], v[124:127]\n"
"buffer_load_dwordx4 v3, s[24:27], s65 offen offset:3072 lds\n"
"v_mul_f32 v57, v57, v17\n"
"v_mul_f32 v58, v58, v18\n"
"v_mul_f32 v59, v59, v19\n"
"v_mul_f32 v60, v60, v20\n"
"v_mul_f32 v61, v61, v21\n"
"v_mul_f32 v62, v62, v22\n"
"v_mfma_f32_16x16x32_bf16 v[124:127], a[192:195], a[80:83], v[124:127]\n"
"v_mul_f32 v63, v63, v23\n"
"v_cvt_pk_bf16_f32 v36, v56, v57\n"
"v_cvt_pk_bf16_f32 v37, v58, v59\n"
"v_cvt_pk_bf16_f32 v38, v60, v61\n"
"v_cvt_pk_bf16_f32 v39, v62, v63\n"
"s_waitcnt vmcnt(12)\n"
"s_barrier\n"
"s_cmp_eq_u32 s13, 0\n"
"s_cbranch_scc1 EV17_%=\n"
"EV17r_%=:\n"
"s_add_u32 m0, s76, 53248\n"
"v_mfma_f32_16x16x32_bf16 v[128:131], a[136:139], a[88:91], v[128:131]\n"
"buffer_load_dwordx4 v2, s[24:27], s66 offen offset:0 lds\n"
"v_mul_f32 v16, v64, v64\n"
"v_mul_f32 v17, v65, v65\n"
"v_mul_f32 v18, v66, v66\n"
"v_mul_f32 v19, v67, v67\n"
"v_mul_f32 v20, v68, v68\n"
"v_mul_f32 v21, v69, v69\n"
"v_mfma_f32_16x16x32_bf16 v[128:131], a[140:143], a[92:95], v[128:131]\n"
"v_mul_f32 v22, v70, v70\n"
"v_mul_f32 v23, v71, v71\n"
"v_fma_f32 v16, v16, s38, v8\n"
"v_fma_f32 v17, v17, s38, v8\n"
"v_fma_f32 v18, v18, s38, v8\n"
"v_fma_f32 v19, v19, s38, v8\n"
"v_mfma_f32_16x16x32_bf16 v[144:147], a[172:175], a[92:95], v[144:147]\n"
"buffer_load_dwordx4 v3, s[24:27], s66 offen offset:1024 lds\n"
"v_fma_f32 v20, v20, s38, v8\n"
"v_fma_f32 v21, v21, s38, v8\n"
"v_fma_f32 v22, v22, s38, v8\n"
"v_fma_f32 v23, v23, s38, v8\n"
"v_mul_f32 v16, v16, v64\n"
"v_mfma_f32_16x16x32_bf16 v[144:147], a[168:171], a[88:91], v[144:147]\n"
"v_mul_f32 v17, v17, v65\n"
"v_mul_f32 v18, v18, v66\n"
"v_mul_f32 v19, v19, v67\n"
"v_mul_f32 v20, v20, v68\n"
"v_mul_f32 v21, v21, v69\n"
"v_mul_f32 v22, v22, v70\n"
"v_mfma_f32_16x16x32_bf16 v[132:135], a[144:147], a[88:91], v[132:135]\n"
"buffer_load_dwordx4 v2, s[24:27], s67 offen offset:2048 lds\n"
"v_add_u32 v2, 512, v2\n"
"v_mul_f32 v23, v23, v71\n"
"v_exp_f32 v16, v16\n"
"v_exp_f32 v17, v17\n"
"v_exp_f32 v18, v18\n"
"v_exp_f32 v19, v19\n"
"v_exp_f32 v20, v20\n"
"v_mfma_f32_16x16x32_bf16 v[132:135], a[148:151], a[92:95], v[132:135]\n"
"v_exp_f32 v21, v21\n"
"v_exp_f32 v22, v22\n"
"v_exp_f32 v23, v23\n"
"v_mul_f32 v80, v80, v64\n"
"v_mul_f32 v81, v81, v65\n"
"v_mfma_f32_16x16x32_bf16 v[148:151], a[180:183], a[92:95], v[148:151]\n"
"buffer_load_dwordx4 v3, s[24:27], s67 offen offset:3072 lds\n"
"v_add_u32 v3, 512, v3\n"
"v_mul_f32 v82, v82, v66\n"
"v_mul_f32 v83, v83, v67\n"
"v_mul_f32 v84, v84, v68\n"
"v_mul_f32 v85, v85, v69\n"
"v_mul_f32 v86, v86, v70\n"
"v_mul_f32 v87, v87, v71\n"
"v_mfma_f32_16x16x32_bf16 v[148:151], a[176:179], a[88:91], v[148:151]\n"
"v_add_f32 v16, 1.0, v16\n"
"v_add_f32 v17, 1.0, v17\n"
"v_add_f32 v18, 1.0, v18\n"
"v_add_f32 v19, 1.0, v19\n"
"v_add_f32 v20, 1.0, v20\n"
"v_add_f32 v21, 1.0, v21\n"
"s_add_u32 m0, s76, 98304\n"
"v_mfma_f32_16x16x32_bf16 v[136:139], a[152:155], a[88:91], v[136:139]\n"
"buffer_load_dwordx4 v2, s[24:27], s64 offen offset:0 lds\n"
"v_add_f32 v22, 1.0, v22\n"
"v_add_f32 v23, 1.0, v23\n"
"v_rcp_f32 v16, v16\n"
"v_rcp_f32 v17, v17\n"
"v_rcp_f32 v18, v18\n"
"v_mfma_f32_16x16x32_bf16 v[136:139], a[156:159], a[92:95], v[136:139]\n"
"v_rcp_f32 v19, v19\n"
"v_rcp_f32 v20, v20\n"
"v_rcp_f32 v21, v21\n"
"v_rcp_f32 v22, v22\n"
"v_rcp_f32 v23, v23\n"
"v_mul_f32 v80, v80, v16\n"
"v_mfma_f32_16x16x32_bf16 v[152:155], a[188:191], a[92:95], v[152:155]\n"
"buffer_load_dwordx4 v3, s[24:27], s64 offen offset:1024 lds\n"
"v_mul_f32 v81, v81, v17\n"
"v_mul_f32 v82, v82, v18\n"
"v_mul_f32 v83, v83, v19\n"
"v_mul_f32 v84, v84, v20\n"
"v_mul_f32 v85, v85, v21\n"
"v_mul_f32 v86, v86, v22\n"
"v_mfma_f32_16x16x32_bf16 v[152:155], a[184:187], a[88:91], v[152:155]\n"
"v_mul_f32 v87, v87, v23\n"
"v_cvt_pk_bf16_f32 v64, v80, v81\n"
"v_cvt_pk_bf16_f32 v65, v82, v83\n"
"v_cvt_pk_bf16_f32 v66, v84, v85\n"
"v_cvt_pk_bf16_f32 v67, v86, v87\n"
"v_mfma_f32_16x16x32_bf16 v[140:143], a[160:163], a[88:91], v[140:143]\n"
"buffer_load_dwordx4 v2, s[24:27], s65 offen offset:2048 lds\n"
"v_mul_f32 v16, v72, v72\n"
"v_mul_f32 v17, v73, v73\n"
"v_mul_f32 v18, v74, v74\n"
"v_mul_f32 v19, v75, v75\n"
"v_mul_f32 v20, v76, v76\n"
"v_mul_f32 v21, v77, v77\n"
"v_mfma_f32_16x16x32_bf16 v[140:143], a[164:167], a[92:95], v[140:143]\n"
"v_mul_f32 v22, v78, v78\n"
"v_mul_f32 v23, v79, v79\n"
"v_fma_f32 v16, v16, s38, v8\n"
"v_fma_f32 v17, v17, s38, v8\n"
"v_fma_f32 v18, v18, s38, v8\n"
"v_fma_f32 v19, v19, s38, v8\n"
"v_mfma_f32_16x16x32_bf16 v[156:159], a[196:199], a[92:95], v[156:159]\n"
"buffer_load_dwordx4 v3, s[24:27], s65 offen offset:3072 lds\n"
"v_fma_f32 v20, v20, s38, v8\n"
"v_fma_f32 v21, v21, s38, v8\n"
"v_fma_f32 v22, v22, s38, v8\n"
"v_fma_f32 v23, v23, s38, v8\n"
"v_mul_f32 v16, v16, v72\n"
"v_mfma_f32_16x16x32_bf16 v[156:159], a[192:195], a[88:91], v[156:159]\n"
"v_mul_f32 v17, v17, v73\n"
"v_mul_f32 v18, v18, v74\n"
"v_mul_f32 v19, v19, v75\n"
"v_mul_f32 v20, v20, v76\n"
"v_mul_f32 v21, v21, v77\n"
"v_mul_f32 v22, v22, v78\n"
"s_add_u32 m0, s76, 102400\n"
"v_mfma_f32_16x16x32_bf16 v[160:163], a[136:139], a[96:99], v[160:163]\n"
"buffer_load_dwordx4 v2, s[24:27], s66 offen offset:0 lds\n"
"v_mul_f32 v23, v23, v79\n"
"v_exp_f32 v16, v16\n"
"v_exp_f32 v17, v17\n"
"v_exp_f32 v18, v18\n"
"v_exp_f32 v19, v19\n"
"v_exp_f32 v20, v20\n"
"v_mfma_f32_16x16x32_bf16 v[160:163], a[140:143], a[100:103], v[160:163]\n"
"v_exp_f32 v21, v21\n"
"v_exp_f32 v22, v22\n"
"v_exp_f32 v23, v23\n"
"v_mul_f32 v88, v88, v72\n"
"v_mul_f32 v89, v89, v73\n"
"v_mfma_f32_16x16x32_bf16 v[176:179], a[172:175], a[100:103], v[176:179]\n"
"buffer_load_dwordx4 v3, s[24:27], s66 offen offset:1024 lds\n"
"v_mul_f32 v90, v90, v74\n"
"v_mul_f32 v91, v91, v75\n"
"v_mul_f32 v92, v92, v76\n"
"v_mul_f32 v93, v93, v77\n"
"v_mul_f32 v94, v94, v78\n"
"v_mul_f32 v95, v95, v79\n"
"v_mfma_f32_16x16x32_bf16 v[176:179], a[168:171], a[96:99], v[176:179]\n"
"v_add_f32 v16, 1.0, v16\n"
"v_add_f32 v17, 1.0, v17\n"
"v_add_f32 v18, 1.0, v18\n"
"v_add_f32 v19, 1.0, v19\n"
"v_add_f32 v20, 1.0, v20\n"
"v_add_f32 v21, 1.0, v21\n"
"v_mfma_f32_16x16x32_bf16 v[164:167], a[144:147], a[96:99], v[164:167]\n"
"buffer_load_dwordx4 v2, s[24:27], s67 offen offset:2048 lds\n"
"v_add_u32 v2, 512, v2\n"
"v_add_f32 v22, 1.0, v22\n"
"v_add_f32 v23, 1.0, v23\n"
"v_rcp_f32 v16, v16\n"
"v_rcp_f32 v17, v17\n"
"v_rcp_f32 v18, v18\n"
"v_mfma_f32_16x16x32_bf16 v[164:167], a[148:151], a[100:103], v[164:167]\n"
"v_rcp_f32 v19, v19\n"
"v_rcp_f32 v20, v20\n"
"v_rcp_f32 v21, v21\n"
"v_rcp_f32 v22, v22\n"
"v_rcp_f32 v23, v23\n"
"v_mul_f32 v88, v88, v16\n"
"v_mfma_f32_16x16x32_bf16 v[180:183], a[180:183], a[100:103], v[180:183]\n"
"buffer_load_dwordx4 v3, s[24:27], s67 offen offset:3072 lds\n"
"v_add_u32 v3, 512, v3\n"
"v_mul_f32 v89, v89, v17\n"
"v_mul_f32 v90, v90, v18\n"
"v_mul_f32 v91, v91, v19\n"
"v_mul_f32 v92, v92, v20\n"
"v_mul_f32 v93, v93, v21\n"
"v_mul_f32 v94, v94, v22\n"
"v_mfma_f32_16x16x32_bf16 v[180:183], a[176:179], a[96:99], v[180:183]\n"
"v_mul_f32 v95, v95, v23\n"
"v_cvt_pk_bf16_f32 v68, v88, v89\n"
"v_cvt_pk_bf16_f32 v69, v90, v91\n"
"v_cvt_pk_bf16_f32 v70, v92, v93\n"
"v_cvt_pk_bf16_f32 v71, v94, v95\n"
"v_mfma_f32_16x16x32_bf16 v[168:171], a[152:155], a[96:99], v[168:171]\n"
"v_mul_f32 v16, v96, v96\n"
"v_mul_f32 v17, v97, v97\n"
"v_mul_f32 v18, v98, v98\n"
"v_mul_f32 v19, v99, v99\n"
"v_mul_f32 v20, v100, v100\n"
"v_mul_f32 v21, v101, v101\n"
"v_mfma_f32_16x16x32_bf16 v[168:171], a[156:159], a[100:103], v[168:171]\n"
"v_mul_f32 v22, v102, v102\n"
"v_mul_f32 v23, v103, v103\n"
"v_fma_f32 v16, v16, s38, v8\n"
"v_fma_f32 v17, v17, s38, v8\n"
"v_fma_f32 v18, v18, s38, v8\n"
"v_fma_f32 v19, v19, s38, v8\n"
"v_mfma_f32_16x16x32_bf16 v[184:187], a[188:191], a[100:103], v[184:187]\n"
"v_fma_f32 v20, v20, s38, v8\n"
"v_fma_f32 v21, v21, s38, v8\n"
"v_fma_f32 v22, v22, s38, v8\n"
"v_fma_f32 v23, v23, s38, v8\n"
"v_mul_f32 v16, v16, v96\n"
"v_mfma_f32_16x16x32_bf16 v[184:187], a[184:187], a[96:99], v[184:187]\n"
"v_mul_f32 v17, v17, v97\n"
"v_mul_f32 v18, v18, v98\n"
"v_mul_f32 v19, v19, v99\n"
"v_mul_f32 v20, v20, v100\n"
"v_mul_f32 v21, v21, v101\n"
"v_mul_f32 v22, v22, v102\n"
"v_mfma_f32_16x16x32_bf16 v[172:175], a[160:163], a[96:99], v[172:175]\n"
"v_mul_f32 v23, v23, v103\n"
"v_exp_f32 v16, v16\n"
"v_exp_f32 v17, v17\n"
"v_exp_f32 v18, v18\n"
"v_exp_f32 v19, v19\n"
"v_exp_f32 v20, v20\n"
"v_mfma_f32_16x16x32_bf16 v[172:175], a[164:167], a[100:103], v[172:175]\n"
"v_exp_f32 v21, v21\n"
"v_exp_f32 v22, v22\n"
"v_exp_f32 v23, v23\n"
"v_mul_f32 v112, v112, v96\n"
"v_mul_f32 v113, v113, v97\n"
"v_mfma_f32_16x16x32_bf16 v[188:191], a[196:199], a[100:103], v[188:191]\n"
"v_mul_f32 v114, v114, v98\n"
"v_mul_f32 v115, v115, v99\n"
"v_mul_f32 v116, v116, v100\n"
"v_mul_f32 v117, v117, v101\n"
"v_mul_f32 v118, v118, v102\n"
"v_mul_f32 v119, v119, v103\n"
"v_mfma_f32_16x16x32_bf16 v[188:191], a[192:195], a[96:99], v[188:191]\n"
"v_add_f32 v16, 1.0, v16\n"
"v_add_f32 v17, 1.0, v17\n"
"v_add_f32 v18, 1.0, v18\n"
"v_add_f32 v19, 1.0, v19\n"
"v_add_f32 v20, 1.0, v20\n"
"v_add_f32 v21, 1.0, v21\n"
"v_mfma_f32_16x16x32_bf16 v[192:195], a[136:139], a[104:107], v[192:195]\n"
"v_add_f32 v22, 1.0, v22\n"
"v_add_f32 v23, 1.0, v23\n"
"v_rcp_f32 v16, v16\n"
"v_rcp_f32 v17, v17\n"
"v_rcp_f32 v18, v18\n"
"v_mfma_f32_16x16x32_bf16 v[192:195], a[140:143], a[108:111], v[192:195]\n"
"v_rcp_f32 v19, v19\n"
"v_rcp_f32 v20, v20\n"
"v_rcp_f32 v21, v21\n"
"v_rcp_f32 v22, v22\n"
"v_rcp_f32 v23, v23\n"
"v_mul_f32 v112, v112, v16\n"
"v_mfma_f32_16x16x32_bf16 v[208:211], a[172:175], a[108:111], v[208:211]\n"
"v_mul_f32 v113, v113, v17\n"
"v_mul_f32 v114, v114, v18\n"
"v_mul_f32 v115, v115, v19\n"
"v_mul_f32 v116, v116, v20\n"
"v_mul_f32 v117, v117, v21\n"
"v_mul_f32 v118, v118, v22\n"
"v_mfma_f32_16x16x32_bf16 v[208:211], a[168:171], a[104:107], v[208:211]\n"
"v_mul_f32 v119, v119, v23\n"
"v_cvt_pk_bf16_f32 v96, v112, v113\n"
"v_cvt_pk_bf16_f32 v97, v114, v115\n"
"v_cvt_pk_bf16_f32 v98, v116, v117\n"
"v_cvt_pk_bf16_f32 v99, v118, v119\n"
"buffer_load_dword v11, v10, s[56:59], 0 offen offset:0 sc1\n"
"v_mfma_f32_16x16x32_bf16 v[196:199], a[144:147], a[104:107], v[196:199]\n"
"v_mul_f32 v16, v104, v104\n"
"v_mul_f32 v17, v105, v105\n"
"v_mul_f32 v18, v106, v106\n"
"v_mul_f32 v19, v107, v107\n"
"v_mul_f32 v20, v108, v108\n"
"v_mul_f32 v21, v109, v109\n"
"v_mfma_f32_16x16x32_bf16 v[196:199], a[148:151], a[108:111], v[196:199]\n"
"v_mul_f32 v22, v110, v110\n"
"v_mul_f32 v23, v111, v111\n"
"v_fma_f32 v16, v16, s38, v8\n"
"v_fma_f32 v17, v17, s38, v8\n"
"v_fma_f32 v18, v18, s38, v8\n"
"v_fma_f32 v19, v19, s38, v8\n"
"v_mfma_f32_16x16x32_bf16 v[212:215], a[180:183], a[108:111], v[212:215]\n"
"v_fma_f32 v20, v20, s38, v8\n"
"v_fma_f32 v21, v21, s38, v8\n"
"v_fma_f32 v22, v22, s38, v8\n"
"v_fma_f32 v23, v23, s38, v8\n"
"v_mul_f32 v16, v16, v104\n"
"v_mfma_f32_16x16x32_bf16 v[212:215], a[176:179], a[104:107], v[212:215]\n"
"v_mul_f32 v17, v17, v105\n"
"v_mul_f32 v18, v18, v106\n"
"v_mul_f32 v19, v19, v107\n"
"v_mul_f32 v20, v20, v108\n"
"v_mul_f32 v21, v21, v109\n"
"v_mul_f32 v22, v22, v110\n"
"v_mfma_f32_16x16x32_bf16 v[200:203], a[152:155], a[104:107], v[200:203]\n"
"v_mul_f32 v23, v23, v111\n"
"v_exp_f32 v16, v16\n"
"v_exp_f32 v17, v17\n"
"v_exp_f32 v18, v18\n"
"v_exp_f32 v19, v19\n"
"v_exp_f32 v20, v20\n"
"v_mfma_f32_16x16x32_bf16 v[200:203], a[156:159], a[108:111], v[200:203]\n"
"v_exp_f32 v21, v21\n"
"v_exp_f32 v22, v22\n"
"v_exp_f32 v23, v23\n"
"v_mul_f32 v120, v120, v104\n"
"v_mul_f32 v121, v121, v105\n"
"v_mfma_f32_16x16x32_bf16 v[216:219], a[188:191], a[108:111], v[216:219]\n"
"v_mul_f32 v122, v122, v106\n"
"v_mul_f32 v123, v123, v107\n"
"v_mul_f32 v124, v124, v108\n"
"v_mul_f32 v125, v125, v109\n"
"v_mul_f32 v126, v126, v110\n"
"v_mul_f32 v127, v127, v111\n"
"v_mfma_f32_16x16x32_bf16 v[216:219], a[184:187], a[104:107], v[216:219]\n"
"v_add_f32 v16, 1.0, v16\n"
"v_add_f32 v17, 1.0, v17\n"
"v_add_f32 v18, 1.0, v18\n"
"v_add_f32 v19, 1.0, v19\n"
"v_add_f32 v20, 1.0, v20\n"
"v_add_f32 v21, 1.0, v21\n"
"v_mfma_f32_16x16x32_bf16 v[204:207], a[160:163], a[104:107], v[204:207]\n"
"v_add_f32 v22, 1.0, v22\n"
"v_add_f32 v23, 1.0, v23\n"
"v_rcp_f32 v16, v16\n"
"v_rcp_f32 v17, v17\n"
"v_rcp_f32 v18, v18\n"
"v_mfma_f32_16x16x32_bf16 v[204:207], a[164:167], a[108:111], v[204:207]\n"
"v_rcp_f32 v19, v19\n"
"v_rcp_f32 v20, v20\n"
"v_rcp_f32 v21, v21\n"
"v_rcp_f32 v22, v22\n"
"v_rcp_f32 v23, v23\n"
"v_mul_f32 v120, v120, v16\n"
"v_mfma_f32_16x16x32_bf16 v[220:223], a[196:199], a[108:111], v[220:223]\n"
"v_mul_f32 v121, v121, v17\n"
"v_mul_f32 v122, v122, v18\n"
"v_mul_f32 v123, v123, v19\n"
"v_mul_f32 v124, v124, v20\n"
"v_mul_f32 v125, v125, v21\n"
"v_mul_f32 v126, v126, v22\n"
"v_mfma_f32_16x16x32_bf16 v[220:223], a[192:195], a[104:107], v[220:223]\n"
"v_mul_f32 v127, v127, v23\n"
"v_cvt_pk_bf16_f32 v100, v120, v121\n"
"v_cvt_pk_bf16_f32 v101, v122, v123\n"
"v_cvt_pk_bf16_f32 v102, v124, v125\n"
"v_cvt_pk_bf16_f32 v103, v126, v127\n"
"s_waitcnt vmcnt(0)\n"
"v_cmp_ne_u32 vcc, 1, v11\n"
"s_nop 4\n"
"s_cbranch_vccnz PL18_%=\n"
"PL18d_%=:\n"
"s_add_u32 m0, s77, 0\n"
"v_mfma_f32_16x16x32_bf16 v[224:227], a[136:139], a[112:115], v[224:227]\n"
"buffer_load_dwordx4 v4, s[56:59], 0 offen offset:0 sc1 lds\n"
"v_mul_f32 v16, v128, v128\n"
"v_mul_f32 v17, v129, v129\n"
"v_mul_f32 v18, v130, v130\n"
"v_mul_f32 v19, v131, v131\n"
"v_mul_f32 v20, v132, v132\n"
"v_mul_f32 v21, v133, v133\n"
"v_mfma_f32_16x16x32_bf16 v[224:227], a[140:143], a[116:119], v[224:227]\n"
"buffer_load_dwordx4 v4, s[56:59], 0 offen offset:1024 sc1 lds\n"
"v_mul_f32 v22, v134, v134\n"
"v_mul_f32 v23, v135, v135\n"
"v_fma_f32 v16, v16, s38, v8\n"
"v_fma_f32 v17, v17, s38, v8\n"
"v_fma_f32 v18, v18, s38, v8\n"
"v_fma_f32 v19, v19, s38, v8\n"
"v_mfma_f32_16x16x32_bf16 v[240:243], a[172:175], a[116:119], v[240:243]\n"
"buffer_load_dwordx4 v4, s[56:59], 0 offen offset:2048 sc1 lds\n"
"v_fma_f32 v20, v20, s38, v8\n"
"v_fma_f32 v21, v21, s38, v8\n"
"v_fma_f32 v22, v22, s38, v8\n"
"v_fma_f32 v23, v23, s38, v8\n"
"v_mul_f32 v16, v16, v128\n"
"v_mfma_f32_16x16x32_bf16 v[240:243], a[168:171], a[112:115], v[240:243]\n"
"buffer_load_dwordx4 v4, s[56:59], 0 offen offset:3072 sc1 lds\n"
"v_add_u32 v4, 524288, v4\n"
"v_mul_f32 v17, v17, v129\n"
"v_mul_f32 v18, v18, v130\n"
"v_mul_f32 v19, v19, v131\n"
"v_mul_f32 v20, v20, v132\n"
"v_mul_f32 v21, v21, v133\n"
"v_mul_f32 v22, v22, v134\n"
"s_add_u32 m0, s77, 49152\n"
"v_mfma_f32_16x16x32_bf16 v[228:231], a[144:147], a[112:115], v[228:231]\n"
"buffer_load_dwordx4 v4, s[56:59], 0 offen offset:0 sc1 lds\n"
"v_mul_f32 v23, v23, v135\n"
"v_exp_f32 v16, v16\n"
"v_exp_f32 v17, v17\n"
"v_exp_f32 v18, v18\n"
"v_exp_f32 v19, v19\n"
"v_exp_f32 v20, v20\n"
"v_mfma_f32_16x16x32_bf16 v[228:231], a[148:151], a[116:119], v[228:231]\n"
"buffer_load_dwordx4 v4, s[56:59], 0 offen offset:1024 sc1 lds\n"
"v_exp_f32 v21, v21\n"
"v_exp_f32 v22, v22\n"
"v_exp_f32 v23, v23\n"
"v_mul_f32 v144, v144, v128\n"
"v_mul_f32 v145, v145, v129\n"
"v_mfma_f32_16x16x32_bf16 v[244:247], a[180:183], a[116:119], v[244:247]\n"
"buffer_load_dwordx4 v4, s[56:59], 0 offen offset:2048 sc1 lds\n"
"v_mul_f32 v146, v146, v130\n"
"v_mul_f32 v147, v147, v131\n"
"v_mul_f32 v148, v148, v132\n"
"v_mul_f32 v149, v149, v133\n"
"v_mul_f32 v150, v150, v134\n"
"v_mul_f32 v151, v151, v135\n"
"v_mfma_f32_16x16x32_bf16 v[244:247], a[176:179], a[112:115], v[244:247]\n"
"buffer_load_dwordx4 v4, s[56:59], 0 offen offset:3072 sc1 lds\n"
"v_add_u32 v4, 524288, v4\n"
"v_add_f32 v16, 1.0, v16\n"
"v_add_f32 v17, 1.0, v17\n"
"v_add_f32 v18, 1.0, v18\n"
"v_add_f32 v19, 1.0, v19\n"
"v_add_f32 v20, 1.0, v20\n"
"v_add_f32 v21, 1.0, v21\n"
"s_add_u32 m0, s77, 98304\n"
"v_mfma_f32_16x16x32_bf16 v[232:235], a[152:155], a[112:115], v[232:235]\n"
"buffer_load_dwordx4 v4, s[56:59], 0 offen offset:0 sc1 lds\n"
"v_add_f32 v22, 1.0, v22\n"
"v_add_f32 v23, 1.0, v23\n"
"v_rcp_f32 v16, v16\n"
"v_rcp_f32 v17, v17\n"
"v_rcp_f32 v18, v18\n"
"v_mfma_f32_16x16x32_bf16 v[232:235], a[156:159], a[116:119], v[232:235]\n"
"buffer_load_dwordx4 v4, s[56:59], 0 offen offset:1024 sc1 lds\n"
"v_rcp_f32 v19, v19\n"
"v_rcp_f32 v20, v20\n"
"v_rcp_f32 v21, v21\n"
"v_rcp_f32 v22, v22\n"
"v_rcp_f32 v23, v23\n"
"v_mul_f32 v144, v144, v16\n"
"v_mfma_f32_16x16x32_bf16 v[248:251], a[188:191], a[116:119], v[248:251]\n"
"buffer_load_dwordx4 v4, s[56:59], 0 offen offset:2048 sc1 lds\n"
"v_mul_f32 v145, v145, v17\n"
"v_mul_f32 v146, v146, v18\n"
"v_mul_f32 v147, v147, v19\n"
"v_mul_f32 v148, v148, v20\n"
"v_mul_f32 v149, v149, v21\n"
"v_mul_f32 v150, v150, v22\n"
"v_mfma_f32_16x16x32_bf16 v[248:251], a[184:187], a[112:115], v[248:251]\n"
"buffer_load_dwordx4 v4, s[56:59], 0 offen offset:3072 sc1 lds\n"
"v_add_u32 v4, 524288, v4\n"
"v_mul_f32 v151, v151, v23\n"
"v_cvt_pk_bf16_f32 v128, v144, v145\n"
"v_cvt_pk_bf16_f32 v129, v146, v147\n"
"v_cvt_pk_bf16_f32 v130, v148, v149\n"
"v_cvt_pk_bf16_f32 v131, v150, v151\n"
"s_mov_b32 s82, 0\n"
"v_mfma_f32_16x16x32_bf16 v[236:239], a[160:163], a[112:115], v[236:239]\n"
"buffer_store_dwordx4 v[32:35], v7, s[56:59], s82 offen offset:0 sc1\n"
"v_mul_f32 v16, v136, v136\n"
"v_mul_f32 v17, v137, v137\n"
"v_mul_f32 v18, v138, v138\n"
"v_mul_f32 v19, v139, v139\n"
"v_mul_f32 v20, v140, v140\n"
"v_mul_f32 v21, v141, v141\n"
"v_mfma_f32_16x16x32_bf16 v[236:239], a[164:167], a[116:119], v[236:239]\n"
"v_mul_f32 v22, v142, v142\n"
"v_mul_f32 v23, v143, v143\n"
"v_fma_f32 v16, v16, s38, v8\n"
"v_fma_f32 v17, v17, s38, v8\n"
"v_fma_f32 v18, v18, s38, v8\n"
"v_fma_f32 v19, v19, s38, v8\n"
"v_mfma_f32_16x16x32_bf16 v[252:255], a[196:199], a[116:119], v[252:255]\n"
"v_fma_f32 v20, v20, s38, v8\n"
"v_fma_f32 v21, v21, s38, v8\n"
"v_fma_f32 v22, v22, s38, v8\n"
"v_fma_f32 v23, v23, s38, v8\n"
"v_mul_f32 v16, v16, v136\n"
"v_mfma_f32_16x16x32_bf16 v[252:255], a[192:195], a[112:115], v[252:255]\n"
"v_mul_f32 v17, v17, v137\n"
"v_mul_f32 v18, v18, v138\n"
"v_mul_f32 v19, v19, v139\n"
"v_mul_f32 v20, v20, v140\n"
"v_mul_f32 v21, v21, v141\n"
"v_mul_f32 v22, v22, v142\n"
"s_mov_b32 s82, 0\n"
"v_mfma_f32_16x16x32_bf16 a[0:3], a[136:139], a[120:123], a[0:3]\n"
"buffer_store_dwordx4 v[36:39], v7, s[56:59], s82 offen offset:1024 sc1\n"
"v_mul_f32 v23, v23, v143\n"
"v_exp_f32 v16, v16\n"
"v_exp_f32 v17, v17\n"
"v_exp_f32 v18, v18\n"
"v_exp_f32 v19, v19\n"
"v_exp_f32 v20, v20\n"
"v_mfma_f32_16x16x32_bf16 a[0:3], a[140:143], a[124:127], a[0:3]\n"
"v_exp_f32 v21, v21\n"
"v_exp_f32 v22, v22\n"
"v_exp_f32 v23, v23\n"
"v_mul_f32 v152, v152, v136\n"
"v_mul_f32 v153, v153, v137\n"
"v_mfma_f32_16x16x32_bf16 a[16:19], a[172:175], a[124:127], a[16:19]\n"
"v_mul_f32 v154, v154, v138\n"
"v_mul_f32 v155, v155, v139\n"
"v_mul_f32 v156, v156, v140\n"
"v_mul_f32 v157, v157, v141\n"
"v_mul_f32 v158, v158, v142\n"
"v_mul_f32 v159, v159, v143\n"
"v_mfma_f32_16x16x32_bf16 a[16:19], a[168:171], a[120:123], a[16:19]\n"
"v_add_f32 v16, 1.0, v16\n"
"v_add_f32 v17, 1.0, v17\n"
"v_add_f32 v18, 1.0, v18\n"
"v_add_f32 v19, 1.0, v19\n"
"v_add_f32 v20, 1.0, v20\n"
"v_add_f32 v21, 1.0, v21\n"
"s_mov_b32 s82, 0\n"
"v_mfma_f32_16x16x32_bf16 a[4:7], a[144:147], a[120:123], a[4:7]\n"
"buffer_store_dwordx4 v[64:67], v7, s[56:59], s82 offen offset:2048 sc1\n"
"v_add_f32 v22, 1.0, v22\n"
"v_add_f32 v23, 1.0, v23\n"
"v_rcp_f32 v16, v16\n"
"v_rcp_f32 v17, v17\n"
"v_rcp_f32 v18, v18\n"
"v_mfma_f32_16x16x32_bf16 a[4:7], a[148:151], a[124:127], a[4:7]\n"
"v_rcp_f32 v19, v19\n"
"v_rcp_f32 v20, v20\n"
"v_rcp_f32 v21, v21\n"
"v_rcp_f32 v22, v22\n"
"v_rcp_f32 v23, v23\n"
"v_mul_f32 v152, v152, v16\n"
"v_mfma_f32_16x16x32_bf16 a[20:23], a[180:183], a[124:127], a[20:23]\n"
"v_mul_f32 v153, v153, v17\n"
"v_mul_f32 v154, v154, v18\n"
"v_mul_f32 v155, v155, v19\n"
"v_mul_f32 v156, v156, v20\n"
"v_mul_f32 v157, v157, v21\n"
"v_mul_f32 v158, v158, v22\n"
"v_mfma_f32_16x16x32_bf16 a[20:23], a[176:179], a[120:123], a[20:23]\n"
"v_mul_f32 v159, v159, v23\n"
"v_cvt_pk_bf16_f32 v132, v152, v153\n"
"v_cvt_pk_bf16_f32 v133, v154, v155\n"
"v_cvt_pk_bf16_f32 v134, v156, v157\n"
"v_cvt_pk_bf16_f32 v135, v158, v159\n"
"s_mov_b32 s82, 0\n"
"v_mfma_f32_16x16x32_bf16 a[8:11], a[152:155], a[120:123], a[8:11]\n"
"buffer_store_dwordx4 v[68:71], v7, s[56:59], s82 offen offset:3072 sc1\n"
"v_mul_f32 v16, v160, v160\n"
"v_mul_f32 v17, v161, v161\n"
"v_mul_f32 v18, v162, v162\n"
"v_mul_f32 v19, v163, v163\n"
"v_mul_f32 v20, v164, v164\n"
"v_mul_f32 v21, v165, v165\n"
"v_mul_f32 v22, v166, v166\n"
"v_mul_f32 v23, v167, v167\n"
"v_fma_f32 v16, v16, s38, v8\n"
"v_mfma_f32_16x16x32_bf16 a[8:11], a[156:159], a[124:127], a[8:11]\n"
"v_fma_f32 v17, v17, s38, v8\n"
"v_fma_f32 v18, v18, s38, v8\n"
"v_fma_f32 v19, v19, s38, v8\n"
"v_fma_f32 v20, v20, s38, v8\n"
"v_fma_f32 v21, v21, s38, v8\n"
"v_fma_f32 v22, v22, s38, v8\n"
"v_fma_f32 v23, v23, s38, v8\n"
"v_mul_f32 v16, v16, v160\n"
"s_mov_b32 s82, 8192\n"
"v_mfma_f32_16x16x32_bf16 a[24:27], a[188:191], a[124:127], a[24:27]\n"
"buffer_store_dwordx4 v[96:99], v7, s[56:59], s82 offen offset:0 sc1\n"
"v_mul_f32 v17, v17, v161\n"
"v_mul_f32 v18, v18, v162\n"
"v_mul_f32 v19, v19, v163\n"
"v_mul_f32 v20, v20, v164\n"
"v_mul_f32 v21, v21, v165\n"
"v_mul_f32 v22, v22, v166\n"
"v_mul_f32 v23, v23, v167\n"
"v_exp_f32 v16, v16\n"
"v_exp_f32 v17, v17\n"
"v_mfma_f32_16x16x32_bf16 a[24:27], a[184:187], a[120:123], a[24:27]\n"
"v_exp_f32 v18, v18\n"
"v_exp_f32 v19, v19\n"
"v_exp_f32 v20, v20\n"
"v_exp_f32 v21, v21\n"
"v_exp_f32 v22, v22\n"
"v_exp_f32 v23, v23\n"
"v_mul_f32 v176, v176, v160\n"
"v_mul_f32 v177, v177, v161\n"
"v_mfma_f32_16x16x32_bf16 a[12:15], a[160:163], a[120:123], a[12:15]\n"
"v_mul_f32 v178, v178, v162\n"
"v_mul_f32 v179, v179, v163\n"
"v_mul_f32 v180, v180, v164\n"
"v_mul_f32 v181, v181, v165\n"
"v_mul_f32 v182, v182, v166\n"
"v_mul_f32 v183, v183, v167\n"
"v_add_f32 v16, 1.0, v16\n"
"v_add_f32 v17, 1.0, v17\n"
"v_add_f32 v18, 1.0, v18\n"
"s_mov_b32 s82, 8192\n"
"v_mfma_f32_16x16x32_bf16 a[12:15], a[164:167], a[124:127], a[12:15]\n"
"buffer_store_dwordx4 v[100:103], v7, s[56:59], s82 offen offset:1024 sc1\n"
"v_add_f32 v19, 1.0, v19\n"
"v_add_f32 v20, 1.0, v20\n"
"v_add_f32 v21, 1.0, v21\n"
"v_add_f32 v22, 1.0, v22\n"
"v_add_f32 v23, 1.0, v23\n"
"v_rcp_f32 v16, v16\n"
"v_rcp_f32 v17, v17\n"
"v_rcp_f32 v18, v18\n"
"v_mfma_f32_16x16x32_bf16 a[28:31], a[196:199], a[124:127], a[28:31]\n"
"v_rcp_f32 v19, v19\n"
"v_rcp_f32 v20, v20\n"
"v_rcp_f32 v21, v21\n"
"v_rcp_f32 v22, v22\n"
"v_rcp_f32 v23, v23\n"
"v_mul_f32 v176, v176, v16\n"
"v_mul_f32 v177, v177, v17\n"
"v_mul_f32 v178, v178, v18\n"
"v_mul_f32 v179, v179, v19\n"
"v_mfma_f32_16x16x32_bf16 a[28:31], a[192:195], a[120:123], a[28:31]\n"
"v_mul_f32 v180, v180, v20\n"
"v_mul_f32 v181, v181, v21\n"
"v_mul_f32 v182, v182, v22\n"
"v_mul_f32 v183, v183, v23\n"
"v_cvt_pk_bf16_f32 v160, v176, v177\n"
"v_cvt_pk_bf16_f32 v161, v178, v179\n"
"v_cvt_pk_bf16_f32 v162, v180, v181\n"
"v_cvt_pk_bf16_f32 v163, v182, v183\n"
"v_lshrrev_b32 v13, 4, v1\n"
"v_bfe_u32 v14, v1, 1, 3\n"
"v_xor_b32 v13, v13, v14\n"
"v_lshlrev_b32 v13, 4, v13\n"
"v_and_b32 v14, 15, v1\n"
"v_lshl_add_u32 v12, v14, 7, v13\n"
"s_lshr_b32 s14, s13, 1\n"
"s_lshl_b32 s14, s14, 14\n"
"v_add_u32 v12, s14, v12\n"
"v_xor_b32 v13, 64, v12\n"
"s_and_b32 s14, s13, 1\n"
"s_lshl_b32 s14, s14, 13\n"
"s_add_u32 s14, s14, 32768\n"
"v_lshlrev_b32 v14, 4, v1\n"
"v_add_u32 v14, s14, v14\n"
"s_waitcnt vmcnt(14)\n"
"s_barrier\n"
"ds_read_b128 a[32:35], v12 offset:0\n"
"v_mul_f32 v16, v168, v168\n"
"v_mul_f32 v17, v169, v169\n"
"v_mul_f32 v18, v170, v170\n"
"ds_read_b128 a[36:39], v13 offset:0\n"
"v_mul_f32 v19, v171, v171\n"
"v_mul_f32 v20, v172, v172\n"
"v_mul_f32 v21, v173, v173\n"
"ds_read_b128 a[96:99], v14 offset:0\n"
"v_mul_f32 v22, v174, v174\n"
"v_mul_f32 v23, v175, v175\n"
"ds_read_b128 a[100:103], v14 offset:1024\n"
"v_fma_f32 v16, v16, s38, v8\n"
"v_fma_f32 v17, v17, s38, v8\n"
"v_fma_f32 v18, v18, s38, v8\n"
"ds_read_b128 a[104:107], v14 offset:2048\n"
"v_fma_f32 v19, v19, s38, v8\n"
"v_fma_f32 v20, v20, s38, v8\n"
"ds_read_b128 a[108:111], v14 offset:3072\n"
"v_fma_f32 v21, v21, s38, v8\n"
"v_fma_f32 v22, v22, s38, v8\n"
"v_fma_f32 v23, v23, s38, v8\n"
"ds_read_b128 a[112:115], v14 offset:4096\n"
"v_mul_f32 v16, v16, v168\n"
"v_mul_f32 v17, v17, v169\n"
"ds_read_b128 a[116:119], v14 offset:5120\n"
"v_mul_f32 v18, v18, v170\n"
"v_mul_f32 v19, v19, v171\n"
"v_mul_f32 v20, v20, v172\n"
"ds_read_b128 a[120:123], v14 offset:6144\n"
"v_mul_f32 v21, v21, v173\n"
"v_mul_f32 v22, v22, v174\n"
"ds_read_b128 a[124:127], v14 offset:7168\n"
"v_mul_f32 v23, v23, v175\n"
"v_exp_f32 v16, v16\n"
"v_exp_f32 v17, v17\n"
"ds_read_b128 a[40:43], v12 offset:2048\n"
"v_exp_f32 v18, v18\n"
"v_exp_f32 v19, v19\n"
"ds_read_b128 a[44:47], v13 offset:2048\n"
"v_exp_f32 v20, v20\n"
"v_exp_f32 v21, v21\n"
"v_exp_f32 v22, v22\n"
"ds_read_b128 a[48:51], v12 offset:4096\n"
"v_exp_f32 v23, v23\n"
"v_mul_f32 v184, v184, v168\n"
"ds_read_b128 a[52:55], v13 offset:4096\n"
"v_mul_f32 v185, v185, v169\n"
"v_mul_f32 v186, v186, v170\n"
"v_mul_f32 v187, v187, v171\n"
"ds_read_b128 a[56:59], v12 offset:6144\n"
"v_mul_f32 v188, v188, v172\n"
"v_mul_f32 v189, v189, v173\n"
"ds_read_b128 a[60:63], v13 offset:6144\n"
"v_mul_f32 v190, v190, v174\n"
"v_mul_f32 v191, v191, v175\n"
"v_add_f32 v16, 1.0, v16\n"
"ds_read_b128 a[64:67], v12 offset:8192\n"
"v_add_f32 v17, 1.0, v17\n"
"v_add_f32 v18, 1.0, v18\n"
"ds_read_b128 a[68:71], v13 offset:8192\n"
"v_add_f32 v19, 1.0, v19\n"
"v_add_f32 v20, 1.0, v20\n"
"v_add_f32 v21, 1.0, v21\n"
"ds_read_b128 a[72:75], v12 offset:10240\n"
"v_add_f32 v22, 1.0, v22\n"
"v_add_f32 v23, 1.0, v23\n"
"ds_read_b128 a[76:79], v13 offset:10240\n"
"v_rcp_f32 v16, v16\n"
"v_rcp_f32 v17, v17\n"
"v_rcp_f32 v18, v18\n"
"ds_read_b128 a[80:83], v12 offset:12288\n"
"v_rcp_f32 v19, v19\n"
"v_rcp_f32 v20, v20\n"
"ds_read_b128 a[84:87], v13 offset:12288\n"
"v_rcp_f32 v21, v21\n"
"v_rcp_f32 v22, v22\n"
"v_rcp_f32 v23, v23\n"
"ds_read_b128 a[88:91], v12 offset:14336\n"
"v_mul_f32 v184, v184, v16\n"
"v_mul_f32 v185, v185, v17\n"
"ds_read_b128 a[92:95], v13 offset:14336\n"
"v_mul_f32 v186, v186, v18\n"
"v_mul_f32 v187, v187, v19\n"
"v_mul_f32 v188, v188, v20\n"
"s_mov_b32 s82, 8192\n"
"buffer_store_dwordx4 v[128:131], v7, s[56:59], s82 offen offset:2048 sc1\n"
"v_mul_f32 v189, v189, v21\n"
"v_mul_f32 v190, v190, v22\n"
"s_mov_b32 s82, 8192\n"
"buffer_store_dwordx4 v[132:135], v7, s[56:59], s82 offen offset:3072 sc1\n"
"v_mul_f32 v191, v191, v23\n"
"v_cvt_pk_bf16_f32 v164, v184, v185\n"
"v_cvt_pk_bf16_f32 v165, v186, v187\n"
"s_mov_b32 s82, 16384\n"
"buffer_store_dwordx4 v[160:163], v7, s[56:59], s82 offen offset:0 sc1\n"
"v_cvt_pk_bf16_f32 v166, v188, v189\n"
"v_cvt_pk_bf16_f32 v167, v190, v191\n"
"buffer_load_dword v11, v10, s[56:59], 0 offen offset:4 sc1\n"
"s_waitcnt lgkmcnt(14)\n"
"v_mfma_f32_16x16x32_bf16 a[224:227], a[96:99], a[32:35], 0\n"
"s_add_u32 s76, s76, s8\n"
"s_add_u32 s77, s77, s8\n"
"s_cmp_eq_u32 s7, 98304\n"
"v_mfma_f32_16x16x32_bf16 a[224:227], a[100:103], a[36:39], a[224:227]\n"
"s_cselect_b32 s8, 4294868992, s9\n"
"s_add_u32 s7, s7, s8\n"
"v_add_u32 v12, s8, v12\n"
"v_mfma_f32_16x16x32_bf16 a[228:231], a[108:111], a[36:39], 0\n"
"v_add_u32 v13, s8, v13\n"
"v_add_u32 v14, s8, v14\n"
"v_mfma_f32_16x16x32_bf16 a[228:231], a[104:107], a[32:35], a[228:231]\n"
"v_mul_f32 v16, v192, v192\n"
"v_mul_f32 v17, v193, v193\n"
"v_mfma_f32_16x16x32_bf16 a[232:235], a[112:115], a[32:35], 0\n"
"v_fma_f32 v16, v16, s38, v8\n"
"v_fma_f32 v17, v17, s38, v8\n"
"v_mfma_f32_16x16x32_bf16 a[232:235], a[116:119], a[36:39], a[232:235]\n"
"v_mul_f32 v16, v16, v192\n"
"v_mul_f32 v17, v17, v193\n"
"v_mfma_f32_16x16x32_bf16 a[236:239], a[124:127], a[36:39], 0\n"
"v_exp_f32 v16, v16\n"
"v_mfma_f32_16x16x32_bf16 a[236:239], a[120:123], a[32:35], a[236:239]\n"
"v_exp_f32 v17, v17\n"
"s_waitcnt lgkmcnt(13)\n"
"v_mfma_f32_16x16x32_bf16 a[252:255], a[120:123], a[40:43], 0\n"
"v_mul_f32 v208, v208, v192\n"
"v_mul_f32 v209, v209, v193\n"
"s_waitcnt lgkmcnt(12)\n"
"v_mfma_f32_16x16x32_bf16 a[252:255], a[124:127], a[44:47], a[252:255]\n"
"v_add_f32 v16, 1.0, v16\n"
"v_add_f32 v17, 1.0, v17\n"
"v_mfma_f32_16x16x32_bf16 a[248:251], a[116:119], a[44:47], 0\n"
"v_rcp_f32 v16, v16\n"
"v_mfma_f32_16x16x32_bf16 a[248:251], a[112:115], a[40:43], a[248:251]\n"
"v_rcp_f32 v17, v17\n"
"v_mfma_f32_16x16x32_bf16 a[244:247], a[104:107], a[40:43], 0\n"
"v_mul_f32 v208, v208, v16\n"
"v_mul_f32 v209, v209, v17\n"
"v_mfma_f32_16x16x32_bf16 a[244:247], a[108:111], a[44:47], a[244:247]\n"
"v_cvt_pk_bf16_f32 v192, v208, v209\n"
"v_mul_f32 v18, v194, v194\n"
"v_mfma_f32_16x16x32_bf16 a[240:243], a[100:103], a[44:47], 0\n"
"v_mul_f32 v19, v195, v195\n"
"v_fma_f32 v18, v18, s38, v8\n"
"v_mfma_f32_16x16x32_bf16 a[240:243], a[96:99], a[40:43], a[240:243]\n"
"s_waitcnt vmcnt(14) lgkmcnt(0)\n"
"s_barrier\n"
"v_fma_f32 v19, v19, s38, v8\n"
"v_mfma_f32_16x16x32_bf16 v[40:43], a[96:99], a[48:51], 0\n"
"ds_read_b128 a[128:131], v12 offset:0\n"
"v_mul_f32 v18, v18, v194\n"
"v_mfma_f32_16x16x32_bf16 v[40:43], a[100:103], a[52:55], v[40:43]\n"
"ds_read_b128 a[132:135], v13 offset:0\n"
"s_add_u32 m0, s76, 0\n"
"v_mfma_f32_16x16x32_bf16 v[44:47], a[108:111], a[52:55], 0\n"
"s_mov_b32 s82, 16384\n"
"buffer_store_dwordx4 v[164:167], v7, s[56:59], s82 offen offset:1024 sc1\n"
"v_mul_f32 v19, v19, v195\n"
"v_mul_f32 v20, v196, v196\n"
"v_mul_f32 v21, v197, v197\n"
"v_mfma_f32_16x16x32_bf16 v[44:47], a[104:107], a[48:51], v[44:47]\n"
"ds_read_b128 a[192:195], v14 offset:0\n"
"v_fma_f32 v20, v20, s38, v8\n"
"v_mfma_f32_16x16x32_bf16 v[48:51], a[112:115], a[48:51], 0\n"
"buffer_load_dwordx4 v2, s[24:27], s64 offen offset:0 lds\n"
"v_fma_f32 v21, v21, s38, v8\n"
"v_mfma_f32_16x16x32_bf16 v[48:51], a[116:119], a[52:55], v[48:51]\n"
"ds_read_b128 a[196:199], v14 offset:1024\n"
"v_mul_f32 v20, v20, v196\n"
"v_mfma_f32_16x16x32_bf16 v[52:55], a[124:127], a[52:55], 0\n"
"ds_read_b128 a[200:203], v14 offset:2048\n"
"v_mul_f32 v21, v21, v197\n"
"v_mfma_f32_16x16x32_bf16 v[52:55], a[120:123], a[48:51], v[52:55]\n"
"buffer_load_dwordx4 v3, s[24:27], s64 offen offset:1024 lds\n"
"v_mul_f32 v22, v198, v198\n"
"v_mfma_f32_16x16x32_bf16 v[76:79], a[120:123], a[56:59], 0\n"
"ds_read_b128 a[204:207], v14 offset:3072\n"
"v_mul_f32 v23, v199, v199\n"
"v_mfma_f32_16x16x32_bf16 v[76:79], a[124:127], a[60:63], v[76:79]\n"
"ds_read_b128 a[208:211], v14 offset:4096\n"
"v_fma_f32 v22, v22, s38, v8\n"
"v_mfma_f32_16x16x32_bf16 v[72:75], a[116:119], a[60:63], 0\n"
"buffer_load_dwordx4 v2, s[24:27], s65 offen offset:2048 lds\n"
"v_fma_f32 v23, v23, s38, v8\n"
"v_mfma_f32_16x16x32_bf16 v[72:75], a[112:115], a[56:59], v[72:75]\n"
"ds_read_b128 a[212:215], v14 offset:5120\n"
"v_mul_f32 v22, v22, v198\n"
"v_mfma_f32_16x16x32_bf16 v[60:63], a[104:107], a[56:59], 0\n"
"ds_read_b128 a[216:219], v14 offset:6144\n"
"v_mul_f32 v23, v23, v199\n"
"v_mfma_f32_16x16x32_bf16 v[60:63], a[108:111], a[60:63], v[60:63]\n"
"ds_read_b128 a[220:223], v14 offset:7168\n"
"v_mul_f32 v24, v200, v200\n"
"v_mfma_f32_16x16x32_bf16 v[56:59], a[100:103], a[60:63], 0\n"
"buffer_load_dwordx4 v3, s[24:27], s65 offen offset:3072 lds\n"
"v_mul_f32 v25, v201, v201\n"
"v_mfma_f32_16x16x32_bf16 v[56:59], a[96:99], a[56:59], v[56:59]\n"
"ds_read_b128 a[136:139], v12 offset:2048\n"
"s_add_u32 m0, s76, 4096\n"
"v_mfma_f32_16x16x32_bf16 v[80:83], a[96:99], a[64:67], 0\n"
"ds_read_b128 a[140:143], v13 offset:2048\n"
"v_fma_f32 v24, v24, s38, v8\n"
"v_mfma_f32_16x16x32_bf16 v[80:83], a[100:103], a[68:71], v[80:83]\n"
"buffer_load_dwordx4 v2, s[24:27], s66 offen offset:0 lds\n"
"v_fma_f32 v25, v25, s38, v8\n"
"v_mfma_f32_16x16x32_bf16 v[84:87], a[108:111], a[68:71], 0\n"
"ds_read_b128 a[144:147], v12 offset:4096\n"
"v_mul_f32 v24, v24, v200\n"
"v_mfma_f32_16x16x32_bf16 v[84:87], a[104:107], a[64:67], v[84:87]\n"
"ds_read_b128 a[148:151], v13 offset:4096\n"
"v_mul_f32 v25, v25, v201\n"
"v_mfma_f32_16x16x32_bf16 v[88:91], a[112:115], a[64:67], 0\n"
"buffer_load_dwordx4 v3, s[24:27], s66 offen offset:1024 lds\n"
"v_mul_f32 v26, v202, v202\n"
"v_mfma_f32_16x16x32_bf16 v[88:91], a[116:119], a[68:71], v[88:91]\n"
"ds_read_b128 a[152:155], v12 offset:6144\n"
"v_mul_f32 v27, v203, v203\n"
"v_mfma_f32_16x16x32_bf16 v[92:95], a[124:127], a[68:71], 0\n"
"ds_read_b128 a[156:159], v13 offset:6144\n"
"v_fma_f32 v26, v26, s38, v8\n"
"v_mfma_f32_16x16x32_bf16 v[92:95], a[120:123], a[64:67], v[92:95]\n"
"ds_read_b128 a[160:163], v12 offset:8192\n"
"v_fma_f32 v27, v27, s38, v8\n"
"v_mfma_f32_16x16x32_bf16 v[116:119], a[120:123], a[72:75], 0\n"
"buffer_load_dwordx4 v2, s[24:27], s67 offen offset:2048 lds\n"
"v_mul_f32 v26, v26, v202\n"
"v_mfma_f32_16x16x32_bf16 v[116:119], a[124:127], a[76:79], v[116:119]\n"
"ds_read_b128 a[164:167], v13 offset:8192\n"
"v_subrev_u32 v2, 1408, v2\n"
"v_mfma_f32_16x16x32_bf16 v[112:115], a[116:119], a[76:79], 0\n"
"ds_read_b128 a[168:171], v12 offset:10240\n"
"v_mul_f32 v27, v27, v203\n"
"v_mfma_f32_16x16x32_bf16 v[112:115], a[112:115], a[72:75], v[112:115]\n"
"buffer_load_dwordx4 v3, s[24:27], s67 offen offset:3072 lds\n"
"v_mul_f32 v28, v204, v204\n"
"v_mfma_f32_16x16x32_bf16 v[108:111], a[104:107], a[72:75], 0\n"
"ds_read_b128 a[172:175], v13 offset:10240\n"
"v_subrev_u32 v3, 1408, v3\n"
"v_mfma_f32_16x16x32_bf16 v[108:111], a[108:111], a[76:79], v[108:111]\n"
"ds_read_b128 a[176:179], v12 offset:12288\n"
"s_add_u32 m0, s77, 0\n"
"v_mfma_f32_16x16x32_bf16 v[104:107], a[100:103], a[76:79], 0\n"
"ds_read_b128 a[180:183], v13 offset:12288\n"
"v_mul_f32 v29, v205, v205\n"
"v_mfma_f32_16x16x32_bf16 v[104:107], a[96:99], a[72:75], v[104:107]\n"
"buffer_load_dwordx4 v4, s[56:59], 0 offen offset:0 sc1 lds\n"
"v_fma_f32 v28, v28, s38, v8\n"
"v_mfma_f32_16x16x32_bf16 v[120:123], a[96:99], a[80:83], 0\n"
"ds_read_b128 a[184:187], v12 offset:14336\n"
"v_fma_f32 v29, v29, s38, v8\n"
"v_mfma_f32_16x16x32_bf16 v[120:123], a[100:103], a[84:87], v[120:123]\n"
"ds_read_b128 a[188:191], v13 offset:14336\n"
"v_mul_f32 v28, v28, v204\n"
"v_mfma_f32_16x16x32_bf16 v[124:127], a[108:111], a[84:87], 0\n"
"buffer_load_dwordx4 v4, s[56:59], 0 offen offset:1024 sc1 lds\n"
"v_mul_f32 v29, v29, v205\n"
"v_mfma_f32_16x16x32_bf16 v[124:127], a[104:107], a[80:83], v[124:127]\n"
"buffer_load_dwordx4 v4, s[56:59], 0 offen offset:2048 sc1 lds\n"
"v_mul_f32 v30, v206, v206\n"
"v_mfma_f32_16x16x32_bf16 v[136:139], a[112:115], a[80:83], 0\n"
"v_exp_f32 v18, v18\n"
"v_mfma_f32_16x16x32_bf16 v[136:139], a[116:119], a[84:87], v[136:139]\n"
"v_exp_f32 v19, v19\n"
"v_mfma_f32_16x16x32_bf16 v[140:143], a[124:127], a[84:87], 0\n"
"v_exp_f32 v20, v20\n"
"v_mfma_f32_16x16x32_bf16 v[140:143], a[120:123], a[80:83], v[140:143]\n"
"buffer_load_dwordx4 v4, s[56:59], 0 offen offset:3072 sc1 lds\n"
"v_mul_f32 v210, v210, v194\n"
"v_mfma_f32_16x16x32_bf16 v[156:159], a[120:123], a[88:91], 0\n"
"v_subrev_u32 v4, 1441792, v4\n"
"s_add_u32 s76, s76, s8\n"
"v_mfma_f32_16x16x32_bf16 v[156:159], a[124:127], a[92:95], v[156:159]\n"
"s_add_u32 s77, s77, s8\n"
"s_cmp_eq_u32 s7, 98304\n"
"s_cselect_b32 s8, 4294868992, s9\n"
"v_mfma_f32_16x16x32_bf16 v[152:155], a[116:119], a[92:95], 0\n"
"s_add_u32 s7, s7, s8\n"
"v_add_u32 v12, s8, v12\n"
"v_mul_f32 v211, v211, v195\n"
"v_mfma_f32_16x16x32_bf16 v[152:155], a[112:115], a[88:91], v[152:155]\n"
"v_add_u32 v13, s8, v13\n"
"v_add_f32 v18, 1.0, v18\n"
"v_mfma_f32_16x16x32_bf16 v[148:151], a[104:107], a[88:91], 0\n"
"v_add_u32 v14, s8, v14\n"
"v_add_f32 v19, 1.0, v19\n"
"v_mfma_f32_16x16x32_bf16 v[148:151], a[108:111], a[92:95], v[148:151]\n"
"v_rcp_f32 v18, v18\n"
"v_mfma_f32_16x16x32_bf16 v[144:147], a[100:103], a[92:95], 0\n"
"v_rcp_f32 v19, v19\n"
"v_mfma_f32_16x16x32_bf16 v[144:147], a[96:99], a[88:91], v[144:147]\n"
"s_waitcnt lgkmcnt(14)\n"
"v_mfma_f32_16x16x32_bf16 a[224:227], a[192:195], a[128:131], a[224:227]\n"
"s_waitcnt vmcnt(23) lgkmcnt(0)\n"
"s_barrier\n"
"s_waitcnt vmcnt(13)\n"
"v_mfma_f32_16x16x32_bf16 a[224:227], a[196:199], a[132:135], a[224:227]\n"
"v_cmp_ne_u32 vcc, 1, v11\n"
"v_mul_f32 v210, v210, v18\n"
"v_mfma_f32_16x16x32_bf16 a[228:231], a[204:207], a[132:135], a[228:231]\n"
"s_nop 4\n"
"s_cbranch_vccnz PL19_%=\n"
"PL19d_%=:\n"
"v_mfma_f32_16x16x32_bf16 a[228:231], a[200:203], a[128:131], a[228:231]\n"
"ds_read_b128 a[32:35], v12 offset:0\n"
"s_add_u32 m0, s76, 0\n"
"v_mfma_f32_16x16x32_bf16 a[232:235], a[208:211], a[128:131], a[232:235]\n"
"ds_read_b128 a[36:39], v13 offset:0\n"
"v_mul_f32 v211, v211, v19\n"
"v_mfma_f32_16x16x32_bf16 a[232:235], a[212:215], a[132:135], a[232:235]\n"
"buffer_load_dwordx4 v2, s[24:27], s64 offen offset:0 lds\n"
"v_cvt_pk_bf16_f32 v193, v210, v211\n"
"v_mfma_f32_16x16x32_bf16 a[236:239], a[220:223], a[132:135], a[236:239]\n"
"ds_read_b128 a[96:99], v14 offset:0\n"
"v_mul_f32 v31, v207, v207\n"
"v_mfma_f32_16x16x32_bf16 a[236:239], a[216:219], a[128:131], a[236:239]\n"
"ds_read_b128 a[100:103], v14 offset:1024\n"
"v_fma_f32 v30, v30, s38, v8\n"
"v_mfma_f32_16x16x32_bf16 a[252:255], a[216:219], a[136:139], a[252:255]\n"
"buffer_load_dwordx4 v3, s[24:27], s64 offen offset:1024 lds\n"
"v_fma_f32 v31, v31, s38, v8\n"
"v_mfma_f32_16x16x32_bf16 a[252:255], a[220:223], a[140:143], a[252:255]\n"
"ds_read_b128 a[104:107], v14 offset:2048\n"
"v_mul_f32 v30, v30, v206\n"
"v_mfma_f32_16x16x32_bf16 a[248:251], a[212:215], a[140:143], a[248:251]\n"
"v_exp_f32 v21, v21\n"
"v_mfma_f32_16x16x32_bf16 a[248:251], a[208:211], a[136:139], a[248:251]\n"
"ds_read_b128 a[108:111], v14 offset:3072\n"
"v_mul_f32 v212, v212, v196\n"
"v_mfma_f32_16x16x32_bf16 a[244:247], a[200:203], a[136:139], a[244:247]\n"
"buffer_load_dwordx4 v2, s[24:27], s65 offen offset:2048 lds\n"
"v_mul_f32 v213, v213, v197\n"
"v_mfma_f32_16x16x32_bf16 a[244:247], a[204:207], a[140:143], a[244:247]\n"
"ds_read_b128 a[112:115], v14 offset:4096\n"
"v_add_f32 v20, 1.0, v20\n"
"v_mfma_f32_16x16x32_bf16 a[240:243], a[196:199], a[140:143], a[240:243]\n"
"v_exp_f32 v22, v22\n"
"v_mfma_f32_16x16x32_bf16 a[240:243], a[192:195], a[136:139], a[240:243]\n"
"ds_read_b128 a[116:119], v14 offset:5120\n"
"v_add_f32 v21, 1.0, v21\n"
"v_mfma_f32_16x16x32_bf16 v[40:43], a[192:195], a[144:147], v[40:43]\n"
"v_rcp_f32 v20, v20\n"
"v_mfma_f32_16x16x32_bf16 v[40:43], a[196:199], a[148:151], v[40:43]\n"
"ds_read_b128 a[120:123], v14 offset:6144\n"
"v_mul_f32 v31, v31, v207\n"
"v_mfma_f32_16x16x32_bf16 v[44:47], a[204:207], a[148:151], v[44:47]\n"
"buffer_load_dwordx4 v3, s[24:27], s65 offen offset:3072 lds\n"
"v_mul_f32 v16, v224, v224\n"
"v_mfma_f32_16x16x32_bf16 v[44:47], a[200:203], a[144:147], v[44:47]\n"
"ds_read_b128 a[124:127], v14 offset:7168\n"
"v_mul_f32 v17, v225, v225\n"
"v_mfma_f32_16x16x32_bf16 v[48:51], a[208:211], a[144:147], v[48:51]\n"
"s_add_u32 m0, s76, 4096\n"
"v_rcp_f32 v21, v21\n"
"v_mfma_f32_16x16x32_bf16 v[48:51], a[212:215], a[148:151], v[48:51]\n"
"ds_read_b128 a[40:43], v12 offset:2048\n"
"v_mul_f32 v212, v212, v20\n"
"v_mfma_f32_16x16x32_bf16 v[52:55], a[220:223], a[148:151], v[52:55]\n"
"buffer_load_dwordx4 v2, s[24:27], s66 offen offset:0 lds\n"
"v_mul_f32 v213, v213, v21\n"
"v_mfma_f32_16x16x32_bf16 v[52:55], a[216:219], a[144:147], v[52:55]\n"
"ds_read_b128 a[44:47], v13 offset:2048\n"
"v_cvt_pk_bf16_f32 v194, v212, v213\n"
"v_mfma_f32_16x16x32_bf16 v[76:79], a[216:219], a[152:155], v[76:79]\n"
"v_exp_f32 v23, v23\n"
"v_mfma_f32_16x16x32_bf16 v[76:79], a[220:223], a[156:159], v[76:79]\n"
"ds_read_b128 a[48:51], v12 offset:4096\n"
"v_mul_f32 v214, v214, v198\n"
"v_mfma_f32_16x16x32_bf16 v[72:75], a[212:215], a[156:159], v[72:75]\n"
"buffer_load_dwordx4 v3, s[24:27], s66 offen offset:1024 lds\n"
"v_mul_f32 v215, v215, v199\n"
"v_mfma_f32_16x16x32_bf16 v[72:75], a[208:211], a[152:155], v[72:75]\n"
"ds_read_b128 a[52:55], v13 offset:4096\n"
"v_add_f32 v22, 1.0, v22\n"
"v_mfma_f32_16x16x32_bf16 v[60:63], a[200:203], a[152:155], v[60:63]\n"
"v_exp_f32 v24, v24\n"
"v_mfma_f32_16x16x32_bf16 v[60:63], a[204:207], a[156:159], v[60:63]\n"
"ds_read_b128 a[56:59], v12 offset:6144\n"
"v_add_f32 v23, 1.0, v23\n"
"v_mfma_f32_16x16x32_bf16 v[56:59], a[196:199], a[156:159], v[56:59]\n"
"v_rcp_f32 v22, v22\n"
"v_mfma_f32_16x16x32_bf16 v[56:59], a[192:195], a[152:155], v[56:59]\n"
"ds_read_b128 a[60:63], v13 offset:6144\n"
"v_fma_f32 v16, v16, s38, v8\n"
"v_mfma_f32_16x16x32_bf16 v[80:83], a[192:195], a[160:163], v[80:83]\n"
"buffer_load_dwordx4 v2, s[24:27], s67 offen offset:2048 lds\n"
"v_add_u32 v2, 512, v2\n"
"v_mfma_f32_16x16x32_bf16 v[80:83], a[196:199], a[164:167], v[80:83]\n"
"ds_read_b128 a[64:67], v12 offset:8192\n"
"v_fma_f32 v17, v17, s38, v8\n"
"v_mfma_f32_16x16x32_bf16 v[84:87], a[204:207], a[164:167], v[84:87]\n"
"v_rcp_f32 v23, v23\n"
"v_mfma_f32_16x16x32_bf16 v[84:87], a[200:203], a[160:163], v[84:87]\n"
"ds_read_b128 a[68:71], v13 offset:8192\n"
"v_mul_f32 v214, v214, v22\n"
"v_mfma_f32_16x16x32_bf16 v[88:91], a[208:211], a[160:163], v[88:91]\n"
"buffer_load_dwordx4 v3, s[24:27], s67 offen offset:3072 lds\n"
"v_mul_f32 v215, v215, v23\n"
"v_mfma_f32_16x16x32_bf16 v[88:91], a[212:215], a[164:167], v[88:91]\n"
"ds_read_b128 a[72:75], v12 offset:10240\n"
"v_add_u32 v3, 512, v3\n"
"v_mfma_f32_16x16x32_bf16 v[92:95], a[220:223], a[164:167], v[92:95]\n"
"v_exp_f32 v25, v25\n"
"v_mfma_f32_16x16x32_bf16 v[92:95], a[216:219], a[160:163], v[92:95]\n"
"ds_read_b128 a[76:79], v13 offset:10240\n"
"s_add_u32 m0, s77, 0\n"
"v_mfma_f32_16x16x32_bf16 v[116:119], a[216:219], a[168:171], v[116:119]\n"
"buffer_load_dwordx4 v4, s[56:59], 0 offen offset:0 sc1 lds\n"
"v_cvt_pk_bf16_f32 v195, v214, v215\n"
"v_mfma_f32_16x16x32_bf16 v[116:119], a[220:223], a[172:175], v[116:119]\n"
"ds_read_b128 a[80:83], v12 offset:12288\n"
"v_mul_f32 v216, v216, v200\n"
"v_mfma_f32_16x16x32_bf16 v[112:115], a[212:215], a[172:175], v[112:115]\n"
"v_exp_f32 v26, v26\n"
"v_mfma_f32_16x16x32_bf16 v[112:115], a[208:211], a[168:171], v[112:115]\n"
"ds_read_b128 a[84:87], v13 offset:12288\n"
"v_mul_f32 v217, v217, v201\n"
"v_mfma_f32_16x16x32_bf16 v[108:111], a[200:203], a[168:171], v[108:111]\n"
"v_exp_f32 v27, v27\n"
"v_mfma_f32_16x16x32_bf16 v[108:111], a[204:207], a[172:175], v[108:111]\n"
"ds_read_b128 a[88:91], v12 offset:14336\n"
"v_add_f32 v24, 1.0, v24\n"
"v_mfma_f32_16x16x32_bf16 v[104:107], a[196:199], a[172:175], v[104:107]\n"
"buffer_load_dwordx4 v4, s[56:59], 0 offen offset:1024 sc1 lds\n"
"v_add_f32 v25, 1.0, v25\n"
"v_mfma_f32_16x16x32_bf16 v[104:107], a[192:195], a[168:171], v[104:107]\n"
"ds_read_b128 a[92:95], v13 offset:14336\n"
"v_mul_f32 v218, v218, v202\n"
"v_mfma_f32_16x16x32_bf16 v[120:123], a[192:195], a[176:179], v[120:123]\n"
"v_rcp_f32 v24, v24\n"
"v_mfma_f32_16x16x32_bf16 v[120:123], a[196:199], a[180:183], v[120:123]\n"
"v_rcp_f32 v25, v25\n"
"v_mfma_f32_16x16x32_bf16 v[124:127], a[204:207], a[180:183], v[124:127]\n"
"buffer_load_dwordx4 v4, s[56:59], 0 offen offset:2048 sc1 lds\n"
"v_mul_f32 v216, v216, v24\n"
"v_mfma_f32_16x16x32_bf16 v[124:127], a[200:203], a[176:179], v[124:127]\n"
"v_exp_f32 v28, v28\n"
"v_mfma_f32_16x16x32_bf16 v[136:139], a[208:211], a[176:179], v[136:139]\n"
"v_exp_f32 v29, v29\n"
"v_mfma_f32_16x16x32_bf16 v[136:139], a[212:215], a[180:183], v[136:139]\n"
"v_exp_f32 v30, v30\n"
"v_mfma_f32_16x16x32_bf16 v[140:143], a[220:223], a[180:183], v[140:143]\n"
"v_exp_f32 v31, v31\n"
"v_mfma_f32_16x16x32_bf16 v[140:143], a[216:219], a[176:179], v[140:143]\n"
"buffer_load_dwordx4 v4, s[56:59], 0 offen offset:3072 sc1 lds\n"
"v_mul_f32 v217, v217, v25\n"
"v_mfma_f32_16x16x32_bf16 v[156:159], a[216:219], a[184:187], v[156:159]\n"
"v_add_u32 v4, 524288, v4\n"
"s_add_u32 s76, s76, s8\n"
"v_mfma_f32_16x16x32_bf16 v[156:159], a[220:223], a[188:191], v[156:159]\n"
"s_add_u32 s77, s77, s8\n"
"s_cmp_eq_u32 s7, 98304\n"
"s_cselect_b32 s8, 4294868992, s9\n"
"v_mfma_f32_16x16x32_bf16 v[152:155], a[212:215], a[188:191], v[152:155]\n"
"s_add_u32 s7, s7, s8\n"
"v_add_u32 v12, s8, v12\n"
"v_cvt_pk_bf16_f32 v196, v216, v217\n"
"v_mfma_f32_16x16x32_bf16 v[152:155], a[208:211], a[184:187], v[152:155]\n"
"v_add_u32 v13, s8, v13\n"
"v_mul_f32 v219, v219, v203\n"
"v_mfma_f32_16x16x32_bf16 v[148:151], a[200:203], a[184:187], v[148:151]\n"
"v_add_u32 v14, s8, v14\n"
"v_add_f32 v26, 1.0, v26\n"
"v_mfma_f32_16x16x32_bf16 v[148:151], a[204:207], a[188:191], v[148:151]\n"
"v_add_f32 v27, 1.0, v27\n"
"v_mul_f32 v220, v220, v204\n"
"v_mfma_f32_16x16x32_bf16 v[144:147], a[196:199], a[188:191], v[144:147]\n"
"v_rcp_f32 v26, v26\n"
"v_mfma_f32_16x16x32_bf16 v[144:147], a[192:195], a[184:187], v[144:147]\n"
"s_waitcnt lgkmcnt(14)\n"
"v_mfma_f32_16x16x32_bf16 a[224:227], a[96:99], a[32:35], a[224:227]\n"
"s_waitcnt vmcnt(12) lgkmcnt(0)\n"
"s_barrier\n"
"v_mul_f32 v221, v221, v205\n"
"v_mfma_f32_16x16x32_bf16 a[224:227], a[100:103], a[36:39], a[224:227]\n"
"ds_read_b128 a[128:131], v12 offset:0\n"
"v_add_f32 v28, 1.0, v28\n"
"v_mfma_f32_16x16x32_bf16 a[228:231], a[108:111], a[36:39], a[228:231]\n"
"s_add_u32 m0, s76, 0\n"
"v_rcp_f32 v27, v27\n"
"v_mfma_f32_16x16x32_bf16 a[228:231], a[104:107], a[32:35], a[228:231]\n"
"s_mov_b32 s82, 16384\n"
"buffer_store_dwordx4 v[192:195], v7, s[56:59], s82 offen offset:2048 sc1\n"
"v_mul_f32 v218, v218, v26\n"
"v_mul_f32 v219, v219, v27\n"
"v_cvt_pk_bf16_f32 v197, v218, v219\n"
"v_mfma_f32_16x16x32_bf16 a[232:235], a[112:115], a[32:35], a[232:235]\n"
"ds_read_b128 a[132:135], v13 offset:0\n"
"v_add_f32 v29, 1.0, v29\n"
"v_mfma_f32_16x16x32_bf16 a[232:235], a[116:119], a[36:39], a[232:235]\n"
"buffer_load_dwordx4 v2, s[24:27], s64 offen offset:0 lds\n"
"v_mul_f32 v222, v222, v206\n"
"v_mfma_f32_16x16x32_bf16 a[236:239], a[124:127], a[36:39], a[236:239]\n"
"ds_read_b128 a[192:195], v14 offset:0\n"
"v_mul_f32 v223, v223, v207\n"
"v_mfma_f32_16x16x32_bf16 a[236:239], a[120:123], a[32:35], a[236:239]\n"
"ds_read_b128 a[196:199], v14 offset:1024\n"
"v_add_f32 v30, 1.0, v30\n"
"v_mfma_f32_16x16x32_bf16 a[252:255], a[120:123], a[40:43], a[252:255]\n"
"buffer_load_dwordx4 v3, s[24:27], s64 offen offset:1024 lds\n"
"v_add_f32 v31, 1.0, v31\n"
"v_mfma_f32_16x16x32_bf16 a[252:255], a[124:127], a[44:47], a[252:255]\n"
"ds_read_b128 a[200:203], v14 offset:2048\n"
"v_mul_f32 v16, v16, v224\n"
"v_mfma_f32_16x16x32_bf16 a[248:251], a[116:119], a[44:47], a[248:251]\n"
"v_rcp_f32 v28, v28\n"
"v_mfma_f32_16x16x32_bf16 a[248:251], a[112:115], a[40:43], a[248:251]\n"
"ds_read_b128 a[204:207], v14 offset:3072\n"
"v_mul_f32 v17, v17, v225\n"
"v_mfma_f32_16x16x32_bf16 a[244:247], a[104:107], a[40:43], a[244:247]\n"
"buffer_load_dwordx4 v2, s[24:27], s65 offen offset:2048 lds\n"
"v_mul_f32 v18, v226, v226\n"
"v_mfma_f32_16x16x32_bf16 a[244:247], a[108:111], a[44:47], a[244:247]\n"
"ds_read_b128 a[208:211], v14 offset:4096\n"
"v_mul_f32 v19, v227, v227\n"
"v_mfma_f32_16x16x32_bf16 a[240:243], a[100:103], a[44:47], a[240:243]\n"
"v_rcp_f32 v29, v29\n"
"v_mfma_f32_16x16x32_bf16 a[240:243], a[96:99], a[40:43], a[240:243]\n"
"ds_read_b128 a[212:215], v14 offset:5120\n"
"v_mul_f32 v220, v220, v28\n"
"v_mfma_f32_16x16x32_bf16 v[40:43], a[96:99], a[48:51], v[40:43]\n"
"v_rcp_f32 v30, v30\n"
"v_mfma_f32_16x16x32_bf16 v[40:43], a[100:103], a[52:55], v[40:43]\n"
"ds_read_b128 a[216:219], v14 offset:6144\n"
"v_mul_f32 v221, v221, v29\n"
"v_mfma_f32_16x16x32_bf16 v[44:47], a[108:111], a[52:55], v[44:47]\n"
"buffer_load_dwordx4 v3, s[24:27], s65 offen offset:3072 lds\n"
"v_cvt_pk_bf16_f32 v198, v220, v221\n"
"v_mfma_f32_16x16x32_bf16 v[44:47], a[104:107], a[48:51], v[44:47]\n"
"ds_read_b128 a[220:223], v14 offset:7168\n"
"v_fma_f32 v18, v18, s38, v8\n"
"v_mfma_f32_16x16x32_bf16 v[48:51], a[112:115], a[48:51], v[48:51]\n"
"s_add_u32 m0, s76, 4096\n"
"v_rcp_f32 v31, v31\n"
"v_mfma_f32_16x16x32_bf16 v[48:51], a[116:119], a[52:55], v[48:51]\n"
"ds_read_b128 a[136:139], v12 offset:2048\n"
"v_mul_f32 v222, v222, v30\n"
"v_mfma_f32_16x16x32_bf16 v[52:55], a[124:127], a[52:55], v[52:55]\n"
"buffer_load_dwordx4 v2, s[24:27], s66 offen offset:0 lds\n"
"v_mul_f32 v223, v223, v31\n"
"v_mfma_f32_16x16x32_bf16 v[52:55], a[120:123], a[48:51], v[52:55]\n"
"ds_read_b128 a[140:143], v13 offset:2048\n"
"v_cvt_pk_bf16_f32 v199, v222, v223\n"
"v_mfma_f32_16x16x32_bf16 v[76:79], a[120:123], a[56:59], v[76:79]\n"
"v_exp_f32 v16, v16\n"
"v_mfma_f32_16x16x32_bf16 v[76:79], a[124:127], a[60:63], v[76:79]\n"
"ds_read_b128 a[144:147], v12 offset:4096\n"
"v_fma_f32 v19, v19, s38, v8\n"
"v_mfma_f32_16x16x32_bf16 v[72:75], a[116:119], a[60:63], v[72:75]\n"
"buffer_load_dwordx4 v3, s[24:27], s66 offen offset:1024 lds\n"
"v_mul_f32 v18, v18, v226\n"
"v_mfma_f32_16x16x32_bf16 v[72:75], a[112:115], a[56:59], v[72:75]\n"
"ds_read_b128 a[148:151], v13 offset:4096\n"
"v_mul_f32 v19, v19, v227\n"
"v_mfma_f32_16x16x32_bf16 v[60:63], a[104:107], a[56:59], v[60:63]\n"
"v_exp_f32 v17, v17\n"
"v_mfma_f32_16x16x32_bf16 v[60:63], a[108:111], a[60:63], v[60:63]\n"
"ds_read_b128 a[152:155], v12 offset:6144\n"
"v_mul_f32 v240, v240, v224\n"
"v_mfma_f32_16x16x32_bf16 v[56:59], a[100:103], a[60:63], v[56:59]\n"
"v_exp_f32 v18, v18\n"
"v_mfma_f32_16x16x32_bf16 v[56:59], a[96:99], a[56:59], v[56:59]\n"
"ds_read_b128 a[156:159], v13 offset:6144\n"
"v_mul_f32 v241, v241, v225\n"
"v_mfma_f32_16x16x32_bf16 v[80:83], a[96:99], a[64:67], v[80:83]\n"
"buffer_load_dwordx4 v2, s[24:27], s67 offen offset:2048 lds\n"
"v_add_u32 v2, 512, v2\n"
"v_mfma_f32_16x16x32_bf16 v[80:83], a[100:103], a[68:71], v[80:83]\n"
"ds_read_b128 a[160:163], v12 offset:8192\n"
"v_add_f32 v16, 1.0, v16\n"
"v_mfma_f32_16x16x32_bf16 v[84:87], a[108:111], a[68:71], v[84:87]\n"
"v_exp_f32 v19, v19\n"
"v_mfma_f32_16x16x32_bf16 v[84:87], a[104:107], a[64:67], v[84:87]\n"
"ds_read_b128 a[164:167], v13 offset:8192\n"
"v_add_f32 v17, 1.0, v17\n"
"v_mfma_f32_16x16x32_bf16 v[88:91], a[112:115], a[64:67], v[88:91]\n"
"buffer_load_dwordx4 v3, s[24:27], s67 offen offset:3072 lds\n"
"v_mul_f32 v242, v242, v226\n"
"v_mfma_f32_16x16x32_bf16 v[88:91], a[116:119], a[68:71], v[88:91]\n"
"ds_read_b128 a[168:171], v12 offset:10240\n"
"v_add_u32 v3, 512, v3\n"
"v_mfma_f32_16x16x32_bf16 v[92:95], a[124:127], a[68:71], v[92:95]\n"
"v_rcp_f32 v16, v16\n"
"v_mfma_f32_16x16x32_bf16 v[92:95], a[120:123], a[64:67], v[92:95]\n"
"ds_read_b128 a[172:175], v13 offset:10240\n"
"s_add_u32 m0, s77, 0\n"
"v_mfma_f32_16x16x32_bf16 v[116:119], a[120:123], a[72:75], v[116:119]\n"
"buffer_load_dwordx4 v4, s[56:59], 0 offen offset:0 sc1 lds\n"
"v_mul_f32 v243, v243, v227\n"
"v_mfma_f32_16x16x32_bf16 v[116:119], a[124:127], a[76:79], v[116:119]\n"
"ds_read_b128 a[176:179], v12 offset:12288\n"
"v_add_f32 v18, 1.0, v18\n"
"v_mfma_f32_16x16x32_bf16 v[112:115], a[116:119], a[76:79], v[112:115]\n"
"v_rcp_f32 v17, v17\n"
"v_mfma_f32_16x16x32_bf16 v[112:115], a[112:115], a[72:75], v[112:115]\n"
"ds_read_b128 a[180:183], v13 offset:12288\n"
"v_mul_f32 v240, v240, v16\n"
"v_mfma_f32_16x16x32_bf16 v[108:111], a[104:107], a[72:75], v[108:111]\n"
"v_mul_f32 v241, v241, v17\n"
"v_cvt_pk_bf16_f32 v224, v240, v241\n"
"v_mfma_f32_16x16x32_bf16 v[108:111], a[108:111], a[76:79], v[108:111]\n"
"ds_read_b128 a[184:187], v12 offset:14336\n"
"v_add_f32 v19, 1.0, v19\n"
"v_mfma_f32_16x16x32_bf16 v[104:107], a[100:103], a[76:79], v[104:107]\n"
"buffer_load_dwordx4 v4, s[56:59], 0 offen offset:1024 sc1 lds\n"
"v_mul_f32 v20, v228, v228\n"
"v_mfma_f32_16x16x32_bf16 v[104:107], a[96:99], a[72:75], v[104:107]\n"
"ds_read_b128 a[188:191], v13 offset:14336\n"
"v_mul_f32 v21, v229, v229\n"
"v_mfma_f32_16x16x32_bf16 v[120:123], a[96:99], a[80:83], v[120:123]\n"
"v_rcp_f32 v18, v18\n"
"v_mfma_f32_16x16x32_bf16 v[120:123], a[100:103], a[84:87], v[120:123]\n"
"v_rcp_f32 v19, v19\n"
"v_mfma_f32_16x16x32_bf16 v[124:127], a[108:111], a[84:87], v[124:127]\n"
"buffer_load_dwordx4 v4, s[56:59], 0 offen offset:2048 sc1 lds\n"
"v_mul_f32 v242, v242, v18\n"
"v_mfma_f32_16x16x32_bf16 v[124:127], a[104:107], a[80:83], v[124:127]\n"
"v_mul_f32 v243, v243, v19\n"
"v_cvt_pk_bf16_f32 v225, v242, v243\n"
"v_mfma_f32_16x16x32_bf16 v[136:139], a[112:115], a[80:83], v[136:139]\n"
"v_fma_f32 v20, v20, s38, v8\n"
"v_fma_f32 v21, v21, s38, v8\n"
"v_mfma_f32_16x16x32_bf16 v[136:139], a[116:119], a[84:87], v[136:139]\n"
"v_mul_f32 v20, v20, v228\n"
"v_mul_f32 v21, v21, v229\n"
"v_mfma_f32_16x16x32_bf16 v[140:143], a[124:127], a[84:87], v[140:143]\n"
"v_exp_f32 v20, v20\n"
"v_mfma_f32_16x16x32_bf16 v[140:143], a[120:123], a[80:83], v[140:143]\n"
"buffer_load_dwordx4 v4, s[56:59], 0 offen offset:3072 sc1 lds\n"
"v_mul_f32 v22, v230, v230\n"
"v_mfma_f32_16x16x32_bf16 v[156:159], a[120:123], a[88:91], v[156:159]\n"
"v_add_u32 v4, 524288, v4\n"
"s_add_u32 s76, s76, s8\n"
"v_mfma_f32_16x16x32_bf16 v[156:159], a[124:127], a[92:95], v[156:159]\n"
"s_add_u32 s77, s77, s8\n"
"s_cmp_eq_u32 s7, 98304\n"
"s_cselect_b32 s8, 4294868992, s9\n"
"v_mfma_f32_16x16x32_bf16 v[152:155], a[116:119], a[92:95], v[152:155]\n"
"s_add_u32 s7, s7, s8\n"
"v_add_u32 v12, s8, v12\n"
"v_mul_f32 v23, v231, v231\n"
"v_mfma_f32_16x16x32_bf16 v[152:155], a[112:115], a[88:91], v[152:155]\n"
"v_add_u32 v13, s8, v13\n"
"v_fma_f32 v22, v22, s38, v8\n"
"v_mfma_f32_16x16x32_bf16 v[148:151], a[104:107], a[88:91], v[148:151]\n"
"v_add_u32 v14, s8, v14\n"
"v_fma_f32 v23, v23, s38, v8\n"
"v_mfma_f32_16x16x32_bf16 v[148:151], a[108:111], a[92:95], v[148:151]\n"
"v_exp_f32 v21, v21\n"
"v_mfma_f32_16x16x32_bf16 v[144:147], a[100:103], a[92:95], v[144:147]\n"
"v_mul_f32 v244, v244, v228\n"
"v_mul_f32 v245, v245, v229\n"
"v_mfma_f32_16x16x32_bf16 v[144:147], a[96:99], a[88:91], v[144:147]\n"
"s_waitcnt lgkmcnt(14)\n"
"v_mfma_f32_16x16x32_bf16 a[224:227], a[192:195], a[128:131], a[224:227]\n"
"s_waitcnt vmcnt(13) lgkmcnt(0)\n"
"s_barrier\n"
"v_add_f32 v20, 1.0, v20\n"
"v_mfma_f32_16x16x32_bf16 a[224:227], a[196:199], a[132:135], a[224:227]\n"
"ds_read_b128 a[32:35], v12 offset:0\n"
"v_add_f32 v21, 1.0, v21\n"
"v_mfma_f32_16x16x32_bf16 a[228:231], a[204:207], a[132:135], a[228:231]\n"
"s_add_u32 m0, s76, 0\n"
"v_rcp_f32 v20, v20\n"
"v_mfma_f32_16x16x32_bf16 a[228:231], a[200:203], a[128:131], a[228:231]\n"
"s_mov_b32 s82, 16384\n"
"buffer_store_dwordx4 v[196:199], v7, s[56:59], s82 offen offset:3072 sc1\n"
"v_mul_f32 v22, v22, v230\n"
"v_mul_f32 v23, v23, v231\n"
"v_mul_f32 v24, v232, v232\n"
"v_mfma_f32_16x16x32_bf16 a[232:235], a[208:211], a[128:131], a[232:235]\n"
"ds_read_b128 a[36:39], v13 offset:0\n"
"v_mul_f32 v25, v233, v233\n"
"v_mfma_f32_16x16x32_bf16 a[232:235], a[212:215], a[132:135], a[232:235]\n"
"buffer_load_dwordx4 v2, s[24:27], s64 offen offset:0 lds\n"
"v_fma_f32 v24, v24, s38, v8\n"
"v_mfma_f32_16x16x32_bf16 a[236:239], a[220:223], a[132:135], a[236:239]\n"
"ds_read_b128 a[96:99], v14 offset:0\n"
"v_fma_f32 v25, v25, s38, v8\n"
"v_mfma_f32_16x16x32_bf16 a[236:239], a[216:219], a[128:131], a[236:239]\n"
"ds_read_b128 a[100:103], v14 offset:1024\n"
"v_mul_f32 v24, v24, v232\n"
"v_mfma_f32_16x16x32_bf16 a[252:255], a[216:219], a[136:139], a[252:255]\n"
"buffer_load_dwordx4 v3, s[24:27], s64 offen offset:1024 lds\n"
"v_mul_f32 v25, v25, v233\n"
"v_mfma_f32_16x16x32_bf16 a[252:255], a[220:223], a[140:143], a[252:255]\n"
"ds_read_b128 a[104:107], v14 offset:2048\n"
"v_mul_f32 v26, v234, v234\n"
"v_mfma_f32_16x16x32_bf16 a[248:251], a[212:215], a[140:143], a[248:251]\n"
"v_rcp_f32 v21, v21\n"
"v_mfma_f32_16x16x32_bf16 a[248:251], a[208:211], a[136:139], a[248:251]\n"
"ds_read_b128 a[108:111], v14 offset:3072\n"
"v_mul_f32 v244, v244, v20\n"
"v_mfma_f32_16x16x32_bf16 a[244:247], a[200:203], a[136:139], a[244:247]\n"
"buffer_load_dwordx4 v2, s[24:27], s65 offen offset:2048 lds\n"
"v_mul_f32 v245, v245, v21\n"
"v_mfma_f32_16x16x32_bf16 a[244:247], a[204:207], a[140:143], a[244:247]\n"
"ds_read_b128 a[112:115], v14 offset:4096\n"
"v_cvt_pk_bf16_f32 v226, v244, v245\n"
"v_mfma_f32_16x16x32_bf16 a[240:243], a[196:199], a[140:143], a[240:243]\n"
"v_exp_f32 v22, v22\n"
"v_mfma_f32_16x16x32_bf16 a[240:243], a[192:195], a[136:139], a[240:243]\n"
"ds_read_b128 a[116:119], v14 offset:5120\n"
"v_mul_f32 v27, v235, v235\n"
"v_mfma_f32_16x16x32_bf16 v[40:43], a[192:195], a[144:147], v[40:43]\n"
"v_exp_f32 v23, v23\n"
"v_mfma_f32_16x16x32_bf16 v[40:43], a[196:199], a[148:151], v[40:43]\n"
"ds_read_b128 a[120:123], v14 offset:6144\n"
"v_mul_f32 v246, v246, v230\n"
"v_mfma_f32_16x16x32_bf16 v[44:47], a[204:207], a[148:151], v[44:47]\n"
"buffer_load_dwordx4 v3, s[24:27], s65 offen offset:3072 lds\n"
"v_mul_f32 v247, v247, v231\n"
"v_mfma_f32_16x16x32_bf16 v[44:47], a[200:203], a[144:147], v[44:47]\n"
"ds_read_b128 a[124:127], v14 offset:7168\n"
"v_add_f32 v22, 1.0, v22\n"
"v_mfma_f32_16x16x32_bf16 v[48:51], a[208:211], a[144:147], v[48:51]\n"
"s_add_u32 m0, s76, 4096\n"
"v_exp_f32 v24, v24\n"
"v_mfma_f32_16x16x32_bf16 v[48:51], a[212:215], a[148:151], v[48:51]\n"
"ds_read_b128 a[40:43], v12 offset:2048\n"
"v_add_f32 v23, 1.0, v23\n"
"v_mfma_f32_16x16x32_bf16 v[52:55], a[220:223], a[148:151], v[52:55]\n"
"buffer_load_dwordx4 v2, s[24:27], s66 offen offset:0 lds\n"
"v_fma_f32 v26, v26, s38, v8\n"
"v_mfma_f32_16x16x32_bf16 v[52:55], a[216:219], a[144:147], v[52:55]\n"
"ds_read_b128 a[44:47], v13 offset:2048\n"
"v_fma_f32 v27, v27, s38, v8\n"
"v_mfma_f32_16x16x32_bf16 v[76:79], a[216:219], a[152:155], v[76:79]\n"
"v_rcp_f32 v22, v22\n"
"v_mfma_f32_16x16x32_bf16 v[76:79], a[220:223], a[156:159], v[76:79]\n"
"ds_read_b128 a[48:51], v12 offset:4096\n"
"v_mul_f32 v26, v26, v234\n"
"v_mfma_f32_16x16x32_bf16 v[72:75], a[212:215], a[156:159], v[72:75]\n"
"buffer_load_dwordx4 v3, s[24:27], s66 offen offset:1024 lds\n"
"v_mul_f32 v27, v27, v235\n"
"v_mfma_f32_16x16x32_bf16 v[72:75], a[208:211], a[152:155], v[72:75]\n"
"ds_read_b128 a[52:55], v13 offset:4096\n"
"v_mul_f32 v28, v236, v236\n"
"v_mfma_f32_16x16x32_bf16 v[60:63], a[200:203], a[152:155], v[60:63]\n"
"v_rcp_f32 v23, v23\n"
"v_mfma_f32_16x16x32_bf16 v[60:63], a[204:207], a[156:159], v[60:63]\n"
"ds_read_b128 a[56:59], v12 offset:6144\n"
"v_mul_f32 v246, v246, v22\n"
"v_mfma_f32_16x16x32_bf16 v[56:59], a[196:199], a[156:159], v[56:59]\n"
"v_exp_f32 v25, v25\n"
"v_mfma_f32_16x16x32_bf16 v[56:59], a[192:195], a[152:155], v[56:59]\n"
"ds_read_b128 a[60:63], v13 offset:6144\n"
"v_mul_f32 v247, v247, v23\n"
"v_mfma_f32_16x16x32_bf16 v[80:83], a[192:195], a[160:163], v[80:83]\n"
"buffer_load_dwordx4 v2, s[24:27], s67 offen offset:2048 lds\n"
"v_add_u32 v2, 512, v2\n"
"v_mfma_f32_16x16x32_bf16 v[80:83], a[196:199], a[164:167], v[80:83]\n"
"ds_read_b128 a[64:67], v12 offset:8192\n"
"v_cvt_pk_bf16_f32 v227, v246, v247\n"
"v_mfma_f32_16x16x32_bf16 v[84:87], a[204:207], a[164:167], v[84:87]\n"
"v_exp_f32 v26, v26\n"
"v_mfma_f32_16x16x32_bf16 v[84:87], a[200:203], a[160:163], v[84:87]\n"
"ds_read_b128 a[68:71], v13 offset:8192\n"
"v_mul_f32 v248, v248, v232\n"
"v_mfma_f32_16x16x32_bf16 v[88:91], a[208:211], a[160:163], v[88:91]\n"
"buffer_load_dwordx4 v3, s[24:27], s67 offen offset:3072 lds\n"
"v_mul_f32 v249, v249, v233\n"
"v_mfma_f32_16x16x32_bf16 v[88:91], a[212:215], a[164:167], v[88:91]\n"
"ds_read_b128 a[72:75], v12 offset:10240\n"
"v_add_u32 v3, 512, v3\n"
"v_mfma_f32_16x16x32_bf16 v[92:95], a[220:223], a[164:167], v[92:95]\n"
"v_exp_f32 v27, v27\n"
"v_mfma_f32_16x16x32_bf16 v[92:95], a[216:219], a[160:163], v[92:95]\n"
"ds_read_b128 a[76:79], v13 offset:10240\n"
"s_add_u32 m0, s77, 0\n"
"v_mfma_f32_16x16x32_bf16 v[116:119], a[216:219], a[168:171], v[116:119]\n"
"buffer_load_dwordx4 v4, s[56:59], 0 offen offset:0 sc1 lds\n"
"v_add_f32 v24, 1.0, v24\n"
"v_mfma_f32_16x16x32_bf16 v[116:119], a[220:223], a[172:175], v[116:119]\n"
"ds_read_b128 a[80:83], v12 offset:12288\n"
"v_add_f32 v25, 1.0, v25\n"
"v_mfma_f32_16x16x32_bf16 v[112:115], a[212:215], a[172:175], v[112:115]\n"
"v_rcp_f32 v24, v24\n"
"v_mfma_f32_16x16x32_bf16 v[112:115], a[208:211], a[168:171], v[112:115]\n"
"ds_read_b128 a[84:87], v13 offset:12288\n"
"v_mul_f32 v250, v250, v234\n"
"v_mfma_f32_16x16x32_bf16 v[108:111], a[200:203], a[168:171], v[108:111]\n"
"v_rcp_f32 v25, v25\n"
"v_mfma_f32_16x16x32_bf16 v[108:111], a[204:207], a[172:175], v[108:111]\n"
"ds_read_b128 a[88:91], v12 offset:14336\n"
"v_mul_f32 v248, v248, v24\n"
"v_mfma_f32_16x16x32_bf16 v[104:107], a[196:199], a[172:175], v[104:107]\n"
"buffer_load_dwordx4 v4, s[56:59], 0 offen offset:1024 sc1 lds\n"
"v_mul_f32 v249, v249, v25\n"
"v_mfma_f32_16x16x32_bf16 v[104:107], a[192:195], a[168:171], v[104:107]\n"
"ds_read_b128 a[92:95], v13 offset:14336\n"
"v_cvt_pk_bf16_f32 v228, v248, v249\n"
"v_mfma_f32_16x16x32_bf16 v[120:123], a[192:195], a[176:179], v[120:123]\n"
"v_mul_f32 v251, v251, v235\n"
"v_add_f32 v26, 1.0, v26\n"
"v_mfma_f32_16x16x32_bf16 v[120:123], a[196:199], a[180:183], v[120:123]\n"
"v_add_f32 v27, 1.0, v27\n"
"v_mul_f32 v29, v237, v237\n"
"v_mfma_f32_16x16x32_bf16 v[124:127], a[204:207], a[180:183], v[124:127]\n"
"buffer_load_dwordx4 v4, s[56:59], 0 offen offset:2048 sc1 lds\n"
"v_fma_f32 v28, v28, s38, v8\n"
"v_mfma_f32_16x16x32_bf16 v[124:127], a[200:203], a[176:179], v[124:127]\n"
"v_rcp_f32 v26, v26\n"
"v_mfma_f32_16x16x32_bf16 v[136:139], a[208:211], a[176:179], v[136:139]\n"
"v_rcp_f32 v27, v27\n"
"v_mfma_f32_16x16x32_bf16 v[136:139], a[212:215], a[180:183], v[136:139]\n"
"v_mul_f32 v250, v250, v26\n"
"v_mul_f32 v251, v251, v27\n"
"v_mfma_f32_16x16x32_bf16 v[140:143], a[220:223], a[180:183], v[140:143]\n"
"v_cvt_pk_bf16_f32 v229, v250, v251\n"
"v_fma_f32 v29, v29, s38, v8\n"
"v_mfma_f32_16x16x32_bf16 v[140:143], a[216:219], a[176:179], v[140:143]\n"
"buffer_load_dwordx4 v4, s[56:59], 0 offen offset:3072 sc1 lds\n"
"v_mul_f32 v28, v28, v236\n"
"v_mfma_f32_16x16x32_bf16 v[156:159], a[216:219], a[184:187], v[156:159]\n"
"v_add_u32 v4, 524288, v4\n"
"s_add_u32 s76, s76, s8\n"
"v_mfma_f32_16x16x32_bf16 v[156:159], a[220:223], a[188:191], v[156:159]\n"
"s_add_u32 s77, s77, s8\n"
"s_cmp_eq_u32 s7, 98304\n"
"s_cselect_b32 s8, 4294868992, s9\n"
"v_mfma_f32_16x16x32_bf16 v[152:155], a[212:215], a[188:191], v[152:155]\n"
"s_add_u32 s7, s7, s8\n"
"v_add_u32 v12, s8, v12\n"
"v_mul_f32 v29, v29, v237\n"
"v_mfma_f32_16x16x32_bf16 v[152:155], a[208:211], a[184:187], v[152:155]\n"
"v_add_u32 v13, s8, v13\n"
"v_mul_f32 v30, v238, v238\n"
"v_mfma_f32_16x16x32_bf16 v[148:151], a[200:203], a[184:187], v[148:151]\n"
"v_add_u32 v14, s8, v14\n"
"v_mul_f32 v31, v239, v239\n"
"v_mfma_f32_16x16x32_bf16 v[148:151], a[204:207], a[188:191], v[148:151]\n"
"v_exp_f32 v28, v28\n"
"v_mfma_f32_16x16x32_bf16 v[144:147], a[196:199], a[188:191], v[144:147]\n"
"v_exp_f32 v29, v29\n"
"v_mfma_f32_16x16x32_bf16 v[144:147], a[192:195], a[184:187], v[144:147]\n"
"s_waitcnt lgkmcnt(14)\n"
"v_mfma_f32_16x16x32_bf16 a[224:227], a[96:99], a[32:35], a[224:227]\n"
"s_waitcnt vmcnt(13) lgkmcnt(0)\n"
"s_barrier\n"
"v_mul_f32 v252, v252, v236\n"
"v_mfma_f32_16x16x32_bf16 a[224:227], a[100:103], a[36:39], a[224:227]\n"
"buffer_load_dword v11, v10, s[56:59], 0 offen offset:8 sc1\n"
"v_mul_f32 v253, v253, v237\n"
"v_mfma_f32_16x16x32_bf16 a[228:231], a[108:111], a[36:39], a[228:231]\n"
"ds_read_b128 a[128:131], v12 offset:0\n"
"s_add_u32 m0, s76, 0\n"
"v_mfma_f32_16x16x32_bf16 a[228:231], a[104:107], a[32:35], a[228:231]\n"
"s_mov_b32 s82, 24576\n"
"buffer_store_dwordx4 v[224:227], v7, s[56:59], s82 offen offset:0 sc1\n"
"v_add_f32 v28, 1.0, v28\n"
"v_add_f32 v29, 1.0, v29\n"
"v_fma_f32 v30, v30, s38, v8\n"
"v_mfma_f32_16x16x32_bf16 a[232:235], a[112:115], a[32:35], a[232:235]\n"
"ds_read_b128 a[132:135], v13 offset:0\n"
"v_fma_f32 v31, v31, s38, v8\n"
"v_mfma_f32_16x16x32_bf16 a[232:235], a[116:119], a[36:39], a[232:235]\n"
"buffer_load_dwordx4 v2, s[24:27], s64 offen offset:0 lds\n"
"v_mul_f32 v30, v30, v238\n"
"v_mfma_f32_16x16x32_bf16 a[236:239], a[124:127], a[36:39], a[236:239]\n"
"ds_read_b128 a[192:195], v14 offset:0\n"
"v_mul_f32 v31, v31, v239\n"
"v_mfma_f32_16x16x32_bf16 a[236:239], a[120:123], a[32:35], a[236:239]\n"
"ds_read_b128 a[196:199], v14 offset:1024\n"
"v_accvgpr_read_b32 v176, a0\n"
"v_mfma_f32_16x16x32_bf16 a[252:255], a[120:123], a[40:43], a[252:255]\n"
"buffer_load_dwordx4 v3, s[24:27], s64 offen offset:1024 lds\n"
"v_accvgpr_read_b32 v177, a1\n"
"v_mfma_f32_16x16x32_bf16 a[252:255], a[124:127], a[44:47], a[252:255]\n"
"ds_read_b128 a[200:203], v14 offset:2048\n"
"v_accvgpr_read_b32 v208, a16\n"
"v_mfma_f32_16x16x32_bf16 a[248:251], a[116:119], a[44:47], a[248:251]\n"
"v_rcp_f32 v28, v28\n"
"v_mfma_f32_16x16x32_bf16 a[248:251], a[112:115], a[40:43], a[248:251]\n"
"ds_read_b128 a[204:207], v14 offset:3072\n"
"v_accvgpr_read_b32 v209, a17\n"
"v_mfma_f32_16x16x32_bf16 a[244:247], a[104:107], a[40:43], a[244:247]\n"
"buffer_load_dwordx4 v2, s[24:27], s65 offen offset:2048 lds\n"
"v_mul_f32 v16, v176, v176\n"
"v_mfma_f32_16x16x32_bf16 a[244:247], a[108:111], a[44:47], a[244:247]\n"
"ds_read_b128 a[208:211], v14 offset:4096\n"
"v_mul_f32 v17, v177, v177\n"
"v_mfma_f32_16x16x32_bf16 a[240:243], a[100:103], a[44:47], a[240:243]\n"
"v_rcp_f32 v29, v29\n"
"v_mfma_f32_16x16x32_bf16 a[240:243], a[96:99], a[40:43], a[240:243]\n"
"ds_read_b128 a[212:215], v14 offset:5120\n"
"v_mul_f32 v252, v252, v28\n"
"v_mfma_f32_16x16x32_bf16 v[40:43], a[96:99], a[48:51], v[40:43]\n"
"v_exp_f32 v30, v30\n"
"v_mfma_f32_16x16x32_bf16 v[40:43], a[100:103], a[52:55], v[40:43]\n"
"ds_read_b128 a[216:219], v14 offset:6144\n"
"v_mul_f32 v253, v253, v29\n"
"v_mfma_f32_16x16x32_bf16 v[44:47], a[108:111], a[52:55], v[44:47]\n"
"buffer_load_dwordx4 v3, s[24:27], s65 offen offset:3072 lds\n"
"v_cvt_pk_bf16_f32 v230, v252, v253\n"
"v_mfma_f32_16x16x32_bf16 v[44:47], a[104:107], a[48:51], v[44:47]\n"
"ds_read_b128 a[220:223], v14 offset:7168\n"
"v_fma_f32 v16, v16, s38, v8\n"
"v_mfma_f32_16x16x32_bf16 v[48:51], a[112:115], a[48:51], v[48:51]\n"
"s_add_u32 m0, s76, 4096\n"
"v_exp_f32 v31, v31\n"
"v_mfma_f32_16x16x32_bf16 v[48:51], a[116:119], a[52:55], v[48:51]\n"
"ds_read_b128 a[136:139], v12 offset:2048\n"
"v_mul_f32 v254, v254, v238\n"
"v_mfma_f32_16x16x32_bf16 v[52:55], a[124:127], a[52:55], v[52:55]\n"
"buffer_load_dwordx4 v2, s[24:27], s66 offen offset:0 lds\n"
"v_mul_f32 v255, v255, v239\n"
"v_mfma_f32_16x16x32_bf16 v[52:55], a[120:123], a[48:51], v[52:55]\n"
"ds_read_b128 a[140:143], v13 offset:2048\n"
"v_add_f32 v30, 1.0, v30\n"
"v_mfma_f32_16x16x32_bf16 v[76:79], a[120:123], a[56:59], v[76:79]\n"
"v_add_f32 v31, 1.0, v31\n"
"v_fma_f32 v17, v17, s38, v8\n"
"v_mfma_f32_16x16x32_bf16 v[76:79], a[124:127], a[60:63], v[76:79]\n"
"ds_read_b128 a[144:147], v12 offset:4096\n"
"v_mul_f32 v16, v16, v176\n"
"v_mfma_f32_16x16x32_bf16 v[72:75], a[116:119], a[60:63], v[72:75]\n"
"buffer_load_dwordx4 v3, s[24:27], s66 offen offset:1024 lds\n"
"v_mul_f32 v17, v17, v177\n"
"v_mfma_f32_16x16x32_bf16 v[72:75], a[112:115], a[56:59], v[72:75]\n"
"ds_read_b128 a[148:151], v13 offset:4096\n"
"v_accvgpr_read_b32 v178, a2\n"
"v_mfma_f32_16x16x32_bf16 v[60:63], a[104:107], a[56:59], v[60:63]\n"
"v_rcp_f32 v30, v30\n"
"v_mfma_f32_16x16x32_bf16 v[60:63], a[108:111], a[60:63], v[60:63]\n"
"ds_read_b128 a[152:155], v12 offset:6144\n"
"v_accvgpr_read_b32 v179, a3\n"
"v_mfma_f32_16x16x32_bf16 v[56:59], a[100:103], a[60:63], v[56:59]\n"
"v_rcp_f32 v31, v31\n"
"v_mfma_f32_16x16x32_bf16 v[56:59], a[96:99], a[56:59], v[56:59]\n"
"ds_read_b128 a[156:159], v13 offset:6144\n"
"v_mul_f32 v254, v254, v30\n"
"v_mfma_f32_16x16x32_bf16 v[80:83], a[96:99], a[64:67], v[80:83]\n"
"buffer_load_dwordx4 v2, s[24:27], s67 offen offset:2048 lds\n"
"v_subrev_u32 v2, 1408, v2\n"
"v_mfma_f32_16x16x32_bf16 v[80:83], a[100:103], a[68:71], v[80:83]\n"
"ds_read_b128 a[160:163], v12 offset:8192\n"
"v_mul_f32 v255, v255, v31\n"
"v_mfma_f32_16x16x32_bf16 v[84:87], a[108:111], a[68:71], v[84:87]\n"
"v_exp_f32 v16, v16\n"
"v_mfma_f32_16x16x32_bf16 v[84:87], a[104:107], a[64:67], v[84:87]\n"
"ds_read_b128 a[164:167], v13 offset:8192\n"
"v_cvt_pk_bf16_f32 v231, v254, v255\n"
"v_mfma_f32_16x16x32_bf16 v[88:91], a[112:115], a[64:67], v[88:91]\n"
"buffer_load_dwordx4 v3, s[24:27], s67 offen offset:3072 lds\n"
"v_accvgpr_read_b32 v210, a18\n"
"v_mfma_f32_16x16x32_bf16 v[88:91], a[116:119], a[68:71], v[88:91]\n"
"ds_read_b128 a[168:171], v12 offset:10240\n"
"v_subrev_u32 v3, 1408, v3\n"
"v_mfma_f32_16x16x32_bf16 v[92:95], a[124:127], a[68:71], v[92:95]\n"
"v_exp_f32 v17, v17\n"
"v_mfma_f32_16x16x32_bf16 v[92:95], a[120:123], a[64:67], v[92:95]\n"
"ds_read_b128 a[172:175], v13 offset:10240\n"
"s_add_u32 m0, s77, 0\n"
"v_mfma_f32_16x16x32_bf16 v[116:119], a[120:123], a[72:75], v[116:119]\n"
"buffer_load_dwordx4 v4, s[56:59], 0 offen offset:0 sc1 lds\n"
"v_mul_f32 v208, v208, v176\n"
"v_mfma_f32_16x16x32_bf16 v[116:119], a[124:127], a[76:79], v[116:119]\n"
"ds_read_b128 a[176:179], v12 offset:12288\n"
"v_mul_f32 v209, v209, v177\n"
"v_mfma_f32_16x16x32_bf16 v[112:115], a[116:119], a[76:79], v[112:115]\n"
"v_add_f32 v16, 1.0, v16\n"
"v_add_f32 v17, 1.0, v17\n"
"v_mfma_f32_16x16x32_bf16 v[112:115], a[112:115], a[72:75], v[112:115]\n"
"ds_read_b128 a[180:183], v13 offset:12288\n"
"v_accvgpr_read_b32 v211, a19\n"
"v_mfma_f32_16x16x32_bf16 v[108:111], a[104:107], a[72:75], v[108:111]\n"
"v_rcp_f32 v16, v16\n"
"v_mfma_f32_16x16x32_bf16 v[108:111], a[108:111], a[76:79], v[108:111]\n"
"ds_read_b128 a[184:187], v12 offset:14336\n"
"v_mul_f32 v18, v178, v178\n"
"v_mfma_f32_16x16x32_bf16 v[104:107], a[100:103], a[76:79], v[104:107]\n"
"buffer_load_dwordx4 v4, s[56:59], 0 offen offset:1024 sc1 lds\n"
"v_mul_f32 v19, v179, v179\n"
"v_mfma_f32_16x16x32_bf16 v[104:107], a[96:99], a[72:75], v[104:107]\n"
"ds_read_b128 a[188:191], v13 offset:14336\n"
"v_fma_f32 v18, v18, s38, v8\n"
"v_mfma_f32_16x16x32_bf16 v[120:123], a[96:99], a[80:83], v[120:123]\n"
"v_rcp_f32 v17, v17\n"
"v_mfma_f32_16x16x32_bf16 v[120:123], a[100:103], a[84:87], v[120:123]\n"
"v_mul_f32 v208, v208, v16\n"
"v_mul_f32 v209, v209, v17\n"
"v_mfma_f32_16x16x32_bf16 v[124:127], a[108:111], a[84:87], v[124:127]\n"
"buffer_load_dwordx4 v4, s[56:59], 0 offen offset:2048 sc1 lds\n"
"v_cvt_pk_bf16_f32 v168, v208, v209\n"
"v_mfma_f32_16x16x32_bf16 v[124:127], a[104:107], a[80:83], v[124:127]\n"
"v_fma_f32 v19, v19, s38, v8\n"
"v_mul_f32 v18, v18, v178\n"
"v_mfma_f32_16x16x32_bf16 v[136:139], a[112:115], a[80:83], v[136:139]\n"
"v_mul_f32 v19, v19, v179\n"
"v_accvgpr_read_b32 v180, a4\n"
"v_mfma_f32_16x16x32_bf16 v[136:139], a[116:119], a[84:87], v[136:139]\n"
"v_exp_f32 v18, v18\n"
"v_mfma_f32_16x16x32_bf16 v[140:143], a[124:127], a[84:87], v[140:143]\n"
"v_exp_f32 v19, v19\n"
"v_mfma_f32_16x16x32_bf16 v[140:143], a[120:123], a[80:83], v[140:143]\n"
"buffer_load_dwordx4 v4, s[56:59], 0 offen offset:3072 sc1 lds\n"
"v_mul_f32 v210, v210, v178\n"
"v_mfma_f32_16x16x32_bf16 v[156:159], a[120:123], a[88:91], v[156:159]\n"
"v_subrev_u32 v4, 1441792, v4\n"
"s_add_u32 s76, s76, s8\n"
"v_mfma_f32_16x16x32_bf16 v[156:159], a[124:127], a[92:95], v[156:159]\n"
"s_add_u32 s77, s77, s8\n"
"s_cmp_eq_u32 s7, 98304\n"
"s_cselect_b32 s8, 4294868992, s9\n"
"v_mfma_f32_16x16x32_bf16 v[152:155], a[116:119], a[92:95], v[152:155]\n"
"s_add_u32 s7, s7, s8\n"
"v_add_u32 v12, s8, v12\n"
"v_mul_f32 v211, v211, v179\n"
"v_mfma_f32_16x16x32_bf16 v[152:155], a[112:115], a[88:91], v[152:155]\n"
"v_add_u32 v13, s8, v13\n"
"v_add_f32 v18, 1.0, v18\n"
"v_mfma_f32_16x16x32_bf16 v[148:151], a[104:107], a[88:91], v[148:151]\n"
"v_add_u32 v14, s8, v14\n"
"v_add_f32 v19, 1.0, v19\n"
"v_mfma_f32_16x16x32_bf16 v[148:151], a[108:111], a[92:95], v[148:151]\n"
"v_rcp_f32 v18, v18\n"
"v_mfma_f32_16x16x32_bf16 v[144:147], a[100:103], a[92:95], v[144:147]\n"
"v_rcp_f32 v19, v19\n"
"v_mfma_f32_16x16x32_bf16 v[144:147], a[96:99], a[88:91], v[144:147]\n"
"s_waitcnt lgkmcnt(14)\n"
"v_mfma_f32_16x16x32_bf16 a[224:227], a[192:195], a[128:131], a[224:227]\n"
"s_waitcnt vmcnt(14) lgkmcnt(0)\n"
"s_barrier\n"
"s_waitcnt vmcnt(13)\n"
"v_mfma_f32_16x16x32_bf16 a[224:227], a[196:199], a[132:135], a[224:227]\n"
"v_cmp_ne_u32 vcc, 1, v11\n"
"v_mul_f32 v210, v210, v18\n"
"v_mfma_f32_16x16x32_bf16 a[228:231], a[204:207], a[132:135], a[228:231]\n"
"s_nop 4\n"
"s_cbranch_vccnz PL20_%=\n"
"PL20d_%=:\n"
"v_mfma_f32_16x16x32_bf16 a[228:231], a[200:203], a[128:131], a[228:231]\n"
"ds_read_b128 a[32:35], v12 offset:0\n"
"s_add_u32 m0, s76, 0\n"
"v_mfma_f32_16x16x32_bf16 a[232:235], a[208:211], a[128:131], a[232:235]\n"
"s_mov_b32 s82, 24576\n"
"buffer_store_dwordx4 v[228:231], v7, s[56:59], s82 offen offset:1024 sc1\n"
"v_mul_f32 v211, v211, v19\n"
"v_cvt_pk_bf16_f32 v169, v210, v211\n"
"v_accvgpr_read_b32 v181, a5\n"
"v_mfma_f32_16x16x32_bf16 a[232:235], a[212:215], a[132:135], a[232:235]\n"
"ds_read_b128 a[36:39], v13 offset:0\n"
"v_accvgpr_read_b32 v212, a20\n"
"v_mfma_f32_16x16x32_bf16 a[236:239], a[220:223], a[132:135], a[236:239]\n"
"buffer_load_dwordx4 v2, s[24:27], s64 offen offset:0 lds\n"
"v_accvgpr_read_b32 v213, a21\n"
"v_mfma_f32_16x16x32_bf16 a[236:239], a[216:219], a[128:131], a[236:239]\n"
"ds_read_b128 a[96:99], v14 offset:0\n"
"v_mul_f32 v20, v180, v180\n"
"v_mfma_f32_16x16x32_bf16 a[252:255], a[216:219], a[136:139], a[252:255]\n"
"ds_read_b128 a[100:103], v14 offset:1024\n"
"v_mul_f32 v21, v181, v181\n"
"v_mfma_f32_16x16x32_bf16 a[252:255], a[220:223], a[140:143], a[252:255]\n"
"buffer_load_dwordx4 v3, s[24:27], s64 offen offset:1024 lds\n"
"v_fma_f32 v20, v20, s38, v8\n"
"v_mfma_f32_16x16x32_bf16 a[248:251], a[212:215], a[140:143], a[248:251]\n"
"ds_read_b128 a[104:107], v14 offset:2048\n"
"v_fma_f32 v21, v21, s38, v8\n"
"v_mfma_f32_16x16x32_bf16 a[248:251], a[208:211], a[136:139], a[248:251]\n"
"ds_read_b128 a[108:111], v14 offset:3072\n"
"v_mul_f32 v20, v20, v180\n"
"v_mfma_f32_16x16x32_bf16 a[244:247], a[200:203], a[136:139], a[244:247]\n"
"buffer_load_dwordx4 v2, s[24:27], s65 offen offset:2048 lds\n"
"v_mul_f32 v21, v21, v181\n"
"v_mfma_f32_16x16x32_bf16 a[244:247], a[204:207], a[140:143], a[244:247]\n"
"ds_read_b128 a[112:115], v14 offset:4096\n"
"v_accvgpr_read_b32 v182, a6\n"
"v_mfma_f32_16x16x32_bf16 a[240:243], a[196:199], a[140:143], a[240:243]\n"
"v_exp_f32 v20, v20\n"
"v_mfma_f32_16x16x32_bf16 a[240:243], a[192:195], a[136:139], a[240:243]\n"
"ds_read_b128 a[116:119], v14 offset:5120\n"
"v_accvgpr_read_b32 v183, a7\n"
"v_mfma_f32_16x16x32_bf16 v[40:43], a[192:195], a[144:147], v[40:43]\n"
"v_exp_f32 v21, v21\n"
"v_mfma_f32_16x16x32_bf16 v[40:43], a[196:199], a[148:151], v[40:43]\n"
"ds_read_b128 a[120:123], v14 offset:6144\n"
"v_mul_f32 v212, v212, v180\n"
"v_mfma_f32_16x16x32_bf16 v[44:47], a[204:207], a[148:151], v[44:47]\n"
"buffer_load_dwordx4 v3, s[24:27], s65 offen offset:3072 lds\n"
"v_mul_f32 v213, v213, v181\n"
"v_mfma_f32_16x16x32_bf16 v[44:47], a[200:203], a[144:147], v[44:47]\n"
"ds_read_b128 a[124:127], v14 offset:7168\n"
"v_add_f32 v20, 1.0, v20\n"
"v_mfma_f32_16x16x32_bf16 v[48:51], a[208:211], a[144:147], v[48:51]\n"
"s_add_u32 m0, s76, 4096\n"
"v_add_f32 v21, 1.0, v21\n"
"v_accvgpr_read_b32 v214, a22\n"
"v_mfma_f32_16x16x32_bf16 v[48:51], a[212:215], a[148:151], v[48:51]\n"
"ds_read_b128 a[40:43], v12 offset:2048\n"
"v_accvgpr_read_b32 v215, a23\n"
"v_mfma_f32_16x16x32_bf16 v[52:55], a[220:223], a[148:151], v[52:55]\n"
"buffer_load_dwordx4 v2, s[24:27], s66 offen offset:0 lds\n"
"v_mul_f32 v22, v182, v182\n"
"v_mfma_f32_16x16x32_bf16 v[52:55], a[216:219], a[144:147], v[52:55]\n"
"ds_read_b128 a[44:47], v13 offset:2048\n"
"v_mul_f32 v23, v183, v183\n"
"v_mfma_f32_16x16x32_bf16 v[76:79], a[216:219], a[152:155], v[76:79]\n"
"v_rcp_f32 v20, v20\n"
"v_mfma_f32_16x16x32_bf16 v[76:79], a[220:223], a[156:159], v[76:79]\n"
"ds_read_b128 a[48:51], v12 offset:4096\n"
"v_fma_f32 v22, v22, s38, v8\n"
"v_mfma_f32_16x16x32_bf16 v[72:75], a[212:215], a[156:159], v[72:75]\n"
"buffer_load_dwordx4 v3, s[24:27], s66 offen offset:1024 lds\n"
"v_fma_f32 v23, v23, s38, v8\n"
"v_mfma_f32_16x16x32_bf16 v[72:75], a[208:211], a[152:155], v[72:75]\n"
"ds_read_b128 a[52:55], v13 offset:4096\n"
"v_mul_f32 v22, v22, v182\n"
"v_mfma_f32_16x16x32_bf16 v[60:63], a[200:203], a[152:155], v[60:63]\n"
"v_rcp_f32 v21, v21\n"
"v_mfma_f32_16x16x32_bf16 v[60:63], a[204:207], a[156:159], v[60:63]\n"
"ds_read_b128 a[56:59], v12 offset:6144\n"
"v_mul_f32 v212, v212, v20\n"
"v_mfma_f32_16x16x32_bf16 v[56:59], a[196:199], a[156:159], v[56:59]\n"
"v_mul_f32 v213, v213, v21\n"
"v_cvt_pk_bf16_f32 v170, v212, v213\n"
"v_mfma_f32_16x16x32_bf16 v[56:59], a[192:195], a[152:155], v[56:59]\n"
"ds_read_b128 a[60:63], v13 offset:6144\n"
"v_mul_f32 v23, v23, v183\n"
"v_mfma_f32_16x16x32_bf16 v[80:83], a[192:195], a[160:163], v[80:83]\n"
"buffer_load_dwordx4 v2, s[24:27], s67 offen offset:2048 lds\n"
"v_add_u32 v2, 512, v2\n"
"v_mfma_f32_16x16x32_bf16 v[80:83], a[196:199], a[164:167], v[80:83]\n"
"ds_read_b128 a[64:67], v12 offset:8192\n"
"v_accvgpr_read_b32 v184, a8\n"
"v_mfma_f32_16x16x32_bf16 v[84:87], a[204:207], a[164:167], v[84:87]\n"
"v_exp_f32 v22, v22\n"
"v_mfma_f32_16x16x32_bf16 v[84:87], a[200:203], a[160:163], v[84:87]\n"
"ds_read_b128 a[68:71], v13 offset:8192\n"
"v_accvgpr_read_b32 v185, a9\n"
"v_mfma_f32_16x16x32_bf16 v[88:91], a[208:211], a[160:163], v[88:91]\n"
"buffer_load_dwordx4 v3, s[24:27], s67 offen offset:3072 lds\n"
"v_accvgpr_read_b32 v216, a24\n"
"v_mfma_f32_16x16x32_bf16 v[88:91], a[212:215], a[164:167], v[88:91]\n"
"ds_read_b128 a[72:75], v12 offset:10240\n"
"v_add_u32 v3, 512, v3\n"
"v_mfma_f32_16x16x32_bf16 v[92:95], a[220:223], a[164:167], v[92:95]\n"
"v_exp_f32 v23, v23\n"
"v_mfma_f32_16x16x32_bf16 v[92:95], a[216:219], a[160:163], v[92:95]\n"
"ds_read_b128 a[76:79], v13 offset:10240\n"
"s_add_u32 m0, s77, 0\n"
"v_mfma_f32_16x16x32_bf16 v[116:119], a[216:219], a[168:171], v[116:119]\n"
"buffer_load_dwordx4 v4, s[56:59], 0 offen offset:0 sc1 lds\n"
"v_mul_f32 v214, v214, v182\n"
"v_mfma_f32_16x16x32_bf16 v[116:119], a[220:223], a[172:175], v[116:119]\n"
"ds_read_b128 a[80:83], v12 offset:12288\n"
"v_mul_f32 v215, v215, v183\n"
"v_mfma_f32_16x16x32_bf16 v[112:115], a[212:215], a[172:175], v[112:115]\n"
"v_add_f32 v22, 1.0, v22\n"
"v_add_f32 v23, 1.0, v23\n"
"v_mfma_f32_16x16x32_bf16 v[112:115], a[208:211], a[168:171], v[112:115]\n"
"ds_read_b128 a[84:87], v13 offset:12288\n"
"v_accvgpr_read_b32 v217, a25\n"
"v_mfma_f32_16x16x32_bf16 v[108:111], a[200:203], a[168:171], v[108:111]\n"
"v_rcp_f32 v22, v22\n"
"v_mfma_f32_16x16x32_bf16 v[108:111], a[204:207], a[172:175], v[108:111]\n"
"ds_read_b128 a[88:91], v12 offset:14336\n"
"v_mul_f32 v24, v184, v184\n"
"v_mfma_f32_16x16x32_bf16 v[104:107], a[196:199], a[172:175], v[104:107]\n"
"buffer_load_dwordx4 v4, s[56:59], 0 offen offset:1024 sc1 lds\n"
"v_mul_f32 v25, v185, v185\n"
"v_mfma_f32_16x16x32_bf16 v[104:107], a[192:195], a[168:171], v[104:107]\n"
"ds_read_b128 a[92:95], v13 offset:14336\n"
"v_fma_f32 v24, v24, s38, v8\n"
"v_mfma_f32_16x16x32_bf16 v[120:123], a[192:195], a[176:179], v[120:123]\n"
"v_rcp_f32 v23, v23\n"
"v_mfma_f32_16x16x32_bf16 v[120:123], a[196:199], a[180:183], v[120:123]\n"
"v_mul_f32 v214, v214, v22\n"
"v_mul_f32 v215, v215, v23\n"
"v_mfma_f32_16x16x32_bf16 v[124:127], a[204:207], a[180:183], v[124:127]\n"
"buffer_load_dwordx4 v4, s[56:59], 0 offen offset:2048 sc1 lds\n"
"v_cvt_pk_bf16_f32 v171, v214, v215\n"
"v_mfma_f32_16x16x32_bf16 v[124:127], a[200:203], a[176:179], v[124:127]\n"
"v_fma_f32 v25, v25, s38, v8\n"
"v_mul_f32 v24, v24, v184\n"
"v_mfma_f32_16x16x32_bf16 v[136:139], a[208:211], a[176:179], v[136:139]\n"
"v_mul_f32 v25, v25, v185\n"
"v_accvgpr_read_b32 v186, a10\n"
"v_mfma_f32_16x16x32_bf16 v[136:139], a[212:215], a[180:183], v[136:139]\n"
"v_exp_f32 v24, v24\n"
"v_mfma_f32_16x16x32_bf16 v[140:143], a[220:223], a[180:183], v[140:143]\n"
"v_exp_f32 v25, v25\n"
"v_mfma_f32_16x16x32_bf16 v[140:143], a[216:219], a[176:179], v[140:143]\n"
"buffer_load_dwordx4 v4, s[56:59], 0 offen offset:3072 sc1 lds\n"
"v_mul_f32 v216, v216, v184\n"
"v_mfma_f32_16x16x32_bf16 v[156:159], a[216:219], a[184:187], v[156:159]\n"
"v_add_u32 v4, 524288, v4\n"
"s_add_u32 s76, s76, s8\n"
"v_mfma_f32_16x16x32_bf16 v[156:159], a[220:223], a[188:191], v[156:159]\n"
"s_add_u32 s77, s77, s8\n"
"s_cmp_eq_u32 s7, 98304\n"
"s_cselect_b32 s8, 4294868992, s9\n"
"v_mfma_f32_16x16x32_bf16 v[152:155], a[212:215], a[188:191], v[152:155]\n"
"s_add_u32 s7, s7, s8\n"
"v_add_u32 v12, s8, v12\n"
"v_mul_f32 v217, v217, v185\n"
"v_mfma_f32_16x16x32_bf16 v[152:155], a[208:211], a[184:187], v[152:155]\n"
"v_add_u32 v13, s8, v13\n"
"v_add_f32 v24, 1.0, v24\n"
"v_mfma_f32_16x16x32_bf16 v[148:151], a[200:203], a[184:187], v[148:151]\n"
"v_add_u32 v14, s8, v14\n"
"v_add_f32 v25, 1.0, v25\n"
"v_mfma_f32_16x16x32_bf16 v[148:151], a[204:207], a[188:191], v[148:151]\n"
"v_rcp_f32 v24, v24\n"
"v_mfma_f32_16x16x32_bf16 v[144:147], a[196:199], a[188:191], v[144:147]\n"
"v_rcp_f32 v25, v25\n"
"v_mfma_f32_16x16x32_bf16 v[144:147], a[192:195], a[184:187], v[144:147]\n"
"s_waitcnt lgkmcnt(14)\n"
"v_mfma_f32_16x16x32_bf16 a[224:227], a[96:99], a[32:35], a[224:227]\n"
"s_waitcnt vmcnt(13) lgkmcnt(0)\n"
"s_barrier\n"
"v_mul_f32 v216, v216, v24\n"
"v_mfma_f32_16x16x32_bf16 a[224:227], a[100:103], a[36:39], a[224:227]\n"
"ds_read_b128 a[128:131], v12 offset:0\n"
"v_mul_f32 v217, v217, v25\n"
"v_mfma_f32_16x16x32_bf16 a[228:231], a[108:111], a[36:39], a[228:231]\n"
"s_add_u32 m0, s76, 0\n"
"v_cvt_pk_bf16_f32 v172, v216, v217\n"
"v_accvgpr_read_b32 v187, a11\n"
"v_mfma_f32_16x16x32_bf16 a[228:231], a[104:107], a[32:35], a[228:231]\n"
"s_mov_b32 s82, 24576\n"
"buffer_store_dwordx4 v[168:171], v7, s[56:59], s82 offen offset:2048 sc1\n"
"v_accvgpr_read_b32 v218, a26\n"
"v_accvgpr_read_b32 v219, a27\n"
"v_mul_f32 v26, v186, v186\n"
"v_mfma_f32_16x16x32_bf16 a[232:235], a[112:115], a[32:35], a[232:235]\n"
"ds_read_b128 a[132:135], v13 offset:0\n"
"v_mul_f32 v27, v187, v187\n"
"v_mfma_f32_16x16x32_bf16 a[232:235], a[116:119], a[36:39], a[232:235]\n"
"buffer_load_dwordx4 v2, s[24:27], s64 offen offset:0 lds\n"
"v_fma_f32 v26, v26, s38, v8\n"
"v_mfma_f32_16x16x32_bf16 a[236:239], a[124:127], a[36:39], a[236:239]\n"
"ds_read_b128 a[192:195], v14 offset:0\n"
"v_fma_f32 v27, v27, s38, v8\n"
"v_mfma_f32_16x16x32_bf16 a[236:239], a[120:123], a[32:35], a[236:239]\n"
"ds_read_b128 a[196:199], v14 offset:1024\n"
"v_mul_f32 v26, v26, v186\n"
"v_mfma_f32_16x16x32_bf16 a[252:255], a[120:123], a[40:43], a[252:255]\n"
"buffer_load_dwordx4 v3, s[24:27], s64 offen offset:1024 lds\n"
"v_mul_f32 v27, v27, v187\n"
"v_mfma_f32_16x16x32_bf16 a[252:255], a[124:127], a[44:47], a[252:255]\n"
"ds_read_b128 a[200:203], v14 offset:2048\n"
"v_accvgpr_read_b32 v188, a12\n"
"v_mfma_f32_16x16x32_bf16 a[248:251], a[116:119], a[44:47], a[248:251]\n"
"v_exp_f32 v26, v26\n"
"v_mfma_f32_16x16x32_bf16 a[248:251], a[112:115], a[40:43], a[248:251]\n"
"ds_read_b128 a[204:207], v14 offset:3072\n"
"v_accvgpr_read_b32 v189, a13\n"
"v_mfma_f32_16x16x32_bf16 a[244:247], a[104:107], a[40:43], a[244:247]\n"
"buffer_load_dwordx4 v2, s[24:27], s65 offen offset:2048 lds\n"
"v_accvgpr_read_b32 v220, a28\n"
"v_mfma_f32_16x16x32_bf16 a[244:247], a[108:111], a[44:47], a[244:247]\n"
"ds_read_b128 a[208:211], v14 offset:4096\n"
"v_accvgpr_read_b32 v221, a29\n"
"v_mfma_f32_16x16x32_bf16 a[240:243], a[100:103], a[44:47], a[240:243]\n"
"v_exp_f32 v27, v27\n"
"v_mfma_f32_16x16x32_bf16 a[240:243], a[96:99], a[40:43], a[240:243]\n"
"ds_read_b128 a[212:215], v14 offset:5120\n"
"v_mul_f32 v218, v218, v186\n"
"v_mfma_f32_16x16x32_bf16 v[40:43], a[96:99], a[48:51], v[40:43]\n"
"v_mul_f32 v219, v219, v187\n"
"v_add_f32 v26, 1.0, v26\n"
"v_mfma_f32_16x16x32_bf16 v[40:43], a[100:103], a[52:55], v[40:43]\n"
"ds_read_b128 a[216:219], v14 offset:6144\n"
"v_add_f32 v27, 1.0, v27\n"
"v_mfma_f32_16x16x32_bf16 v[44:47], a[108:111], a[52:55], v[44:47]\n"
"buffer_load_dwordx4 v3, s[24:27], s65 offen offset:3072 lds\n"
"v_mul_f32 v28, v188, v188\n"
"v_mfma_f32_16x16x32_bf16 v[44:47], a[104:107], a[48:51], v[44:47]\n"
"ds_read_b128 a[220:223], v14 offset:7168\n"
"v_mul_f32 v29, v189, v189\n"
"v_mfma_f32_16x16x32_bf16 v[48:51], a[112:115], a[48:51], v[48:51]\n"
"s_add_u32 m0, s76, 4096\n"
"v_rcp_f32 v26, v26\n"
"v_mfma_f32_16x16x32_bf16 v[48:51], a[116:119], a[52:55], v[48:51]\n"
"ds_read_b128 a[136:139], v12 offset:2048\n"
"v_fma_f32 v28, v28, s38, v8\n"
"v_mfma_f32_16x16x32_bf16 v[52:55], a[124:127], a[52:55], v[52:55]\n"
"buffer_load_dwordx4 v2, s[24:27], s66 offen offset:0 lds\n"
"v_fma_f32 v29, v29, s38, v8\n"
"v_mfma_f32_16x16x32_bf16 v[52:55], a[120:123], a[48:51], v[52:55]\n"
"ds_read_b128 a[140:143], v13 offset:2048\n"
"v_mul_f32 v28, v28, v188\n"
"v_mfma_f32_16x16x32_bf16 v[76:79], a[120:123], a[56:59], v[76:79]\n"
"v_rcp_f32 v27, v27\n"
"v_mfma_f32_16x16x32_bf16 v[76:79], a[124:127], a[60:63], v[76:79]\n"
"ds_read_b128 a[144:147], v12 offset:4096\n"
"v_mul_f32 v218, v218, v26\n"
"v_mfma_f32_16x16x32_bf16 v[72:75], a[116:119], a[60:63], v[72:75]\n"
"buffer_load_dwordx4 v3, s[24:27], s66 offen offset:1024 lds\n"
"v_mul_f32 v219, v219, v27\n"
"v_mfma_f32_16x16x32_bf16 v[72:75], a[112:115], a[56:59], v[72:75]\n"
"ds_read_b128 a[148:151], v13 offset:4096\n"
"v_cvt_pk_bf16_f32 v173, v218, v219\n"
"v_mfma_f32_16x16x32_bf16 v[60:63], a[104:107], a[56:59], v[60:63]\n"
"v_mul_f32 v29, v29, v189\n"
"v_accvgpr_read_b32 v190, a14\n"
"v_mfma_f32_16x16x32_bf16 v[60:63], a[108:111], a[60:63], v[60:63]\n"
"ds_read_b128 a[152:155], v12 offset:6144\n"
"v_accvgpr_read_b32 v191, a15\n"
"v_mfma_f32_16x16x32_bf16 v[56:59], a[100:103], a[60:63], v[56:59]\n"
"v_exp_f32 v28, v28\n"
"v_mfma_f32_16x16x32_bf16 v[56:59], a[96:99], a[56:59], v[56:59]\n"
"ds_read_b128 a[156:159], v13 offset:6144\n"
"v_accvgpr_read_b32 v222, a30\n"
"v_mfma_f32_16x16x32_bf16 v[80:83], a[96:99], a[64:67], v[80:83]\n"
"buffer_load_dwordx4 v2, s[24:27], s67 offen offset:2048 lds\n"
"v_add_u32 v2, 512, v2\n"
"v_mfma_f32_16x16x32_bf16 v[80:83], a[100:103], a[68:71], v[80:83]\n"
"ds_read_b128 a[160:163], v12 offset:8192\n"
"v_accvgpr_read_b32 v223, a31\n"
"v_mfma_f32_16x16x32_bf16 v[84:87], a[108:111], a[68:71], v[84:87]\n"
"v_exp_f32 v29, v29\n"
"v_mfma_f32_16x16x32_bf16 v[84:87], a[104:107], a[64:67], v[84:87]\n"
"ds_read_b128 a[164:167], v13 offset:8192\n"
"v_mul_f32 v220, v220, v188\n"
"v_mfma_f32_16x16x32_bf16 v[88:91], a[112:115], a[64:67], v[88:91]\n"
"buffer_load_dwordx4 v3, s[24:27], s67 offen offset:3072 lds\n"
"v_mul_f32 v221, v221, v189\n"
"v_mfma_f32_16x16x32_bf16 v[88:91], a[116:119], a[68:71], v[88:91]\n"
"ds_read_b128 a[168:171], v12 offset:10240\n"
"v_add_u32 v3, 512, v3\n"
"v_mfma_f32_16x16x32_bf16 v[92:95], a[124:127], a[68:71], v[92:95]\n"
"v_add_f32 v28, 1.0, v28\n"
"v_add_f32 v29, 1.0, v29\n"
"v_mfma_f32_16x16x32_bf16 v[92:95], a[120:123], a[64:67], v[92:95]\n"
"ds_read_b128 a[172:175], v13 offset:10240\n"
"s_add_u32 m0, s77, 0\n"
"v_mfma_f32_16x16x32_bf16 v[116:119], a[120:123], a[72:75], v[116:119]\n"
"buffer_load_dwordx4 v4, s[56:59], 0 offen offset:0 sc1 lds\n"
"v_mul_f32 v30, v190, v190\n"
"v_mfma_f32_16x16x32_bf16 v[116:119], a[124:127], a[76:79], v[116:119]\n"
"ds_read_b128 a[176:179], v12 offset:12288\n"
"v_mul_f32 v31, v191, v191\n"
"v_mfma_f32_16x16x32_bf16 v[112:115], a[116:119], a[76:79], v[112:115]\n"
"v_rcp_f32 v28, v28\n"
"v_mfma_f32_16x16x32_bf16 v[112:115], a[112:115], a[72:75], v[112:115]\n"
"ds_read_b128 a[180:183], v13 offset:12288\n"
"v_fma_f32 v30, v30, s38, v8\n"
"v_mfma_f32_16x16x32_bf16 v[108:111], a[104:107], a[72:75], v[108:111]\n"
"v_rcp_f32 v29, v29\n"
"v_mfma_f32_16x16x32_bf16 v[108:111], a[108:111], a[76:79], v[108:111]\n"
"ds_read_b128 a[184:187], v12 offset:14336\n"
"v_mul_f32 v220, v220, v28\n"
"v_mfma_f32_16x16x32_bf16 v[104:107], a[100:103], a[76:79], v[104:107]\n"
"buffer_load_dwordx4 v4, s[56:59], 0 offen offset:1024 sc1 lds\n"
"v_mul_f32 v221, v221, v29\n"
"v_mfma_f32_16x16x32_bf16 v[104:107], a[96:99], a[72:75], v[104:107]\n"
"ds_read_b128 a[188:191], v13 offset:14336\n"
"v_cvt_pk_bf16_f32 v174, v220, v221\n"
"v_mfma_f32_16x16x32_bf16 v[120:123], a[96:99], a[80:83], v[120:123]\n"
"v_fma_f32 v31, v31, s38, v8\n"
"v_mul_f32 v30, v30, v190\n"
"v_mfma_f32_16x16x32_bf16 v[120:123], a[100:103], a[84:87], v[120:123]\n"
"v_mul_f32 v31, v31, v191\n"
"v_mfma_f32_16x16x32_bf16 v[124:127], a[108:111], a[84:87], v[124:127]\n"
"buffer_load_dwordx4 v4, s[56:59], 0 offen offset:2048 sc1 lds\n"
"v_mfma_f32_16x16x32_bf16 v[124:127], a[104:107], a[80:83], v[124:127]\n"
"v_exp_f32 v30, v30\n"
"v_mfma_f32_16x16x32_bf16 v[136:139], a[112:115], a[80:83], v[136:139]\n"
"v_exp_f32 v31, v31\n"
"v_mfma_f32_16x16x32_bf16 v[136:139], a[116:119], a[84:87], v[136:139]\n"
"v_mul_f32 v222, v222, v190\n"
"v_mul_f32 v223, v223, v191\n"
"v_mfma_f32_16x16x32_bf16 v[140:143], a[124:127], a[84:87], v[140:143]\n"
"v_add_f32 v30, 1.0, v30\n"
"v_add_f32 v31, 1.0, v31\n"
"v_mfma_f32_16x16x32_bf16 v[140:143], a[120:123], a[80:83], v[140:143]\n"
"buffer_load_dwordx4 v4, s[56:59], 0 offen offset:3072 sc1 lds\n"
"v_mfma_f32_16x16x32_bf16 v[156:159], a[120:123], a[88:91], v[156:159]\n"
"v_add_u32 v4, 524288, v4\n"
"s_add_u32 s76, s76, s8\n"
"v_mfma_f32_16x16x32_bf16 v[156:159], a[124:127], a[92:95], v[156:159]\n"
"s_add_u32 s77, s77, s8\n"
"s_cmp_eq_u32 s7, 98304\n"
"s_cselect_b32 s8, 4294868992, s9\n"
"v_mfma_f32_16x16x32_bf16 v[152:155], a[116:119], a[92:95], v[152:155]\n"
"s_add_u32 s7, s7, s8\n"
"v_add_u32 v12, s8, v12\n"
"v_mfma_f32_16x16x32_bf16 v[152:155], a[112:115], a[88:91], v[152:155]\n"
"v_add_u32 v13, s8, v13\n"
"v_mfma_f32_16x16x32_bf16 v[148:151], a[104:107], a[88:91], v[148:151]\n"
"v_add_u32 v14, s8, v14\n"
"v_mfma_f32_16x16x32_bf16 v[148:151], a[108:111], a[92:95], v[148:151]\n"
"v_rcp_f32 v30, v30\n"
"v_mfma_f32_16x16x32_bf16 v[144:147], a[100:103], a[92:95], v[144:147]\n"
"v_rcp_f32 v31, v31\n"
"v_mfma_f32_16x16x32_bf16 v[144:147], a[96:99], a[88:91], v[144:147]\n"
"s_waitcnt lgkmcnt(14)\n"
"v_mfma_f32_16x16x32_bf16 a[224:227], a[192:195], a[128:131], a[224:227]\n"
"s_waitcnt vmcnt(13) lgkmcnt(0)\n"
"s_barrier\n"
"v_mul_f32 v222, v222, v30\n"
"v_mfma_f32_16x16x32_bf16 a[224:227], a[196:199], a[132:135], a[224:227]\n"
"ds_read_b128 a[32:35], v12 offset:0\n"
"v_mul_f32 v223, v223, v31\n"
"v_mfma_f32_16x16x32_bf16 a[228:231], a[204:207], a[132:135], a[228:231]\n"
"s_add_u32 m0, s76, 0\n"
"v_cvt_pk_bf16_f32 v175, v222, v223\n"
"v_mfma_f32_16x16x32_bf16 a[228:231], a[200:203], a[128:131], a[228:231]\n"
"ds_read_b128 a[36:39], v13 offset:0\n"
"v_mfma_f32_16x16x32_bf16 a[232:235], a[208:211], a[128:131], a[232:235]\n"
"buffer_load_dwordx4 v2, s[24:27], s64 offen offset:0 lds\n"
"v_mfma_f32_16x16x32_bf16 a[232:235], a[212:215], a[132:135], a[232:235]\n"
"ds_read_b128 a[96:99], v14 offset:0\n"
"v_mfma_f32_16x16x32_bf16 a[236:239], a[220:223], a[132:135], a[236:239]\n"
"v_mfma_f32_16x16x32_bf16 a[236:239], a[216:219], a[128:131], a[236:239]\n"
"ds_read_b128 a[100:103], v14 offset:1024\n"
"v_mfma_f32_16x16x32_bf16 a[252:255], a[216:219], a[136:139], a[252:255]\n"
"buffer_load_dwordx4 v3, s[24:27], s64 offen offset:1024 lds\n"
"v_mfma_f32_16x16x32_bf16 a[252:255], a[220:223], a[140:143], a[252:255]\n"
"ds_read_b128 a[104:107], v14 offset:2048\n"
"v_mfma_f32_16x16x32_bf16 a[248:251], a[212:215], a[140:143], a[248:251]\n"
"v_mfma_f32_16x16x32_bf16 a[248:251], a[208:211], a[136:139], a[248:251]\n"
"ds_read_b128 a[108:111], v14 offset:3072\n"
"v_mfma_f32_16x16x32_bf16 a[244:247], a[200:203], a[136:139], a[244:247]\n"
"buffer_load_dwordx4 v2, s[24:27], s65 offen offset:2048 lds\n"
"v_mfma_f32_16x16x32_bf16 a[244:247], a[204:207], a[140:143], a[244:247]\n"
"ds_read_b128 a[112:115], v14 offset:4096\n"
"v_mfma_f32_16x16x32_bf16 a[240:243], a[196:199], a[140:143], a[240:243]\n"
"v_mfma_f32_16x16x32_bf16 a[240:243], a[192:195], a[136:139], a[240:243]\n"
"ds_read_b128 a[116:119], v14 offset:5120\n"
"v_mfma_f32_16x16x32_bf16 v[40:43], a[192:195], a[144:147], v[40:43]\n"
"v_mfma_f32_16x16x32_bf16 v[40:43], a[196:199], a[148:151], v[40:43]\n"
"ds_read_b128 a[120:123], v14 offset:6144\n"
"v_mfma_f32_16x16x32_bf16 v[44:47], a[204:207], a[148:151], v[44:47]\n"
"buffer_load_dwordx4 v3, s[24:27], s65 offen offset:3072 lds\n"
"v_mfma_f32_16x16x32_bf16 v[44:47], a[200:203], a[144:147], v[44:47]\n"
"ds_read_b128 a[124:127], v14 offset:7168\n"
"v_mfma_f32_16x16x32_bf16 v[48:51], a[208:211], a[144:147], v[48:51]\n"
"s_add_u32 m0, s76, 4096\n"
"v_mfma_f32_16x16x32_bf16 v[48:51], a[212:215], a[148:151], v[48:51]\n"
"ds_read_b128 a[40:43], v12 offset:2048\n"
"v_mfma_f32_16x16x32_bf16 v[52:55], a[220:223], a[148:151], v[52:55]\n"
"buffer_load_dwordx4 v2, s[24:27], s66 offen offset:0 lds\n"
"v_mfma_f32_16x16x32_bf16 v[52:55], a[216:219], a[144:147], v[52:55]\n"
"ds_read_b128 a[44:47], v13 offset:2048\n"
"v_mfma_f32_16x16x32_bf16 v[76:79], a[216:219], a[152:155], v[76:79]\n"
"v_mfma_f32_16x16x32_bf16 v[76:79], a[220:223], a[156:159], v[76:79]\n"
"ds_read_b128 a[48:51], v12 offset:4096\n"
"v_mfma_f32_16x16x32_bf16 v[72:75], a[212:215], a[156:159], v[72:75]\n"
"buffer_load_dwordx4 v3, s[24:27], s66 offen offset:1024 lds\n"
"v_mfma_f32_16x16x32_bf16 v[72:75], a[208:211], a[152:155], v[72:75]\n"
"ds_read_b128 a[52:55], v13 offset:4096\n"
"v_mfma_f32_16x16x32_bf16 v[60:63], a[200:203], a[152:155], v[60:63]\n"
"v_mfma_f32_16x16x32_bf16 v[60:63], a[204:207], a[156:159], v[60:63]\n"
"ds_read_b128 a[56:59], v12 offset:6144\n"
"v_mfma_f32_16x16x32_bf16 v[56:59], a[196:199], a[156:159], v[56:59]\n"
"v_mfma_f32_16x16x32_bf16 v[56:59], a[192:195], a[152:155], v[56:59]\n"
"ds_read_b128 a[60:63], v13 offset:6144\n"
"v_mfma_f32_16x16x32_bf16 v[80:83], a[192:195], a[160:163], v[80:83]\n"
"buffer_load_dwordx4 v2, s[24:27], s67 offen offset:2048 lds\n"
"v_add_u32 v2, 512, v2\n"
"v_mfma_f32_16x16x32_bf16 v[80:83], a[196:199], a[164:167], v[80:83]\n"
"ds_read_b128 a[64:67], v12 offset:8192\n"
"v_mfma_f32_16x16x32_bf16 v[84:87], a[204:207], a[164:167], v[84:87]\n"
"v_mfma_f32_16x16x32_bf16 v[84:87], a[200:203], a[160:163], v[84:87]\n"
"ds_read_b128 a[68:71], v13 offset:8192\n"
"v_mfma_f32_16x16x32_bf16 v[88:91], a[208:211], a[160:163], v[88:91]\n"
"buffer_load_dwordx4 v3, s[24:27], s67 offen offset:3072 lds\n"
"v_mfma_f32_16x16x32_bf16 v[88:91], a[212:215], a[164:167], v[88:91]\n"
"ds_read_b128 a[72:75], v12 offset:10240\n"
"v_add_u32 v3, 512, v3\n"
"v_mfma_f32_16x16x32_bf16 v[92:95], a[220:223], a[164:167], v[92:95]\n"
"v_mfma_f32_16x16x32_bf16 v[92:95], a[216:219], a[160:163], v[92:95]\n"
"ds_read_b128 a[76:79], v13 offset:10240\n"
"s_add_u32 m0, s77, 0\n"
"v_mfma_f32_16x16x32_bf16 v[116:119], a[216:219], a[168:171], v[116:119]\n"
"buffer_load_dwordx4 v4, s[56:59], 0 offen offset:0 sc1 lds\n"
"v_mfma_f32_16x16x32_bf16 v[116:119], a[220:223], a[172:175], v[116:119]\n"
"ds_read_b128 a[80:83], v12 offset:12288\n"
"v_mfma_f32_16x16x32_bf16 v[112:115], a[212:215], a[172:175], v[112:115]\n"
"v_mfma_f32_16x16x32_bf16 v[112:115], a[208:211], a[168:171], v[112:115]\n"
"ds_read_b128 a[84:87], v13 offset:12288\n"
"v_mfma_f32_16x16x32_bf16 v[108:111], a[200:203], a[168:171], v[108:111]\n"
"v_mfma_f32_16x16x32_bf16 v[108:111], a[204:207], a[172:175], v[108:111]\n"
"ds_read_b128 a[88:91], v12 offset:14336\n"
"v_mfma_f32_16x16x32_bf16 v[104:107], a[196:199], a[172:175], v[104:107]\n"
"buffer_load_dwordx4 v4, s[56:59], 0 offen offset:1024 sc1 lds\n"
"v_mfma_f32_16x16x32_bf16 v[104:107], a[192:195], a[168:171], v[104:107]\n"
"ds_read_b128 a[92:95], v13 offset:14336\n"
"v_mfma_f32_16x16x32_bf16 v[120:123], a[192:195], a[176:179], v[120:123]\n"
"v_mfma_f32_16x16x32_bf16 v[120:123], a[196:199], a[180:183], v[120:123]\n"
"v_mfma_f32_16x16x32_bf16 v[124:127], a[204:207], a[180:183], v[124:127]\n"
"buffer_load_dwordx4 v4, s[56:59], 0 offen offset:2048 sc1 lds\n"
"v_mfma_f32_16x16x32_bf16 v[124:127], a[200:203], a[176:179], v[124:127]\n"
"v_mfma_f32_16x16x32_bf16 v[136:139], a[208:211], a[176:179], v[136:139]\n"
"v_mfma_f32_16x16x32_bf16 v[136:139], a[212:215], a[180:183], v[136:139]\n"
"v_mfma_f32_16x16x32_bf16 v[140:143], a[220:223], a[180:183], v[140:143]\n"
"v_mfma_f32_16x16x32_bf16 v[140:143], a[216:219], a[176:179], v[140:143]\n"
"buffer_load_dwordx4 v4, s[56:59], 0 offen offset:3072 sc1 lds\n"
"v_mfma_f32_16x16x32_bf16 v[156:159], a[216:219], a[184:187], v[156:159]\n"
"v_add_u32 v4, 524288, v4\n"
"s_add_u32 s76, s76, s8\n"
"v_mfma_f32_16x16x32_bf16 v[156:159], a[220:223], a[188:191], v[156:159]\n"
"s_add_u32 s77, s77, s8\n"
"s_cmp_eq_u32 s7, 98304\n"
"s_cselect_b32 s8, 4294868992, s9\n"
"v_mfma_f32_16x16x32_bf16 v[152:155], a[212:215], a[188:191], v[152:155]\n"
"s_add_u32 s7, s7, s8\n"
"v_add_u32 v12, s8, v12\n"
"v_mfma_f32_16x16x32_bf16 v[152:155], a[208:211], a[184:187], v[152:155]\n"
"v_add_u32 v13, s8, v13\n"
"v_mfma_f32_16x16x32_bf16 v[148:151], a[200:203], a[184:187], v[148:151]\n"
"v_add_u32 v14, s8, v14\n"
"v_mfma_f32_16x16x32_bf16 v[148:151], a[204:207], a[188:191], v[148:151]\n"
"v_mfma_f32_16x16x32_bf16 v[144:147], a[196:199], a[188:191], v[144:147]\n"
"v_mfma_f32_16x16x32_bf16 v[144:147], a[192:195], a[184:187], v[144:147]\n"
"s_waitcnt lgkmcnt(14)\n"
"v_mfma_f32_16x16x32_bf16 a[224:227], a[96:99], a[32:35], a[224:227]\n"
"s_waitcnt vmcnt(12) lgkmcnt(0)\n"
"s_barrier\n"
"v_mfma_f32_16x16x32_bf16 a[224:227], a[100:103], a[36:39], a[224:227]\n"
"buffer_load_dword v11, v10, s[56:59], 0 offen offset:12 sc1\n"
"v_mfma_f32_16x16x32_bf16 a[228:231], a[108:111], a[36:39], a[228:231]\n"
"ds_read_b128 a[128:131], v12 offset:0\n"
"s_add_u32 m0, s76, 0\n"
"v_mfma_f32_16x16x32_bf16 a[228:231], a[104:107], a[32:35], a[228:231]\n"
"s_mov_b32 s82, 24576\n"
"buffer_store_dwordx4 v[172:175], v7, s[56:59], s82 offen offset:3072 sc1\n"
"v_mfma_f32_16x16x32_bf16 a[232:235], a[112:115], a[32:35], a[232:235]\n"
"ds_read_b128 a[132:135], v13 offset:0\n"
"v_mfma_f32_16x16x32_bf16 a[232:235], a[116:119], a[36:39], a[232:235]\n"
"buffer_load_dwordx4 v2, s[24:27], s64 offen offset:0 lds\n"
"v_mfma_f32_16x16x32_bf16 a[236:239], a[124:127], a[36:39], a[236:239]\n"
"ds_read_b128 a[192:195], v14 offset:0\n"
"v_mfma_f32_16x16x32_bf16 a[236:239], a[120:123], a[32:35], a[236:239]\n"
"ds_read_b128 a[196:199], v14 offset:1024\n"
"v_mfma_f32_16x16x32_bf16 a[252:255], a[120:123], a[40:43], a[252:255]\n"
"buffer_load_dwordx4 v3, s[24:27], s64 offen offset:1024 lds\n"
"v_mfma_f32_16x16x32_bf16 a[252:255], a[124:127], a[44:47], a[252:255]\n"
"ds_read_b128 a[200:203], v14 offset:2048\n"
"v_mfma_f32_16x16x32_bf16 a[248:251], a[116:119], a[44:47], a[248:251]\n"
"v_mfma_f32_16x16x32_bf16 a[248:251], a[112:115], a[40:43], a[248:251]\n"
"ds_read_b128 a[204:207], v14 offset:3072\n"
"v_mfma_f32_16x16x32_bf16 a[244:247], a[104:107], a[40:43], a[244:247]\n"
"buffer_load_dwordx4 v2, s[24:27], s65 offen offset:2048 lds\n"
"v_mfma_f32_16x16x32_bf16 a[244:247], a[108:111], a[44:47], a[244:247]\n"
"ds_read_b128 a[208:211], v14 offset:4096\n"
"v_mfma_f32_16x16x32_bf16 a[240:243], a[100:103], a[44:47], a[240:243]\n"
"v_mfma_f32_16x16x32_bf16 a[240:243], a[96:99], a[40:43], a[240:243]\n"
"ds_read_b128 a[212:215], v14 offset:5120\n"
"v_mfma_f32_16x16x32_bf16 v[40:43], a[96:99], a[48:51], v[40:43]\n"
"v_mfma_f32_16x16x32_bf16 v[40:43], a[100:103], a[52:55], v[40:43]\n"
"ds_read_b128 a[216:219], v14 offset:6144\n"
"v_mfma_f32_16x16x32_bf16 v[44:47], a[108:111], a[52:55], v[44:47]\n"
"buffer_load_dwordx4 v3, s[24:27], s65 offen offset:3072 lds\n"
"v_mfma_f32_16x16x32_bf16 v[44:47], a[104:107], a[48:51], v[44:47]\n"
"ds_read_b128 a[220:223], v14 offset:7168\n"
"v_mfma_f32_16x16x32_bf16 v[48:51], a[112:115], a[48:51], v[48:51]\n"
"s_add_u32 m0, s76, 4096\n"
"v_mfma_f32_16x16x32_bf16 v[48:51], a[116:119], a[52:55], v[48:51]\n"
"ds_read_b128 a[136:139], v12 offset:2048\n"
"v_mfma_f32_16x16x32_bf16 v[52:55], a[124:127], a[52:55], v[52:55]\n"
"buffer_load_dwordx4 v2, s[24:27], s66 offen offset:0 lds\n"
"v_mfma_f32_16x16x32_bf16 v[52:55], a[120:123], a[48:51], v[52:55]\n"
"ds_read_b128 a[140:143], v13 offset:2048\n"
"v_mfma_f32_16x16x32_bf16 v[76:79], a[120:123], a[56:59], v[76:79]\n"
"v_mfma_f32_16x16x32_bf16 v[76:79], a[124:127], a[60:63], v[76:79]\n"
"ds_read_b128 a[144:147], v12 offset:4096\n"
"v_mfma_f32_16x16x32_bf16 v[72:75], a[116:119], a[60:63], v[72:75]\n"
"buffer_load_dwordx4 v3, s[24:27], s66 offen offset:1024 lds\n"
"v_mfma_f32_16x16x32_bf16 v[72:75], a[112:115], a[56:59], v[72:75]\n"
"ds_read_b128 a[148:151], v13 offset:4096\n"
"v_mfma_f32_16x16x32_bf16 v[60:63], a[104:107], a[56:59], v[60:63]\n"
"v_mfma_f32_16x16x32_bf16 v[60:63], a[108:111], a[60:63], v[60:63]\n"
"ds_read_b128 a[152:155], v12 offset:6144\n"
"v_mfma_f32_16x16x32_bf16 v[56:59], a[100:103], a[60:63], v[56:59]\n"
"v_mfma_f32_16x16x32_bf16 v[56:59], a[96:99], a[56:59], v[56:59]\n"
"ds_read_b128 a[156:159], v13 offset:6144\n"
"v_mfma_f32_16x16x32_bf16 v[80:83], a[96:99], a[64:67], v[80:83]\n"
"buffer_load_dwordx4 v2, s[24:27], s67 offen offset:2048 lds\n"
"v_subrev_u32 v2, 1408, v2\n"
"v_mfma_f32_16x16x32_bf16 v[80:83], a[100:103], a[68:71], v[80:83]\n"
"ds_read_b128 a[160:163], v12 offset:8192\n"
"v_mfma_f32_16x16x32_bf16 v[84:87], a[108:111], a[68:71], v[84:87]\n"
"v_mfma_f32_16x16x32_bf16 v[84:87], a[104:107], a[64:67], v[84:87]\n"
"ds_read_b128 a[164:167], v13 offset:8192\n"
"v_mfma_f32_16x16x32_bf16 v[88:91], a[112:115], a[64:67], v[88:91]\n"
"buffer_load_dwordx4 v3, s[24:27], s67 offen offset:3072 lds\n"
"v_mfma_f32_16x16x32_bf16 v[88:91], a[116:119], a[68:71], v[88:91]\n"
"ds_read_b128 a[168:171], v12 offset:10240\n"
"v_subrev_u32 v3, 1408, v3\n"
"v_mfma_f32_16x16x32_bf16 v[92:95], a[124:127], a[68:71], v[92:95]\n"
"v_mfma_f32_16x16x32_bf16 v[92:95], a[120:123], a[64:67], v[92:95]\n"
"ds_read_b128 a[172:175], v13 offset:10240\n"
"s_add_u32 m0, s77, 0\n"
"v_mfma_f32_16x16x32_bf16 v[116:119], a[120:123], a[72:75], v[116:119]\n"
"buffer_load_dwordx4 v4, s[56:59], 0 offen offset:0 sc1 lds\n"
"v_mfma_f32_16x16x32_bf16 v[116:119], a[124:127], a[76:79], v[116:119]\n"
"ds_read_b128 a[176:179], v12 offset:12288\n"
"v_mfma_f32_16x16x32_bf16 v[112:115], a[116:119], a[76:79], v[112:115]\n"
"v_mfma_f32_16x16x32_bf16 v[112:115], a[112:115], a[72:75], v[112:115]\n"
"ds_read_b128 a[180:183], v13 offset:12288\n"
"v_mfma_f32_16x16x32_bf16 v[108:111], a[104:107], a[72:75], v[108:111]\n"
"v_mfma_f32_16x16x32_bf16 v[108:111], a[108:111], a[76:79], v[108:111]\n"
"ds_read_b128 a[184:187], v12 offset:14336\n"
"v_mfma_f32_16x16x32_bf16 v[104:107], a[100:103], a[76:79], v[104:107]\n"
"buffer_load_dwordx4 v4, s[56:59], 0 offen offset:1024 sc1 lds\n"
"v_mfma_f32_16x16x32_bf16 v[104:107], a[96:99], a[72:75], v[104:107]\n"
"ds_read_b128 a[188:191], v13 offset:14336\n"
"v_mfma_f32_16x16x32_bf16 v[120:123], a[96:99], a[80:83], v[120:123]\n"
"v_mfma_f32_16x16x32_bf16 v[120:123], a[100:103], a[84:87], v[120:123]\n"
"v_mfma_f32_16x16x32_bf16 v[124:127], a[108:111], a[84:87], v[124:127]\n"
"buffer_load_dwordx4 v4, s[56:59], 0 offen offset:2048 sc1 lds\n"
"v_mfma_f32_16x16x32_bf16 v[124:127], a[104:107], a[80:83], v[124:127]\n"
"v_mfma_f32_16x16x32_bf16 v[136:139], a[112:115], a[80:83], v[136:139]\n"
"v_mfma_f32_16x16x32_bf16 v[136:139], a[116:119], a[84:87], v[136:139]\n"
"v_mfma_f32_16x16x32_bf16 v[140:143], a[124:127], a[84:87], v[140:143]\n"
"v_mfma_f32_16x16x32_bf16 v[140:143], a[120:123], a[80:83], v[140:143]\n"
"buffer_load_dwordx4 v4, s[56:59], 0 offen offset:3072 sc1 lds\n"
"v_mfma_f32_16x16x32_bf16 v[156:159], a[120:123], a[88:91], v[156:159]\n"
"v_subrev_u32 v4, 1441792, v4\n"
"s_add_u32 s76, s76, s8\n"
"v_mfma_f32_16x16x32_bf16 v[156:159], a[124:127], a[92:95], v[156:159]\n"
"s_add_u32 s77, s77, s8\n"
"s_cmp_eq_u32 s7, 98304\n"
"s_cselect_b32 s8, 4294868992, s9\n"
"v_mfma_f32_16x16x32_bf16 v[152:155], a[116:119], a[92:95], v[152:155]\n"
"s_add_u32 s7, s7, s8\n"
"v_add_u32 v12, s8, v12\n"
"v_mfma_f32_16x16x32_bf16 v[152:155], a[112:115], a[88:91], v[152:155]\n"
"v_add_u32 v13, s8, v13\n"
"v_mfma_f32_16x16x32_bf16 v[148:151], a[104:107], a[88:91], v[148:151]\n"
"v_add_u32 v14, s8, v14\n"
"v_mfma_f32_16x16x32_bf16 v[148:151], a[108:111], a[92:95], v[148:151]\n"
"v_mfma_f32_16x16x32_bf16 v[144:147], a[100:103], a[92:95], v[144:147]\n"
"v_mfma_f32_16x16x32_bf16 v[144:147], a[96:99], a[88:91], v[144:147]\n"
"s_waitcnt lgkmcnt(14)\n"
"v_mfma_f32_16x16x32_bf16 a[224:227], a[192:195], a[128:131], a[224:227]\n"
"s_waitcnt vmcnt(14) lgkmcnt(0)\n"
"s_barrier\n"
"s_waitcnt vmcnt(13)\n"
"v_mfma_f32_16x16x32_bf16 a[224:227], a[196:199], a[132:135], a[224:227]\n"
"v_cmp_ne_u32 vcc, 1, v11\n"
"v_mfma_f32_16x16x32_bf16 a[228:231], a[204:207], a[132:135], a[228:231]\n"
"s_nop 4\n"
"s_cbranch_vccnz PL22_%=\n"
"PL22d_%=:\n"
"v_mfma_f32_16x16x32_bf16 a[228:231], a[200:203], a[128:131], a[228:231]\n"
"ds_read_b128 a[32:35], v12 offset:0\n"
"s_add_u32 m0, s76, 0\n"
"v_mfma_f32_16x16x32_bf16 a[232:235], a[208:211], a[128:131], a[232:235]\n"
"ds_read_b128 a[36:39], v13 offset:0\n"
"v_mfma_f32_16x16x32_bf16 a[232:235], a[212:215], a[132:135], a[232:235]\n"
"buffer_load_dwordx4 v2, s[24:27], s64 offen offset:0 lds\n"
"v_mfma_f32_16x16x32_bf16 a[236:239], a[220:223], a[132:135], a[236:239]\n"
"ds_read_b128 a[96:99], v14 offset:0\n"
"v_mfma_f32_16x16x32_bf16 a[236:239], a[216:219], a[128:131], a[236:239]\n"
"ds_read_b128 a[100:103], v14 offset:1024\n"
"v_mfma_f32_16x16x32_bf16 a[252:255], a[216:219], a[136:139], a[252:255]\n"
"buffer_load_dwordx4 v3, s[24:27], s64 offen offset:1024 lds\n"
"v_mfma_f32_16x16x32_bf16 a[252:255], a[220:223], a[140:143], a[252:255]\n"
"ds_read_b128 a[104:107], v14 offset:2048\n"
"v_mfma_f32_16x16x32_bf16 a[248:251], a[212:215], a[140:143], a[248:251]\n"
"v_mfma_f32_16x16x32_bf16 a[248:251], a[208:211], a[136:139], a[248:251]\n"
"ds_read_b128 a[108:111], v14 offset:3072\n"
"v_mfma_f32_16x16x32_bf16 a[244:247], a[200:203], a[136:139], a[244:247]\n"
"buffer_load_dwordx4 v2, s[24:27], s65 offen offset:2048 lds\n"
"v_mfma_f32_16x16x32_bf16 a[244:247], a[204:207], a[140:143], a[244:247]\n"
"ds_read_b128 a[112:115], v14 offset:4096\n"
"v_mfma_f32_16x16x32_bf16 a[240:243], a[196:199], a[140:143], a[240:243]\n"
"v_mfma_f32_16x16x32_bf16 a[240:243], a[192:195], a[136:139], a[240:243]\n"
"ds_read_b128 a[116:119], v14 offset:5120\n"
"v_mfma_f32_16x16x32_bf16 v[40:43], a[192:195], a[144:147], v[40:43]\n"
"v_mfma_f32_16x16x32_bf16 v[40:43], a[196:199], a[148:151], v[40:43]\n"
"ds_read_b128 a[120:123], v14 offset:6144\n"
"v_mfma_f32_16x16x32_bf16 v[44:47], a[204:207], a[148:151], v[44:47]\n"
"buffer_load_dwordx4 v3, s[24:27], s65 offen offset:3072 lds\n"
"v_mfma_f32_16x16x32_bf16 v[44:47], a[200:203], a[144:147], v[44:47]\n"
"ds_read_b128 a[124:127], v14 offset:7168\n"
"v_mfma_f32_16x16x32_bf16 v[48:51], a[208:211], a[144:147], v[48:51]\n"
"s_add_u32 m0, s76, 4096\n"
"v_mfma_f32_16x16x32_bf16 v[48:51], a[212:215], a[148:151], v[48:51]\n"
"ds_read_b128 a[40:43], v12 offset:2048\n"
"v_mfma_f32_16x16x32_bf16 v[52:55], a[220:223], a[148:151], v[52:55]\n"
"buffer_load_dwordx4 v2, s[24:27], s66 offen offset:0 lds\n"
"v_mfma_f32_16x16x32_bf16 v[52:55], a[216:219], a[144:147], v[52:55]\n"
"ds_read_b128 a[44:47], v13 offset:2048\n"
"v_mfma_f32_16x16x32_bf16 v[76:79], a[216:219], a[152:155], v[76:79]\n"
"v_mfma_f32_16x16x32_bf16 v[76:79], a[220:223], a[156:159], v[76:79]\n"
"ds_read_b128 a[48:51], v12 offset:4096\n"
"v_mfma_f32_16x16x32_bf16 v[72:75], a[212:215], a[156:159], v[72:75]\n"
"buffer_load_dwordx4 v3, s[24:27], s66 offen offset:1024 lds\n"
"v_mfma_f32_16x16x32_bf16 v[72:75], a[208:211], a[152:155], v[72:75]\n"
"ds_read_b128 a[52:55], v13 offset:4096\n"
"v_mfma_f32_16x16x32_bf16 v[60:63], a[200:203], a[152:155], v[60:63]\n"
"v_mfma_f32_16x16x32_bf16 v[60:63], a[204:207], a[156:159], v[60:63]\n"
"ds_read_b128 a[56:59], v12 offset:6144\n"
"v_mfma_f32_16x16x32_bf16 v[56:59], a[196:199], a[156:159], v[56:59]\n"
"v_mfma_f32_16x16x32_bf16 v[56:59], a[192:195], a[152:155], v[56:59]\n"
"ds_read_b128 a[60:63], v13 offset:6144\n"
"v_mfma_f32_16x16x32_bf16 v[80:83], a[192:195], a[160:163], v[80:83]\n"
"buffer_load_dwordx4 v2, s[24:27], s67 offen offset:2048 lds\n"
"v_add_u32 v2, 512, v2\n"
"v_mfma_f32_16x16x32_bf16 v[80:83], a[196:199], a[164:167], v[80:83]\n"
"ds_read_b128 a[64:67], v12 offset:8192\n"
"v_mfma_f32_16x16x32_bf16 v[84:87], a[204:207], a[164:167], v[84:87]\n"
"v_mfma_f32_16x16x32_bf16 v[84:87], a[200:203], a[160:163], v[84:87]\n"
"ds_read_b128 a[68:71], v13 offset:8192\n"
"v_mfma_f32_16x16x32_bf16 v[88:91], a[208:211], a[160:163], v[88:91]\n"
"buffer_load_dwordx4 v3, s[24:27], s67 offen offset:3072 lds\n"
"v_mfma_f32_16x16x32_bf16 v[88:91], a[212:215], a[164:167], v[88:91]\n"
"ds_read_b128 a[72:75], v12 offset:10240\n"
"v_add_u32 v3, 512, v3\n"
"v_mfma_f32_16x16x32_bf16 v[92:95], a[220:223], a[164:167], v[92:95]\n"
"v_mfma_f32_16x16x32_bf16 v[92:95], a[216:219], a[160:163], v[92:95]\n"
"ds_read_b128 a[76:79], v13 offset:10240\n"
"s_add_u32 m0, s77, 0\n"
"v_mfma_f32_16x16x32_bf16 v[116:119], a[216:219], a[168:171], v[116:119]\n"
"buffer_load_dwordx4 v4, s[56:59], 0 offen offset:0 sc1 lds\n"
"v_mfma_f32_16x16x32_bf16 v[116:119], a[220:223], a[172:175], v[116:119]\n"
"ds_read_b128 a[80:83], v12 offset:12288\n"
"v_mfma_f32_16x16x32_bf16 v[112:115], a[212:215], a[172:175], v[112:115]\n"
"v_mfma_f32_16x16x32_bf16 v[112:115], a[208:211], a[168:171], v[112:115]\n"
"ds_read_b128 a[84:87], v13 offset:12288\n"
"v_mfma_f32_16x16x32_bf16 v[108:111], a[200:203], a[168:171], v[108:111]\n"
"v_mfma_f32_16x16x32_bf16 v[108:111], a[204:207], a[172:175], v[108:111]\n"
"ds_read_b128 a[88:91], v12 offset:14336\n"
"v_mfma_f32_16x16x32_bf16 v[104:107], a[196:199], a[172:175], v[104:107]\n"
"buffer_load_dwordx4 v4, s[56:59], 0 offen offset:1024 sc1 lds\n"
"v_mfma_f32_16x16x32_bf16 v[104:107], a[192:195], a[168:171], v[104:107]\n"
"ds_read_b128 a[92:95], v13 offset:14336\n"
"v_mfma_f32_16x16x32_bf16 v[120:123], a[192:195], a[176:179], v[120:123]\n"
"v_mfma_f32_16x16x32_bf16 v[120:123], a[196:199], a[180:183], v[120:123]\n"
"v_mfma_f32_16x16x32_bf16 v[124:127], a[204:207], a[180:183], v[124:127]\n"
"buffer_load_dwordx4 v4, s[56:59], 0 offen offset:2048 sc1 lds\n"
"v_mfma_f32_16x16x32_bf16 v[124:127], a[200:203], a[176:179], v[124:127]\n"
"v_mfma_f32_16x16x32_bf16 v[136:139], a[208:211], a[176:179], v[136:139]\n"
"v_mfma_f32_16x16x32_bf16 v[136:139], a[212:215], a[180:183], v[136:139]\n"
"v_mfma_f32_16x16x32_bf16 v[140:143], a[220:223], a[180:183], v[140:143]\n"
"v_mfma_f32_16x16x32_bf16 v[140:143], a[216:219], a[176:179], v[140:143]\n"
"buffer_load_dwordx4 v4, s[56:59], 0 offen offset:3072 sc1 lds\n"
"v_mfma_f32_16x16x32_bf16 v[156:159], a[216:219], a[184:187], v[156:159]\n"
"v_add_u32 v4, 524288, v4\n"
"s_add_u32 s76, s76, s8\n"
"v_mfma_f32_16x16x32_bf16 v[156:159], a[220:223], a[188:191], v[156:159]\n"
"s_add_u32 s77, s77, s8\n"
"s_cmp_eq_u32 s7, 98304\n"
"s_cselect_b32 s8, 4294868992, s9\n"
"v_mfma_f32_16x16x32_bf16 v[152:155], a[212:215], a[188:191], v[152:155]\n"
"s_add_u32 s7, s7, s8\n"
"v_add_u32 v12, s8, v12\n"
"v_mfma_f32_16x16x32_bf16 v[152:155], a[208:211], a[184:187], v[152:155]\n"
"v_add_u32 v13, s8, v13\n"
"v_mfma_f32_16x16x32_bf16 v[148:151], a[200:203], a[184:187], v[148:151]\n"
"v_add_u32 v14, s8, v14\n"
"v_mfma_f32_16x16x32_bf16 v[148:151], a[204:207], a[188:191], v[148:151]\n"
"v_mfma_f32_16x16x32_bf16 v[144:147], a[196:199], a[188:191], v[144:147]\n"
"v_mfma_f32_16x16x32_bf16 v[144:147], a[192:195], a[184:187], v[144:147]\n"
"s_waitcnt lgkmcnt(14)\n"
"v_mfma_f32_16x16x32_bf16 a[224:227], a[96:99], a[32:35], a[224:227]\n"
"s_waitcnt vmcnt(12) lgkmcnt(0)\n"
"s_barrier\n"
"v_mfma_f32_16x16x32_bf16 a[224:227], a[100:103], a[36:39], a[224:227]\n"
"s_cmp_eq_u32 s13, 0\n"
"s_cbranch_scc1 EV21_%=\n"
"EV21r_%=:\n"
"v_mfma_f32_16x16x32_bf16 a[228:231], a[108:111], a[36:39], a[228:231]\n"
"ds_read_b128 a[128:131], v12 offset:0\n"
"s_add_u32 m0, s76, 0\n"
"v_mfma_f32_16x16x32_bf16 a[228:231], a[104:107], a[32:35], a[228:231]\n"
"ds_read_b128 a[132:135], v13 offset:0\n"
"v_mfma_f32_16x16x32_bf16 a[232:235], a[112:115], a[32:35], a[232:235]\n"
"buffer_load_dwordx4 v2, s[24:27], s64 offen offset:0 lds\n"
"v_mfma_f32_16x16x32_bf16 a[232:235], a[116:119], a[36:39], a[232:235]\n"
"ds_read_b128 a[192:195], v14 offset:0\n"
"v_mfma_f32_16x16x32_bf16 a[236:239], a[124:127], a[36:39], a[236:239]\n"
"v_mfma_f32_16x16x32_bf16 a[236:239], a[120:123], a[32:35], a[236:239]\n"
"ds_read_b128 a[196:199], v14 offset:1024\n"
"v_mfma_f32_16x16x32_bf16 a[252:255], a[120:123], a[40:43], a[252:255]\n"
"buffer_load_dwordx4 v3, s[24:27], s64 offen offset:1024 lds\n"
"v_mfma_f32_16x16x32_bf16 a[252:255], a[124:127], a[44:47], a[252:255]\n"
"ds_read_b128 a[200:203], v14 offset:2048\n"
"v_mfma_f32_16x16x32_bf16 a[248:251], a[116:119], a[44:47], a[248:251]\n"
"v_mfma_f32_16x16x32_bf16 a[248:251], a[112:115], a[40:43], a[248:251]\n"
"ds_read_b128 a[204:207], v14 offset:3072\n"
"v_mfma_f32_16x16x32_bf16 a[244:247], a[104:107], a[40:43], a[244:247]\n"
"buffer_load_dwordx4 v2, s[24:27], s65 offen offset:2048 lds\n"
"v_mfma_f32_16x16x32_bf16 a[244:247], a[108:111], a[44:47], a[244:247]\n"
"ds_read_b128 a[208:211], v14 offset:4096\n"
"v_mfma_f32_16x16x32_bf16 a[240:243], a[100:103], a[44:47], a[240:243]\n"
"v_mfma_f32_16x16x32_bf16 a[240:243], a[96:99], a[40:43], a[240:243]\n"
"ds_read_b128 a[212:215], v14 offset:5120\n"
"v_mfma_f32_16x16x32_bf16 v[40:43], a[96:99], a[48:51], v[40:43]\n"
"v_mfma_f32_16x16x32_bf16 v[40:43], a[100:103], a[52:55], v[40:43]\n"
"ds_read_b128 a[216:219], v14 offset:6144\n"
"v_mfma_f32_16x16x32_bf16 v[44:47], a[108:111], a[52:55], v[44:47]\n"
"buffer_load_dwordx4 v3, s[24:27], s65 offen offset:3072 lds\n"
"v_mfma_f32_16x16x32_bf16 v[44:47], a[104:107], a[48:51], v[44:47]\n"
"ds_read_b128 a[220:223], v14 offset:7168\n"
"v_mfma_f32_16x16x32_bf16 v[48:51], a[112:115], a[48:51], v[48:51]\n"
"s_add_u32 m0, s76, 4096\n"
"v_mfma_f32_16x16x32_bf16 v[48:51], a[116:119], a[52:55], v[48:51]\n"
"ds_read_b128 a[136:139], v12 offset:2048\n"
"v_mfma_f32_16x16x32_bf16 v[52:55], a[124:127], a[52:55], v[52:55]\n"
"buffer_load_dwordx4 v2, s[24:27], s66 offen offset:0 lds\n"
"v_mfma_f32_16x16x32_bf16 v[52:55], a[120:123], a[48:51], v[52:55]\n"
"ds_read_b128 a[140:143], v13 offset:2048\n"
"v_mfma_f32_16x16x32_bf16 v[76:79], a[120:123], a[56:59], v[76:79]\n"
"v_mfma_f32_16x16x32_bf16 v[76:79], a[124:127], a[60:63], v[76:79]\n"
"ds_read_b128 a[144:147], v12 offset:4096\n"
"v_mfma_f32_16x16x32_bf16 v[72:75], a[116:119], a[60:63], v[72:75]\n"
"buffer_load_dwordx4 v3, s[24:27], s66 offen offset:1024 lds\n"
"v_mfma_f32_16x16x32_bf16 v[72:75], a[112:115], a[56:59], v[72:75]\n"
"ds_read_b128 a[148:151], v13 offset:4096\n"
"v_mfma_f32_16x16x32_bf16 v[60:63], a[104:107], a[56:59], v[60:63]\n"
"v_mfma_f32_16x16x32_bf16 v[60:63], a[108:111], a[60:63], v[60:63]\n"
"ds_read_b128 a[152:155], v12 offset:6144\n"
"v_mfma_f32_16x16x32_bf16 v[56:59], a[100:103], a[60:63], v[56:59]\n"
"v_mfma_f32_16x16x32_bf16 v[56:59], a[96:99], a[56:59], v[56:59]\n"
"ds_read_b128 a[156:159], v13 offset:6144\n"
"v_mfma_f32_16x16x32_bf16 v[80:83], a[96:99], a[64:67], v[80:83]\n"
"buffer_load_dwordx4 v2, s[24:27], s67 offen offset:2048 lds\n"
"v_add_u32 v2, 512, v2\n"
"v_mfma_f32_16x16x32_bf16 v[80:83], a[100:103], a[68:71], v[80:83]\n"
"ds_read_b128 a[160:163], v12 offset:8192\n"
"v_mfma_f32_16x16x32_bf16 v[84:87], a[108:111], a[68:71], v[84:87]\n"
"v_mfma_f32_16x16x32_bf16 v[84:87], a[104:107], a[64:67], v[84:87]\n"
"ds_read_b128 a[164:167], v13 offset:8192\n"
"v_mfma_f32_16x16x32_bf16 v[88:91], a[112:115], a[64:67], v[88:91]\n"
"buffer_load_dwordx4 v3, s[24:27], s67 offen offset:3072 lds\n"
"v_mfma_f32_16x16x32_bf16 v[88:91], a[116:119], a[68:71], v[88:91]\n"
"ds_read_b128 a[168:171], v12 offset:10240\n"
"v_add_u32 v3, 512, v3\n"
"v_mfma_f32_16x16x32_bf16 v[92:95], a[124:127], a[68:71], v[92:95]\n"
"v_mfma_f32_16x16x32_bf16 v[92:95], a[120:123], a[64:67], v[92:95]\n"
"ds_read_b128 a[172:175], v13 offset:10240\n"
"s_add_u32 m0, s77, 0\n"
"v_mfma_f32_16x16x32_bf16 v[116:119], a[120:123], a[72:75], v[116:119]\n"
"buffer_load_dwordx4 v4, s[56:59], 0 offen offset:0 sc1 lds\n"
"v_mfma_f32_16x16x32_bf16 v[116:119], a[124:127], a[76:79], v[116:119]\n"
"ds_read_b128 a[176:179], v12 offset:12288\n"
"v_mfma_f32_16x16x32_bf16 v[112:115], a[116:119], a[76:79], v[112:115]\n"
"v_mfma_f32_16x16x32_bf16 v[112:115], a[112:115], a[72:75], v[112:115]\n"
"ds_read_b128 a[180:183], v13 offset:12288\n"
"v_mfma_f32_16x16x32_bf16 v[108:111], a[104:107], a[72:75], v[108:111]\n"
"v_mfma_f32_16x16x32_bf16 v[108:111], a[108:111], a[76:79], v[108:111]\n"
"ds_read_b128 a[184:187], v12 offset:14336\n"
"v_mfma_f32_16x16x32_bf16 v[104:107], a[100:103], a[76:79], v[104:107]\n"
"buffer_load_dwordx4 v4, s[56:59], 0 offen offset:1024 sc1 lds\n"
"v_mfma_f32_16x16x32_bf16 v[104:107], a[96:99], a[72:75], v[104:107]\n"
"ds_read_b128 a[188:191], v13 offset:14336\n"
"v_mfma_f32_16x16x32_bf16 v[120:123], a[96:99], a[80:83], v[120:123]\n"
"v_mfma_f32_16x16x32_bf16 v[120:123], a[100:103], a[84:87], v[120:123]\n"
"v_mfma_f32_16x16x32_bf16 v[124:127], a[108:111], a[84:87], v[124:127]\n"
"buffer_load_dwordx4 v4, s[56:59], 0 offen offset:2048 sc1 lds\n"
"v_mfma_f32_16x16x32_bf16 v[124:127], a[104:107], a[80:83], v[124:127]\n"
"v_mfma_f32_16x16x32_bf16 v[136:139], a[112:115], a[80:83], v[136:139]\n"
"v_mfma_f32_16x16x32_bf16 v[136:139], a[116:119], a[84:87], v[136:139]\n"
"v_mfma_f32_16x16x32_bf16 v[140:143], a[124:127], a[84:87], v[140:143]\n"
"v_mfma_f32_16x16x32_bf16 v[140:143], a[120:123], a[80:83], v[140:143]\n"
"buffer_load_dwordx4 v4, s[56:59], 0 offen offset:3072 sc1 lds\n"
"v_mfma_f32_16x16x32_bf16 v[156:159], a[120:123], a[88:91], v[156:159]\n"
"v_add_u32 v4, 524288, v4\n"
"s_add_u32 s76, s76, s8\n"
"v_mfma_f32_16x16x32_bf16 v[156:159], a[124:127], a[92:95], v[156:159]\n"
"s_add_u32 s77, s77, s8\n"
"s_cmp_eq_u32 s7, 98304\n"
"s_cselect_b32 s8, 4294868992, s9\n"
"v_mfma_f32_16x16x32_bf16 v[152:155], a[116:119], a[92:95], v[152:155]\n"
"s_add_u32 s7, s7, s8\n"
"v_add_u32 v12, s8, v12\n"
"v_mfma_f32_16x16x32_bf16 v[152:155], a[112:115], a[88:91], v[152:155]\n"
"v_add_u32 v13, s8, v13\n"
"v_mfma_f32_16x16x32_bf16 v[148:151], a[104:107], a[88:91], v[148:151]\n"
"v_add_u32 v14, s8, v14\n"
"v_mfma_f32_16x16x32_bf16 v[148:151], a[108:111], a[92:95], v[148:151]\n"
"v_mfma_f32_16x16x32_bf16 v[144:147], a[100:103], a[92:95], v[144:147]\n"
"v_mfma_f32_16x16x32_bf16 v[144:147], a[96:99], a[88:91], v[144:147]\n"
"P1END_%=:\n"
"s_waitcnt lgkmcnt(14)\n"
"v_mfma_f32_16x16x32_bf16 a[224:227], a[192:195], a[128:131], a[224:227]\n"
"s_waitcnt vmcnt(12) lgkmcnt(0)\n"
"s_barrier\n"
"v_mfma_f32_16x16x32_bf16 a[224:227], a[196:199], a[132:135], a[224:227]\n"
"ds_read_b128 a[32:35], v12 offset:0\n"
"v_mfma_f32_16x16x32_bf16 a[228:231], a[204:207], a[132:135], a[228:231]\n"
"s_add_u32 m0, s76, 0\n"
"v_mfma_f32_16x16x32_bf16 a[228:231], a[200:203], a[128:131], a[228:231]\n"
"s_mul_i32 s14, s4, 56\n"
"ds_read_b128 a[36:39], v13 offset:0\n"
"v_mfma_f32_16x16x32_bf16 a[232:235], a[208:211], a[128:131], a[232:235]\n"
"buffer_load_dwordx4 v2, s[24:27], s64 offen offset:0 lds\n"
"s_mul_i32 s15, s83, 14\n"
"v_mfma_f32_16x16x32_bf16 a[232:235], a[212:215], a[132:135], a[232:235]\n"
"ds_read_b128 a[96:99], v14 offset:0\n"
"v_mfma_f32_16x16x32_bf16 a[236:239], a[220:223], a[132:135], a[236:239]\n"
"s_add_u32 s14, s14, s15\n"
"v_mfma_f32_16x16x32_bf16 a[236:239], a[216:219], a[128:131], a[236:239]\n"
"s_lshl_b32 s14, s14, 15\n"
"ds_read_b128 a[100:103], v14 offset:1024\n"
"v_mfma_f32_16x16x32_bf16 a[252:255], a[216:219], a[136:139], a[252:255]\n"
"buffer_load_dwordx4 v3, s[24:27], s64 offen offset:1024 lds\n"
"v_mfma_f32_16x16x32_bf16 a[252:255], a[220:223], a[140:143], a[252:255]\n"
"s_lshl_b32 s15, s69, 13\n"
"ds_read_b128 a[104:107], v14 offset:2048\n"
"v_mfma_f32_16x16x32_bf16 a[248:251], a[212:215], a[140:143], a[248:251]\n"
"s_add_u32 s14, s14, s15\n"
"v_mfma_f32_16x16x32_bf16 a[248:251], a[208:211], a[136:139], a[248:251]\n"
"ds_read_b128 a[108:111], v14 offset:3072\n"
"v_mfma_f32_16x16x32_bf16 a[244:247], a[200:203], a[136:139], a[244:247]\n"
"s_add_u32 s14, s14, 393216\n"
"buffer_load_dwordx4 v2, s[24:27], s65 offen offset:2048 lds\n"
"v_mfma_f32_16x16x32_bf16 a[244:247], a[204:207], a[140:143], a[244:247]\n"
"v_lshlrev_b32 v20, 4, v1\n"
"v_mfma_f32_16x16x32_bf16 a[240:243], a[196:199], a[140:143], a[240:243]\n"
"ds_read_b128 a[112:115], v14 offset:4096\n"
"v_mfma_f32_16x16x32_bf16 a[240:243], a[192:195], a[136:139], a[240:243]\n"
"v_add_u32 v5, s14, v20\n"
"v_mfma_f32_16x16x32_bf16 v[40:43], a[192:195], a[144:147], v[40:43]\n"
"ds_read_b128 a[116:119], v14 offset:5120\n"
"v_lshrrev_b32 v20, 4, v1\n"
"v_mfma_f32_16x16x32_bf16 v[40:43], a[196:199], a[148:151], v[40:43]\n"
"ds_read_b128 a[120:123], v14 offset:6144\n"
"v_mfma_f32_16x16x32_bf16 v[44:47], a[204:207], a[148:151], v[44:47]\n"
"buffer_load_dwordx4 v3, s[24:27], s65 offen offset:3072 lds\n"
"v_lshlrev_b32 v21, 1, v20\n"
"v_mfma_f32_16x16x32_bf16 v[44:47], a[200:203], a[144:147], v[44:47]\n"
"v_and_b32 v22, 15, v1\n"
"v_mfma_f32_16x16x32_bf16 v[48:51], a[208:211], a[144:147], v[48:51]\n"
"ds_read_b128 a[124:127], v14 offset:7168\n"
"s_add_u32 m0, s76, 4096\n"
"v_mfma_f32_16x16x32_bf16 v[48:51], a[212:215], a[148:151], v[48:51]\n"
"v_xor_b32 v21, v21, v22\n"
"v_mfma_f32_16x16x32_bf16 v[52:55], a[220:223], a[148:151], v[52:55]\n"
"ds_read_b128 a[40:43], v12 offset:2048\n"
"v_mfma_f32_16x16x32_bf16 v[52:55], a[216:219], a[144:147], v[52:55]\n"
"buffer_load_dwordx4 v2, s[24:27], s66 offen offset:0 lds\n"
"v_lshlrev_b32 v21, 4, v21\n"
"v_mfma_f32_16x16x32_bf16 v[76:79], a[216:219], a[152:155], v[76:79]\n"
"ds_read_b128 a[44:47], v13 offset:2048\n"
"s_mul_i32 s14, s83, 896\n"
"v_mfma_f32_16x16x32_bf16 v[76:79], a[220:223], a[156:159], v[76:79]\n"
"ds_read_b128 a[48:51], v12 offset:4096\n"
"v_mfma_f32_16x16x32_bf16 v[72:75], a[212:215], a[156:159], v[72:75]\n"
"s_lshl_b32 s15, s13, 4\n"
"buffer_load_dwordx4 v3, s[24:27], s66 offen offset:1024 lds\n"
"v_mfma_f32_16x16x32_bf16 v[72:75], a[208:211], a[152:155], v[72:75]\n"
"s_add_u32 s14, s14, s15\n"
"ds_read_b128 a[52:55], v13 offset:4096\n"
"v_mfma_f32_16x16x32_bf16 v[60:63], a[200:203], a[152:155], v[60:63]\n"
"v_mfma_f32_16x16x32_bf16 v[60:63], a[204:207], a[156:159], v[60:63]\n"
"v_add_u32 v20, s14, v20\n"
"v_mfma_f32_16x16x32_bf16 v[56:59], a[196:199], a[156:159], v[56:59]\n"
"ds_read_b128 a[56:59], v12 offset:6144\n"
"v_lshl_add_u32 v21, v20, 11, v21\n"
"v_mfma_f32_16x16x32_bf16 v[56:59], a[192:195], a[152:155], v[56:59]\n"
"ds_read_b128 a[60:63], v13 offset:6144\n"
"v_mfma_f32_16x16x32_bf16 v[80:83], a[192:195], a[160:163], v[80:83]\n"
"buffer_load_dwordx4 v2, s[24:27], s67 offen offset:2048 lds\n"
"s_lshl_b32 s14, s5, 8\n"
"v_mfma_f32_16x16x32_bf16 v[80:83], a[196:199], a[164:167], v[80:83]\n"
"v_add_u32 v2, 512, v2\n"
"v_add_u32 v11, s14, v21\n"
"v_mfma_f32_16x16x32_bf16 v[84:87], a[204:207], a[164:167], v[84:87]\n"
"ds_read_b128 a[64:67], v12 offset:8192\n"
"v_mfma_f32_16x16x32_bf16 v[84:87], a[200:203], a[160:163], v[84:87]\n"
"v_xor_b32 v15, 128, v11\n"
"v_mfma_f32_16x16x32_bf16 v[88:91], a[208:211], a[160:163], v[88:91]\n"
"ds_read_b128 a[68:71], v13 offset:8192\n"
"v_add_u32 v15, 7168, v15\n"
"v_mfma_f32_16x16x32_bf16 v[88:91], a[212:215], a[164:167], v[88:91]\n"
"buffer_load_dwordx4 v3, s[24:27], s67 offen offset:3072 lds\n"
"v_mfma_f32_16x16x32_bf16 v[92:95], a[220:223], a[164:167], v[92:95]\n"
"ds_read_b128 a[72:75], v12 offset:10240\n"
"v_add_u32 v3, 512, v3\n"
"v_mfma_f32_16x16x32_bf16 v[92:95], a[216:219], a[160:163], v[92:95]\n"
"s_lshl_b32 s14, s4, 5\n"
"ds_read_b128 a[76:79], v13 offset:10240\n"
"v_mfma_f32_16x16x32_bf16 v[116:119], a[216:219], a[168:171], v[116:119]\n"
"s_add_u32 m0, s77, 0\n"
"v_add_u32 v20, s14, v1\n"
"v_mfma_f32_16x16x32_bf16 v[116:119], a[220:223], a[172:175], v[116:119]\n"
"buffer_load_dwordx4 v4, s[56:59], 0 offen offset:0 sc1 lds\n"
"v_lshlrev_b32 v20, 7, v20\n"
"v_mfma_f32_16x16x32_bf16 v[112:115], a[212:215], a[172:175], v[112:115]\n"
"ds_read_b128 a[80:83], v12 offset:12288\n"
"v_mfma_f32_16x16x32_bf16 v[112:115], a[208:211], a[168:171], v[112:115]\n"
"s_mul_i32 s15, s83, 7\n"
"ds_read_b128 a[84:87], v13 offset:12288\n"
"v_mfma_f32_16x16x32_bf16 v[108:111], a[200:203], a[168:171], v[108:111]\n"
"s_add_u32 s14, s14, s15\n"
"v_mfma_f32_16x16x32_bf16 v[108:111], a[204:207], a[172:175], v[108:111]\n"
"ds_read_b128 a[88:91], v12 offset:14336\n"
"v_mfma_f32_16x16x32_bf16 v[104:107], a[196:199], a[172:175], v[104:107]\n"
"buffer_load_dwordx4 v4, s[56:59], 0 offen offset:1024 sc1 lds\n"
"s_add_u32 s14, s14, 8192\n"
"v_mfma_f32_16x16x32_bf16 v[104:107], a[192:195], a[168:171], v[104:107]\n"
"v_and_b32 v21, 7, v1\n"
"v_mfma_f32_16x16x32_bf16 v[120:123], a[192:195], a[176:179], v[120:123]\n"
"ds_read_b128 a[92:95], v13 offset:14336\n"
"v_mfma_f32_16x16x32_bf16 v[120:123], a[196:199], a[180:183], v[120:123]\n"
"v_min_u32 v21, 6, v21\n"
"v_mfma_f32_16x16x32_bf16 v[124:127], a[204:207], a[180:183], v[124:127]\n"
"v_add_lshl_u32 v21, v21, s14, 2\n"
"v_mfma_f32_16x16x32_bf16 v[124:127], a[200:203], a[176:179], v[124:127]\n"
"buffer_load_dwordx4 v4, s[56:59], 0 offen offset:2048 sc1 lds\n"
"v_mfma_f32_16x16x32_bf16 v[136:139], a[208:211], a[176:179], v[136:139]\n"
"v_cmp_gt_u32 vcc, 32, v1\n"
"v_mfma_f32_16x16x32_bf16 v[136:139], a[212:215], a[180:183], v[136:139]\n"
"s_nop 1\n"
"v_mfma_f32_16x16x32_bf16 v[140:143], a[220:223], a[180:183], v[140:143]\n"
"v_mfma_f32_16x16x32_bf16 v[140:143], a[216:219], a[176:179], v[140:143]\n"
"v_cndmask_b32 v30, v21, v20, vcc\n"
"v_mfma_f32_16x16x32_bf16 v[156:159], a[216:219], a[184:187], v[156:159]\n"
"buffer_load_dwordx4 v4, s[56:59], 0 offen offset:3072 sc1 lds\n"
"v_add_u32 v4, 524288, v4\n"
"v_mfma_f32_16x16x32_bf16 v[156:159], a[220:223], a[188:191], v[156:159]\n"
"s_add_u32 s76, s76, s8\n"
"s_add_u32 s77, s77, s8\n"
"s_cmp_eq_u32 s7, 98304\n"
"v_mfma_f32_16x16x32_bf16 v[152:155], a[212:215], a[188:191], v[152:155]\n"
"s_cselect_b32 s8, 4294868992, s9\n"
"s_add_u32 s7, s7, s8\n"
"v_add_u32 v12, s8, v12\n"
"v_mfma_f32_16x16x32_bf16 v[152:155], a[208:211], a[184:187], v[152:155]\n"
"v_add_u32 v13, s8, v13\n"
"v_mfma_f32_16x16x32_bf16 v[148:151], a[200:203], a[184:187], v[148:151]\n"
"v_add_u32 v14, s8, v14\n"
"v_mfma_f32_16x16x32_bf16 v[148:151], a[204:207], a[188:191], v[148:151]\n"
"v_mfma_f32_16x16x32_bf16 v[144:147], a[196:199], a[188:191], v[144:147]\n"
"v_mfma_f32_16x16x32_bf16 v[144:147], a[192:195], a[184:187], v[144:147]\n"
"s_waitcnt lgkmcnt(14)\n"
"v_mfma_f32_16x16x32_bf16 a[224:227], a[96:99], a[32:35], a[224:227]\n"
"s_waitcnt vmcnt(12) lgkmcnt(0)\n"
"s_barrier\n"
"v_mfma_f32_16x16x32_bf16 a[224:227], a[100:103], a[36:39], a[224:227]\n"
"buffer_load_dword v31, v30, s[56:59], 0 offen offset:0 sc1\n"
"v_mfma_f32_16x16x32_bf16 a[228:231], a[108:111], a[36:39], a[228:231]\n"
"ds_read_b128 a[128:131], v12 offset:0\n"
"s_add_u32 m0, s76, 0\n"
"v_mfma_f32_16x16x32_bf16 a[228:231], a[104:107], a[32:35], a[228:231]\n"
"ds_read_b128 a[132:135], v13 offset:0\n"
"v_mfma_f32_16x16x32_bf16 a[232:235], a[112:115], a[32:35], a[232:235]\n"
"buffer_load_dwordx4 v2, s[24:27], s64 offen offset:0 lds\n"
"v_mfma_f32_16x16x32_bf16 a[232:235], a[116:119], a[36:39], a[232:235]\n"
"ds_read_b128 a[192:195], v14 offset:0\n"
"v_mfma_f32_16x16x32_bf16 a[236:239], a[124:127], a[36:39], a[236:239]\n"
"v_mfma_f32_16x16x32_bf16 a[236:239], a[120:123], a[32:35], a[236:239]\n"
"ds_read_b128 a[196:199], v14 offset:1024\n"
"v_mfma_f32_16x16x32_bf16 a[252:255], a[120:123], a[40:43], a[252:255]\n"
"buffer_load_dwordx4 v3, s[24:27], s64 offen offset:1024 lds\n"
"v_mfma_f32_16x16x32_bf16 a[252:255], a[124:127], a[44:47], a[252:255]\n"
"ds_read_b128 a[200:203], v14 offset:2048\n"
"v_mfma_f32_16x16x32_bf16 a[248:251], a[116:119], a[44:47], a[248:251]\n"
"v_mfma_f32_16x16x32_bf16 a[248:251], a[112:115], a[40:43], a[248:251]\n"
"ds_read_b128 a[204:207], v14 offset:3072\n"
"v_mfma_f32_16x16x32_bf16 a[244:247], a[104:107], a[40:43], a[244:247]\n"
"buffer_load_dwordx4 v2, s[24:27], s65 offen offset:2048 lds\n"
"v_mfma_f32_16x16x32_bf16 a[244:247], a[108:111], a[44:47], a[244:247]\n"
"ds_read_b128 a[208:211], v14 offset:4096\n"
"v_mfma_f32_16x16x32_bf16 a[240:243], a[100:103], a[44:47], a[240:243]\n"
"v_mfma_f32_16x16x32_bf16 a[240:243], a[96:99], a[40:43], a[240:243]\n"
"ds_read_b128 a[212:215], v14 offset:5120\n"
"v_mfma_f32_16x16x32_bf16 v[40:43], a[96:99], a[48:51], v[40:43]\n"
"v_mfma_f32_16x16x32_bf16 v[40:43], a[100:103], a[52:55], v[40:43]\n"
"ds_read_b128 a[216:219], v14 offset:6144\n"
"v_mfma_f32_16x16x32_bf16 v[44:47], a[108:111], a[52:55], v[44:47]\n"
"buffer_load_dwordx4 v3, s[24:27], s65 offen offset:3072 lds\n"
"v_mfma_f32_16x16x32_bf16 v[44:47], a[104:107], a[48:51], v[44:47]\n"
"ds_read_b128 a[220:223], v14 offset:7168\n"
"v_mfma_f32_16x16x32_bf16 v[48:51], a[112:115], a[48:51], v[48:51]\n"
"s_add_u32 m0, s76, 4096\n"
"v_mfma_f32_16x16x32_bf16 v[48:51], a[116:119], a[52:55], v[48:51]\n"
"ds_read_b128 a[136:139], v12 offset:2048\n"
"v_mfma_f32_16x16x32_bf16 v[52:55], a[124:127], a[52:55], v[52:55]\n"
"buffer_load_dwordx4 v2, s[24:27], s66 offen offset:0 lds\n"
"v_mfma_f32_16x16x32_bf16 v[52:55], a[120:123], a[48:51], v[52:55]\n"
"ds_read_b128 a[140:143], v13 offset:2048\n"
"v_mfma_f32_16x16x32_bf16 v[76:79], a[120:123], a[56:59], v[76:79]\n"
"v_mfma_f32_16x16x32_bf16 v[76:79], a[124:127], a[60:63], v[76:79]\n"
"ds_read_b128 a[144:147], v12 offset:4096\n"
"v_mfma_f32_16x16x32_bf16 v[72:75], a[116:119], a[60:63], v[72:75]\n"
"buffer_load_dwordx4 v3, s[24:27], s66 offen offset:1024 lds\n"
"v_mfma_f32_16x16x32_bf16 v[72:75], a[112:115], a[56:59], v[72:75]\n"
"ds_read_b128 a[148:151], v13 offset:4096\n"
"v_mfma_f32_16x16x32_bf16 v[60:63], a[104:107], a[56:59], v[60:63]\n"
"v_mfma_f32_16x16x32_bf16 v[60:63], a[108:111], a[60:63], v[60:63]\n"
"ds_read_b128 a[152:155], v12 offset:6144\n"
"v_mfma_f32_16x16x32_bf16 v[56:59], a[100:103], a[60:63], v[56:59]\n"
"v_mfma_f32_16x16x32_bf16 v[56:59], a[96:99], a[56:59], v[56:59]\n"
"ds_read_b128 a[156:159], v13 offset:6144\n"
"v_mfma_f32_16x16x32_bf16 v[80:83], a[96:99], a[64:67], v[80:83]\n"
"buffer_load_dwordx4 v2, s[24:27], s67 offen offset:2048 lds\n"
"v_subrev_u32 v2, 1408, v2\n"
"v_mfma_f32_16x16x32_bf16 v[80:83], a[100:103], a[68:71], v[80:83]\n"
"ds_read_b128 a[160:163], v12 offset:8192\n"
"v_mfma_f32_16x16x32_bf16 v[84:87], a[108:111], a[68:71], v[84:87]\n"
"v_mfma_f32_16x16x32_bf16 v[84:87], a[104:107], a[64:67], v[84:87]\n"
"ds_read_b128 a[164:167], v13 offset:8192\n"
"v_mfma_f32_16x16x32_bf16 v[88:91], a[112:115], a[64:67], v[88:91]\n"
"buffer_load_dwordx4 v3, s[24:27], s67 offen offset:3072 lds\n"
"v_mfma_f32_16x16x32_bf16 v[88:91], a[116:119], a[68:71], v[88:91]\n"
"ds_read_b128 a[168:171], v12 offset:10240\n"
"v_subrev_u32 v3, 1408, v3\n"
"v_mfma_f32_16x16x32_bf16 v[92:95], a[124:127], a[68:71], v[92:95]\n"
"v_mfma_f32_16x16x32_bf16 v[92:95], a[120:123], a[64:67], v[92:95]\n"
"ds_read_b128 a[172:175], v13 offset:10240\n"
"s_add_u32 m0, s77, 0\n"
"v_mfma_f32_16x16x32_bf16 v[116:119], a[120:123], a[72:75], v[116:119]\n"
"buffer_load_dwordx4 v4, s[56:59], 0 offen offset:0 sc1 lds\n"
"v_mfma_f32_16x16x32_bf16 v[116:119], a[124:127], a[76:79], v[116:119]\n"
"ds_read_b128 a[176:179], v12 offset:12288\n"
"v_mfma_f32_16x16x32_bf16 v[112:115], a[116:119], a[76:79], v[112:115]\n"
"v_mfma_f32_16x16x32_bf16 v[112:115], a[112:115], a[72:75], v[112:115]\n"
"ds_read_b128 a[180:183], v13 offset:12288\n"
"v_mfma_f32_16x16x32_bf16 v[108:111], a[104:107], a[72:75], v[108:111]\n"
"v_mfma_f32_16x16x32_bf16 v[108:111], a[108:111], a[76:79], v[108:111]\n"
"ds_read_b128 a[184:187], v12 offset:14336\n"
"v_mfma_f32_16x16x32_bf16 v[104:107], a[100:103], a[76:79], v[104:107]\n"
"buffer_load_dwordx4 v4, s[56:59], 0 offen offset:1024 sc1 lds\n"
"v_mfma_f32_16x16x32_bf16 v[104:107], a[96:99], a[72:75], v[104:107]\n"
"ds_read_b128 a[188:191], v13 offset:14336\n"
"v_mfma_f32_16x16x32_bf16 v[120:123], a[96:99], a[80:83], v[120:123]\n"
"v_mfma_f32_16x16x32_bf16 v[120:123], a[100:103], a[84:87], v[120:123]\n"
"v_mfma_f32_16x16x32_bf16 v[124:127], a[108:111], a[84:87], v[124:127]\n"
"buffer_load_dwordx4 v4, s[56:59], 0 offen offset:2048 sc1 lds\n"
"v_mfma_f32_16x16x32_bf16 v[124:127], a[104:107], a[80:83], v[124:127]\n"
"v_mfma_f32_16x16x32_bf16 v[136:139], a[112:115], a[80:83], v[136:139]\n"
"v_mfma_f32_16x16x32_bf16 v[136:139], a[116:119], a[84:87], v[136:139]\n"
"v_mfma_f32_16x16x32_bf16 v[140:143], a[124:127], a[84:87], v[140:143]\n"
"v_mfma_f32_16x16x32_bf16 v[140:143], a[120:123], a[80:83], v[140:143]\n"
"buffer_load_dwordx4 v4, s[56:59], 0 offen offset:3072 sc1 lds\n"
"v_mfma_f32_16x16x32_bf16 v[156:159], a[120:123], a[88:91], v[156:159]\n"
"v_subrev_u32 v4, 1441792, v4\n"
"s_add_u32 s76, s76, s8\n"
"v_mfma_f32_16x16x32_bf16 v[156:159], a[124:127], a[92:95], v[156:159]\n"
"s_add_u32 s77, s77, s8\n"
"s_cmp_eq_u32 s7, 98304\n"
"s_cselect_b32 s8, 4294868992, s9\n"
"v_mfma_f32_16x16x32_bf16 v[152:155], a[116:119], a[92:95], v[152:155]\n"
"s_add_u32 s7, s7, s8\n"
"v_add_u32 v12, s8, v12\n"
"v_mfma_f32_16x16x32_bf16 v[152:155], a[112:115], a[88:91], v[152:155]\n"
"v_add_u32 v13, s8, v13\n"
"v_mfma_f32_16x16x32_bf16 v[148:151], a[104:107], a[88:91], v[148:151]\n"
"v_add_u32 v14, s8, v14\n"
"v_mfma_f32_16x16x32_bf16 v[148:151], a[108:111], a[92:95], v[148:151]\n"
"v_mfma_f32_16x16x32_bf16 v[144:147], a[100:103], a[92:95], v[144:147]\n"
"v_mfma_f32_16x16x32_bf16 v[144:147], a[96:99], a[88:91], v[144:147]\n"
"s_waitcnt lgkmcnt(14)\n"
"v_mfma_f32_16x16x32_bf16 a[224:227], a[192:195], a[128:131], a[224:227]\n"
"s_waitcnt vmcnt(13) lgkmcnt(0)\n"
"s_barrier\n"
"s_waitcnt vmcnt(12)\n"
"v_mfma_f32_16x16x32_bf16 a[224:227], a[196:199], a[132:135], a[224:227]\n"
"v_cmp_ne_u32 vcc, 1, v31\n"
"v_mfma_f32_16x16x32_bf16 a[228:231], a[204:207], a[132:135], a[228:231]\n"
"s_nop 4\n"
"s_cbranch_vccnz PL23_%=\n"
"PL23d_%=:\n"
"v_mfma_f32_16x16x32_bf16 a[228:231], a[200:203], a[128:131], a[228:231]\n"
"ds_read_b128 a[32:35], v12 offset:0\n"
"s_add_u32 m0, s76, 0\n"
"v_mfma_f32_16x16x32_bf16 a[232:235], a[208:211], a[128:131], a[232:235]\n"
"ds_read_b128 a[36:39], v13 offset:0\n"
"v_mfma_f32_16x16x32_bf16 a[232:235], a[212:215], a[132:135], a[232:235]\n"
"buffer_load_dwordx4 v5, s[56:59], s10 offen offset:0 sc1 lds\n"
"v_mfma_f32_16x16x32_bf16 a[236:239], a[220:223], a[132:135], a[236:239]\n"
"ds_read_b128 a[96:99], v14 offset:0\n"
"v_mfma_f32_16x16x32_bf16 a[236:239], a[216:219], a[128:131], a[236:239]\n"
"ds_read_b128 a[100:103], v14 offset:1024\n"
"v_mfma_f32_16x16x32_bf16 a[252:255], a[216:219], a[136:139], a[252:255]\n"
"buffer_load_dwordx4 v5, s[56:59], s10 offen offset:1024 sc1 lds\n"
"v_mfma_f32_16x16x32_bf16 a[252:255], a[220:223], a[140:143], a[252:255]\n"
"ds_read_b128 a[104:107], v14 offset:2048\n"
"v_mfma_f32_16x16x32_bf16 a[248:251], a[212:215], a[140:143], a[248:251]\n"
"v_mfma_f32_16x16x32_bf16 a[248:251], a[208:211], a[136:139], a[248:251]\n"
"ds_read_b128 a[108:111], v14 offset:3072\n"
"v_mfma_f32_16x16x32_bf16 a[244:247], a[200:203], a[136:139], a[244:247]\n"
"buffer_load_dwordx4 v5, s[56:59], s10 offen offset:2048 sc1 lds\n"
"v_mfma_f32_16x16x32_bf16 a[244:247], a[204:207], a[140:143], a[244:247]\n"
"ds_read_b128 a[112:115], v14 offset:4096\n"
"v_mfma_f32_16x16x32_bf16 a[240:243], a[196:199], a[140:143], a[240:243]\n"
"v_mfma_f32_16x16x32_bf16 a[240:243], a[192:195], a[136:139], a[240:243]\n"
"ds_read_b128 a[116:119], v14 offset:5120\n"
"v_mfma_f32_16x16x32_bf16 v[40:43], a[192:195], a[144:147], v[40:43]\n"
"v_mfma_f32_16x16x32_bf16 v[40:43], a[196:199], a[148:151], v[40:43]\n"
"ds_read_b128 a[120:123], v14 offset:6144\n"
"v_mfma_f32_16x16x32_bf16 v[44:47], a[204:207], a[148:151], v[44:47]\n"
"buffer_load_dwordx4 v5, s[56:59], s10 offen offset:3072 sc1 lds\n"
"v_mfma_f32_16x16x32_bf16 v[44:47], a[200:203], a[144:147], v[44:47]\n"
"v_bfe_u32 v2, v1, 2, 2\n"
"v_mfma_f32_16x16x32_bf16 v[48:51], a[208:211], a[144:147], v[48:51]\n"
"ds_read_b128 a[124:127], v14 offset:7168\n"
"v_and_b32 v3, 3, v1\n"
"v_mfma_f32_16x16x32_bf16 v[48:51], a[212:215], a[148:151], v[48:51]\n"
"s_add_u32 m0, s76, 4096\n"
"v_lshl_add_u32 v2, v2, 3, v3\n"
"v_mfma_f32_16x16x32_bf16 v[52:55], a[220:223], a[148:151], v[52:55]\n"
"ds_read_b128 a[40:43], v12 offset:2048\n"
"v_mfma_f32_16x16x32_bf16 v[52:55], a[216:219], a[144:147], v[52:55]\n"
"buffer_load_dwordx4 v5, s[56:59], s11 offen offset:0 sc1 lds\n"
"s_lshl_b32 s14, s4, 8\n"
"v_mfma_f32_16x16x32_bf16 v[76:79], a[216:219], a[152:155], v[76:79]\n"
"s_and_b32 s15, s13, 2\n"
"ds_read_b128 a[44:47], v13 offset:2048\n"
"v_mfma_f32_16x16x32_bf16 v[76:79], a[220:223], a[156:159], v[76:79]\n"
"s_lshl_b32 s15, s15, 6\n"
"ds_read_b128 a[48:51], v12 offset:4096\n"
"v_mfma_f32_16x16x32_bf16 v[72:75], a[212:215], a[156:159], v[72:75]\n"
"s_add_u32 s14, s14, s15\n"
"buffer_load_dwordx4 v5, s[56:59], s11 offen offset:1024 sc1 lds\n"
"v_mfma_f32_16x16x32_bf16 v[72:75], a[208:211], a[152:155], v[72:75]\n"
"v_add_u32 v2, s14, v2\n"
"v_mfma_f32_16x16x32_bf16 v[60:63], a[200:203], a[152:155], v[60:63]\n"
"ds_read_b128 a[52:55], v13 offset:4096\n"
"v_lshlrev_b32 v2, 2, v2\n"
"v_mfma_f32_16x16x32_bf16 v[60:63], a[204:207], a[156:159], v[60:63]\n"
"ds_read_b128 a[56:59], v12 offset:6144\n"
"v_mfma_f32_16x16x32_bf16 v[56:59], a[196:199], a[156:159], v[56:59]\n"
"s_xor_b32 s15, s83, 0\n"
"v_mfma_f32_16x16x32_bf16 v[56:59], a[192:195], a[152:155], v[56:59]\n"
"s_lshl_b32 s15, s15, 7\n"
"ds_read_b128 a[60:63], v13 offset:6144\n"
"v_mfma_f32_16x16x32_bf16 v[80:83], a[192:195], a[160:163], v[80:83]\n"
"buffer_load_dwordx4 v5, s[56:59], s11 offen offset:2048 sc1 lds\n"
"s_add_u32 s15, s15, 65536\n"
"v_mfma_f32_16x16x32_bf16 v[80:83], a[196:199], a[164:167], v[80:83]\n"
"buffer_load_dword v16, v2, s[56:59], s15 offen offset:0 sc1\n"
"v_mfma_f32_16x16x32_bf16 v[84:87], a[204:207], a[164:167], v[84:87]\n"
"ds_read_b128 a[64:67], v12 offset:8192\n"
"v_mfma_f32_16x16x32_bf16 v[84:87], a[200:203], a[160:163], v[84:87]\n"
"buffer_load_dword v18, v2, s[56:59], s15 offen offset:16 sc1\n"
"v_mfma_f32_16x16x32_bf16 v[88:91], a[208:211], a[160:163], v[88:91]\n"
"ds_read_b128 a[68:71], v13 offset:8192\n"
"s_xor_b32 s15, s83, 1\n"
"v_mfma_f32_16x16x32_bf16 v[88:91], a[212:215], a[164:167], v[88:91]\n"
"buffer_load_dwordx4 v5, s[56:59], s11 offen offset:3072 sc1 lds\n"
"s_lshl_b32 s15, s15, 7\n"
"v_mfma_f32_16x16x32_bf16 v[92:95], a[220:223], a[164:167], v[92:95]\n"
"ds_read_b128 a[72:75], v12 offset:10240\n"
"v_add_u32 v5, 0x8000, v5\n"
"v_mfma_f32_16x16x32_bf16 v[92:95], a[216:219], a[160:163], v[92:95]\n"
"s_add_u32 s15, s15, 65536\n"
"ds_read_b128 a[76:79], v13 offset:10240\n"
"v_mfma_f32_16x16x32_bf16 v[116:119], a[216:219], a[168:171], v[116:119]\n"
"s_add_u32 m0, s77, 0\n"
"v_mfma_f32_16x16x32_bf16 v[116:119], a[220:223], a[172:175], v[116:119]\n"
"buffer_load_dword v20, v2, s[56:59], s15 offen offset:0 sc1\n"
"v_mfma_f32_16x16x32_bf16 v[112:115], a[212:215], a[172:175], v[112:115]\n"
"buffer_load_dwordx4 v11, s[60:63], 0 offen offset:0 lds\n"
"v_mfma_f32_16x16x32_bf16 v[112:115], a[208:211], a[168:171], v[112:115]\n"
"buffer_load_dword v22, v2, s[56:59], s15 offen offset:16 sc1\n"
"v_mfma_f32_16x16x32_bf16 v[108:111], a[200:203], a[168:171], v[108:111]\n"
"ds_read_b128 a[80:83], v12 offset:12288\n"
"s_xor_b32 s15, s83, 2\n"
"v_mfma_f32_16x16x32_bf16 v[108:111], a[204:207], a[172:175], v[108:111]\n"
"s_lshl_b32 s15, s15, 7\n"
"ds_read_b128 a[84:87], v13 offset:12288\n"
"v_mfma_f32_16x16x32_bf16 v[104:107], a[196:199], a[172:175], v[104:107]\n"
"s_add_u32 s15, s15, 65536\n"
"ds_read_b128 a[88:91], v12 offset:14336\n"
"v_mfma_f32_16x16x32_bf16 v[104:107], a[192:195], a[168:171], v[104:107]\n"
"buffer_load_dwordx4 v15, s[60:63], 0 offen offset:1024 lds\n"
"v_mfma_f32_16x16x32_bf16 v[120:123], a[192:195], a[176:179], v[120:123]\n"
"buffer_load_dword v24, v2, s[56:59], s15 offen offset:0 sc1\n"
"v_mfma_f32_16x16x32_bf16 v[120:123], a[196:199], a[180:183], v[120:123]\n"
"buffer_load_dword v26, v2, s[56:59], s15 offen offset:16 sc1\n"
"v_mfma_f32_16x16x32_bf16 v[124:127], a[204:207], a[180:183], v[124:127]\n"
"ds_read_b128 a[92:95], v13 offset:14336\n"
"s_xor_b32 s15, s83, 3\n"
"v_mfma_f32_16x16x32_bf16 v[124:127], a[200:203], a[176:179], v[124:127]\n"
"s_lshl_b32 s15, s15, 7\n"
"buffer_load_dwordx4 v11, s[60:63], s68 offen offset:2048 lds\n"
"v_mfma_f32_16x16x32_bf16 v[136:139], a[208:211], a[176:179], v[136:139]\n"
"s_add_u32 s15, s15, 65536\n"
"v_add_u32 v11, 0x20000, v11\n"
"v_mfma_f32_16x16x32_bf16 v[136:139], a[212:215], a[180:183], v[136:139]\n"
"buffer_load_dword v28, v2, s[56:59], s15 offen offset:0 sc1\n"
"v_mfma_f32_16x16x32_bf16 v[140:143], a[220:223], a[180:183], v[140:143]\n"
"buffer_load_dword v30, v2, s[56:59], s15 offen offset:16 sc1\n"
"v_mfma_f32_16x16x32_bf16 v[140:143], a[216:219], a[176:179], v[140:143]\n"
"buffer_load_dwordx4 v15, s[60:63], s68 offen offset:3072 lds\n"
"v_mfma_f32_16x16x32_bf16 v[156:159], a[216:219], a[184:187], v[156:159]\n"
"v_add_u32 v15, 0x20000, v15\n"
"s_add_u32 s76, s76, s8\n"
"v_mfma_f32_16x16x32_bf16 v[156:159], a[220:223], a[188:191], v[156:159]\n"
"s_add_u32 s77, s77, s8\n"
"s_cmp_eq_u32 s7, 98304\n"
"s_cselect_b32 s8, 4294868992, s9\n"
"v_mfma_f32_16x16x32_bf16 v[152:155], a[212:215], a[188:191], v[152:155]\n"
"s_add_u32 s7, s7, s8\n"
"v_add_u32 v12, s8, v12\n"
"v_mfma_f32_16x16x32_bf16 v[152:155], a[208:211], a[184:187], v[152:155]\n"
"v_add_u32 v13, s8, v13\n"
"v_mfma_f32_16x16x32_bf16 v[148:151], a[200:203], a[184:187], v[148:151]\n"
"v_add_u32 v14, s8, v14\n"
"v_mfma_f32_16x16x32_bf16 v[148:151], a[204:207], a[188:191], v[148:151]\n"
"v_mfma_f32_16x16x32_bf16 v[144:147], a[196:199], a[188:191], v[144:147]\n"
"v_mfma_f32_16x16x32_bf16 v[144:147], a[192:195], a[184:187], v[144:147]\n"
"s_waitcnt lgkmcnt(14)\n"
"v_mfma_f32_16x16x32_bf16 a[224:227], a[96:99], a[32:35], a[224:227]\n"
"s_waitcnt vmcnt(20) lgkmcnt(0)\n"
"s_barrier\n"
"v_mfma_f32_16x16x32_bf16 a[224:227], a[100:103], a[36:39], a[224:227]\n"
"ds_read_b128 a[128:131], v12 offset:0\n"
"v_mfma_f32_16x16x32_bf16 a[228:231], a[108:111], a[36:39], a[228:231]\n"
"s_add_u32 m0, s76, 0\n"
"v_mfma_f32_16x16x32_bf16 a[228:231], a[104:107], a[32:35], a[228:231]\n"
"s_lshr_b32 s14, s13, 1\n"
"ds_read_b128 a[132:135], v13 offset:0\n"
"v_mfma_f32_16x16x32_bf16 a[232:235], a[112:115], a[32:35], a[232:235]\n"
"buffer_load_dwordx4 v5, s[56:59], s10 offen offset:0 sc1 lds\n"
"s_lshl_b32 s14, s14, 14\n"
"v_mfma_f32_16x16x32_bf16 a[232:235], a[116:119], a[36:39], a[232:235]\n"
"ds_read_b128 a[192:195], v14 offset:0\n"
"v_mfma_f32_16x16x32_bf16 a[236:239], a[124:127], a[36:39], a[236:239]\n"
"s_add_u32 s14, s14, 49152\n"
"v_mfma_f32_16x16x32_bf16 a[236:239], a[120:123], a[32:35], a[236:239]\n"
"ds_read_b128 a[196:199], v14 offset:1024\n"
"v_mfma_f32_16x16x32_bf16 a[252:255], a[120:123], a[40:43], a[252:255]\n"
"buffer_load_dwordx4 v5, s[56:59], s10 offen offset:1024 sc1 lds\n"
"v_lshlrev_b32 v2, 4, v1\n"
"v_mfma_f32_16x16x32_bf16 a[252:255], a[124:127], a[44:47], a[252:255]\n"
"ds_read_b128 a[200:203], v14 offset:2048\n"
"v_mfma_f32_16x16x32_bf16 a[248:251], a[116:119], a[44:47], a[248:251]\n"
"v_add_u32 v6, s14, v2\n"
"v_mfma_f32_16x16x32_bf16 a[248:251], a[112:115], a[40:43], a[248:251]\n"
"ds_read_b128 a[204:207], v14 offset:3072\n"
"v_mfma_f32_16x16x32_bf16 a[244:247], a[104:107], a[40:43], a[244:247]\n"
"s_and_b32 s14, s13, 1\n"
"buffer_load_dwordx4 v5, s[56:59], s10 offen offset:2048 sc1 lds\n"
"v_mfma_f32_16x16x32_bf16 a[244:247], a[108:111], a[44:47], a[244:247]\n"
"ds_read_b128 a[208:211], v14 offset:4096\n"
"v_mfma_f32_16x16x32_bf16 a[240:243], a[100:103], a[44:47], a[240:243]\n"
"s_lshl_b32 s14, s14, 2\n"
"v_mfma_f32_16x16x32_bf16 a[240:243], a[96:99], a[40:43], a[240:243]\n"
"ds_read_b128 a[212:215], v14 offset:5120\n"
"v_mfma_f32_16x16x32_bf16 v[40:43], a[96:99], a[48:51], v[40:43]\n"
"v_lshrrev_b32 v2, 4, v1\n"
"v_mfma_f32_16x16x32_bf16 v[40:43], a[100:103], a[52:55], v[40:43]\n"
"ds_read_b128 a[216:219], v14 offset:6144\n"
"v_mfma_f32_16x16x32_bf16 v[44:47], a[108:111], a[52:55], v[44:47]\n"
"buffer_load_dwordx4 v5, s[56:59], s10 offen offset:3072 sc1 lds\n"
"v_bfe_u32 v3, v1, 2, 2\n"
"v_mfma_f32_16x16x32_bf16 v[44:47], a[104:107], a[48:51], v[44:47]\n"
"ds_read_b128 a[220:223], v14 offset:7168\n"
"v_mfma_f32_16x16x32_bf16 v[48:51], a[112:115], a[48:51], v[48:51]\n"
"v_lshl_add_u32 v2, v2, 2, v3\n"
"s_add_u32 m0, s76, 4096\n"
"v_mfma_f32_16x16x32_bf16 v[48:51], a[116:119], a[52:55], v[48:51]\n"
"ds_read_b128 a[136:139], v12 offset:2048\n"
"v_mfma_f32_16x16x32_bf16 v[52:55], a[124:127], a[52:55], v[52:55]\n"
"buffer_load_dwordx4 v5, s[56:59], s11 offen offset:0 sc1 lds\n"
"v_lshlrev_b32 v2, 8, v2\n"
"v_mfma_f32_16x16x32_bf16 v[52:55], a[120:123], a[48:51], v[52:55]\n"
"ds_read_b128 a[140:143], v13 offset:2048\n"
"v_mfma_f32_16x16x32_bf16 v[76:79], a[120:123], a[56:59], v[76:79]\n"
"v_and_b32 v4, 3, v1\n"
"v_mfma_f32_16x16x32_bf16 v[76:79], a[124:127], a[60:63], v[76:79]\n"
"ds_read_b128 a[144:147], v12 offset:4096\n"
"v_mfma_f32_16x16x32_bf16 v[72:75], a[116:119], a[60:63], v[72:75]\n"
"v_lshl_add_u32 v2, v4, 3, v2\n"
"v_mfma_f32_16x16x32_bf16 v[72:75], a[112:115], a[56:59], v[72:75]\n"
"buffer_load_dwordx4 v5, s[56:59], s11 offen offset:1024 sc1 lds\n"
"v_add_u32 v2, 81920, v2\n"
"v_mfma_f32_16x16x32_bf16 v[60:63], a[104:107], a[56:59], v[60:63]\n"
"ds_read_b128 a[148:151], v13 offset:4096\n"
"v_mfma_f32_16x16x32_bf16 v[60:63], a[108:111], a[60:63], v[60:63]\n"
"v_bfe_u32 v3, v1, 2, 3\n"
"v_mfma_f32_16x16x32_bf16 v[56:59], a[100:103], a[60:63], v[56:59]\n"
"ds_read_b128 a[152:155], v12 offset:6144\n"
"v_mfma_f32_16x16x32_bf16 v[56:59], a[96:99], a[56:59], v[56:59]\n"
"v_xor_b32 v3, s14, v3\n"
"v_mfma_f32_16x16x32_bf16 v[80:83], a[96:99], a[64:67], v[80:83]\n"
"ds_read_b128 a[156:159], v13 offset:6144\n"
"v_mfma_f32_16x16x32_bf16 v[80:83], a[100:103], a[68:71], v[80:83]\n"
"buffer_load_dwordx4 v5, s[56:59], s11 offen offset:2048 sc1 lds\n"
"v_xor_b32 v4, 0, v3\n"
"v_mfma_f32_16x16x32_bf16 v[84:87], a[108:111], a[68:71], v[84:87]\n"
"ds_read_b128 a[160:163], v12 offset:8192\n"
"v_mfma_f32_16x16x32_bf16 v[84:87], a[104:107], a[64:67], v[84:87]\n"
"v_lshl_add_u32 v7, v4, 5, v2\n"
"v_mfma_f32_16x16x32_bf16 v[88:91], a[112:115], a[64:67], v[88:91]\n"
"ds_read_b128 a[164:167], v13 offset:8192\n"
"v_mfma_f32_16x16x32_bf16 v[88:91], a[116:119], a[68:71], v[88:91]\n"
"buffer_load_dwordx4 v5, s[56:59], s11 offen offset:3072 sc1 lds\n"
"v_xor_b32 v4, 1, v3\n"
"v_mfma_f32_16x16x32_bf16 v[92:95], a[124:127], a[68:71], v[92:95]\n"
"ds_read_b128 a[168:171], v12 offset:10240\n"
"v_add_u32 v5, 0x8000, v5\n"
"v_mfma_f32_16x16x32_bf16 v[92:95], a[120:123], a[64:67], v[92:95]\n"
"v_lshl_add_u32 v8, v4, 5, v2\n"
"v_mfma_f32_16x16x32_bf16 v[116:119], a[120:123], a[72:75], v[116:119]\n"
"ds_read_b128 a[172:175], v13 offset:10240\n"
"s_add_u32 m0, s77, 0\n"
"v_mfma_f32_16x16x32_bf16 v[116:119], a[124:127], a[76:79], v[116:119]\n"
"buffer_load_dwordx4 v11, s[60:63], 0 offen offset:0 lds\n"
"v_xor_b32 v4, 2, v3\n"
"v_mfma_f32_16x16x32_bf16 v[112:115], a[116:119], a[76:79], v[112:115]\n"
"ds_read_b128 a[176:179], v12 offset:12288\n"
"v_mfma_f32_16x16x32_bf16 v[112:115], a[112:115], a[72:75], v[112:115]\n"
"v_lshl_add_u32 v9, v4, 5, v2\n"
"v_mfma_f32_16x16x32_bf16 v[108:111], a[104:107], a[72:75], v[108:111]\n"
"ds_read_b128 a[180:183], v13 offset:12288\n"
"v_mfma_f32_16x16x32_bf16 v[108:111], a[108:111], a[76:79], v[108:111]\n"
"v_xor_b32 v4, 3, v3\n"
"v_mfma_f32_16x16x32_bf16 v[104:107], a[100:103], a[76:79], v[104:107]\n"
"ds_read_b128 a[184:187], v12 offset:14336\n"
"v_mfma_f32_16x16x32_bf16 v[104:107], a[96:99], a[72:75], v[104:107]\n"
"buffer_load_dwordx4 v15, s[60:63], 0 offen offset:1024 lds\n"
"v_lshl_add_u32 v10, v4, 5, v2\n"
"v_mfma_f32_16x16x32_bf16 v[120:123], a[96:99], a[80:83], v[120:123]\n"
"ds_read_b128 a[188:191], v13 offset:14336\n"
"v_mfma_f32_16x16x32_bf16 v[120:123], a[100:103], a[84:87], v[120:123]\n"
"v_mfma_f32_16x16x32_bf16 v[124:127], a[108:111], a[84:87], v[124:127]\n"
"buffer_load_dwordx4 v11, s[60:63], s68 offen offset:2048 lds\n"
"v_mfma_f32_16x16x32_bf16 v[124:127], a[104:107], a[80:83], v[124:127]\n"
"v_add_u32 v11, 0x20000, v11\n"
"v_mfma_f32_16x16x32_bf16 v[136:139], a[112:115], a[80:83], v[136:139]\n"
"v_mfma_f32_16x16x32_bf16 v[136:139], a[116:119], a[84:87], v[136:139]\n"
"v_mfma_f32_16x16x32_bf16 v[140:143], a[124:127], a[84:87], v[140:143]\n"
"v_mfma_f32_16x16x32_bf16 v[140:143], a[120:123], a[80:83], v[140:143]\n"
"buffer_load_dwordx4 v15, s[60:63], s68 offen offset:3072 lds\n"
"v_mfma_f32_16x16x32_bf16 v[156:159], a[120:123], a[88:91], v[156:159]\n"
"v_add_u32 v15, 0x20000, v15\n"
"s_add_u32 s76, s76, s8\n"
"v_mfma_f32_16x16x32_bf16 v[156:159], a[124:127], a[92:95], v[156:159]\n"
"s_add_u32 s77, s77, s8\n"
"s_cmp_eq_u32 s7, 98304\n"
"s_cselect_b32 s8, 4294868992, s9\n"
"v_mfma_f32_16x16x32_bf16 v[152:155], a[116:119], a[92:95], v[152:155]\n"
"v_mfma_f32_16x16x32_bf16 v[152:155], a[112:115], a[88:91], v[152:155]\n"
"s_add_u32 s7, s7, s8\n"
"v_mfma_f32_16x16x32_bf16 v[148:151], a[104:107], a[88:91], v[148:151]\n"
"v_mfma_f32_16x16x32_bf16 v[148:151], a[108:111], a[92:95], v[148:151]\n"
"v_mfma_f32_16x16x32_bf16 v[144:147], a[100:103], a[92:95], v[144:147]\n"
"v_mfma_f32_16x16x32_bf16 v[144:147], a[96:99], a[88:91], v[144:147]\n"
"s_waitcnt lgkmcnt(14)\n"
"v_mfma_f32_16x16x32_bf16 a[224:227], a[192:195], a[128:131], a[224:227]\n"
"s_waitcnt vmcnt(12) lgkmcnt(0)\n"
"s_barrier\n"
"v_mfma_f32_16x16x32_bf16 a[224:227], a[196:199], a[132:135], a[224:227]\n"
"ds_read_b128 a[32:35], v6 offset:0\n"
"v_mfma_f32_16x16x32_bf16 a[228:231], a[204:207], a[132:135], a[228:231]\n"
"s_mov_b32 s40, s34\n"
"s_and_b32 s41, s35, 0xffff\n"
"ds_read_b128 a[36:39], v6 offset:1024\n"
"v_mfma_f32_16x16x32_bf16 a[228:231], a[200:203], a[128:131], a[228:231]\n"
"s_add_u32 m0, s76, 0\n"
"s_mov_b32 s42, 4194304\n"
"buffer_load_dwordx4 v5, s[56:59], s10 offen offset:0 sc1 lds\n"
"v_mfma_f32_16x16x32_bf16 a[232:235], a[208:211], a[128:131], a[232:235]\n"
"s_mov_b32 s43, 0x20000\n"
"ds_read_b64_tr_b16 a[96:97], v7 offset:0\n"
"v_mfma_f32_16x16x32_bf16 a[232:235], a[212:215], a[132:135], a[232:235]\n"
"s_lshl_b32 s38, s4, 3\n"
"ds_read_b64_tr_b16 a[98:99], v7 offset:4096\n"
"v_mfma_f32_16x16x32_bf16 a[236:239], a[220:223], a[132:135], a[236:239]\n"
"s_add_u32 s38, s38, s5\n"
"v_mfma_f32_16x16x32_bf16 a[236:239], a[216:219], a[128:131], a[236:239]\n"
"s_lshl_b32 s38, s38, 2\n"
"ds_read_b64_tr_b16 a[100:101], v7 offset:8192\n"
"v_mfma_f32_16x16x32_bf16 a[252:255], a[216:219], a[136:139], a[252:255]\n"
"buffer_load_dwordx4 v5, s[56:59], s10 offen offset:1024 sc1 lds\n"
"s_add_u32 s39, s38, s83\n"
"v_mfma_f32_16x16x32_bf16 a[252:255], a[220:223], a[140:143], a[252:255]\n"
"ds_read_b64_tr_b16 a[102:103], v7 offset:12288\n"
"v_lshlrev_b32 v2, 4, v1\n"
"v_mfma_f32_16x16x32_bf16 a[248:251], a[212:215], a[140:143], a[248:251]\n"
"s_lshl_b32 s14, s13, 12\n"
"ds_read_b64_tr_b16 a[104:105], v8 offset:0\n"
"v_mfma_f32_16x16x32_bf16 a[248:251], a[208:211], a[136:139], a[248:251]\n"
"s_add_u32 s14, s14, 23461888\n"
"ds_read_b64_tr_b16 a[106:107], v8 offset:4096\n"
"v_mfma_f32_16x16x32_bf16 a[244:247], a[200:203], a[136:139], a[244:247]\n"
"v_add_u32 v2, s14, v2\n"
"v_mfma_f32_16x16x32_bf16 a[244:247], a[204:207], a[140:143], a[244:247]\n"
"buffer_load_dwordx4 v5, s[56:59], s10 offen offset:2048 sc1 lds\n"
"s_xor_b32 s14, s39, 1\n"
"v_mfma_f32_16x16x32_bf16 a[240:243], a[196:199], a[140:143], a[240:243]\n"
"ds_read_b64_tr_b16 a[108:109], v8 offset:8192\n"
"s_lshl_b32 s14, s14, 16\n"
"v_mfma_f32_16x16x32_bf16 a[240:243], a[192:195], a[136:139], a[240:243]\n"
"ds_read_b64_tr_b16 a[110:111], v8 offset:12288\n"
"s_add_u32 s69, s14, 16384\n"
"v_mfma_f32_16x16x32_bf16 v[40:43], a[192:195], a[144:147], v[40:43]\n"
"s_lshl_b32 s14, s39, 16\n"
"s_add_u32 s65, s14, 16384\n"
"ds_read_b64_tr_b16 a[112:113], v9 offset:0\n"
"v_mfma_f32_16x16x32_bf16 v[40:43], a[196:199], a[148:151], v[40:43]\n"
"s_xor_b32 s14, s39, 2\n"
"ds_read_b64_tr_b16 a[114:115], v9 offset:4096\n"
"v_mfma_f32_16x16x32_bf16 v[44:47], a[204:207], a[148:151], v[44:47]\n"
"buffer_load_dwordx4 v5, s[56:59], s10 offen offset:3072 sc1 lds\n"
"s_lshl_b32 s14, s14, 16\n"
"v_mfma_f32_16x16x32_bf16 v[44:47], a[200:203], a[144:147], v[44:47]\n"
"s_add_u32 s70, s14, 32768\n"
"ds_read_b64_tr_b16 a[116:117], v9 offset:8192\n"
"v_mfma_f32_16x16x32_bf16 v[48:51], a[208:211], a[144:147], v[48:51]\n"
"s_lshl_b32 s14, s39, 16\n"
"ds_read_b64_tr_b16 a[118:119], v9 offset:12288\n"
"v_mfma_f32_16x16x32_bf16 v[48:51], a[212:215], a[148:151], v[48:51]\n"
"s_add_u32 m0, s76, 4096\n"
"s_add_u32 s66, s14, 32768\n"
"buffer_load_dwordx4 v5, s[56:59], s11 offen offset:0 sc1 lds\n"
"v_mfma_f32_16x16x32_bf16 v[52:55], a[220:223], a[148:151], v[52:55]\n"
"s_xor_b32 s14, s39, 3\n"
"ds_read_b64_tr_b16 a[120:121], v10 offset:0\n"
"v_mfma_f32_16x16x32_bf16 v[52:55], a[216:219], a[144:147], v[52:55]\n"
"s_lshl_b32 s14, s14, 16\n"
"ds_read_b64_tr_b16 a[122:123], v10 offset:4096\n"
"v_mfma_f32_16x16x32_bf16 v[76:79], a[216:219], a[152:155], v[76:79]\n"
"s_add_u32 s71, s14, 49152\n"
"v_mfma_f32_16x16x32_bf16 v[76:79], a[220:223], a[156:159], v[76:79]\n"
"s_lshl_b32 s14, s39, 16\n"
"ds_read_b64_tr_b16 a[124:125], v10 offset:8192\n"
"v_mfma_f32_16x16x32_bf16 v[72:75], a[212:215], a[156:159], v[72:75]\n"
"s_add_u32 s67, s14, 49152\n"
"ds_read_b64_tr_b16 a[126:127], v10 offset:12288\n"
"v_mfma_f32_16x16x32_bf16 v[72:75], a[208:211], a[152:155], v[72:75]\n"
"buffer_load_dwordx4 v5, s[56:59], s11 offen offset:1024 sc1 lds\n"
"s_lshl_b32 s14, s13, 7\n"
"v_mfma_f32_16x16x32_bf16 v[60:63], a[200:203], a[152:155], v[60:63]\n"
"s_add_u32 s14, s14, 262144\n"
"v_and_b32 v3, 3, v1\n"
"v_mfma_f32_16x16x32_bf16 v[60:63], a[204:207], a[156:159], v[60:63]\n"
"ds_read_b128 a[40:43], v6 offset:2048\n"
"v_lshl_add_u32 v14, v3, 4, s14\n"
"v_mfma_f32_16x16x32_bf16 v[56:59], a[196:199], a[156:159], v[56:59]\n"
"ds_read_b128 a[44:47], v6 offset:3072\n"
"v_xor_b32 v3, s39, v3\n"
"v_mfma_f32_16x16x32_bf16 v[56:59], a[192:195], a[152:155], v[56:59]\n"
"v_lshl_add_u32 v4, v3, 9, v14\n"
"v_mfma_f32_16x16x32_bf16 v[80:83], a[192:195], a[160:163], v[80:83]\n"
"ds_read_b128 a[48:51], v6 offset:4096\n"
"v_mfma_f32_16x16x32_bf16 v[80:83], a[196:199], a[164:167], v[80:83]\n"
"buffer_load_dwordx4 v5, s[56:59], s11 offen offset:2048 sc1 lds\n"
"s_lshl_b32 s14, s39, 9\n"
"v_mfma_f32_16x16x32_bf16 v[84:87], a[204:207], a[164:167], v[84:87]\n"
"ds_read_b128 a[52:55], v6 offset:5120\n"
"v_add_u32 v14, s14, v14\n"
"v_mfma_f32_16x16x32_bf16 v[84:87], a[200:203], a[160:163], v[84:87]\n"
"v_mov_b32 v12, 1\n"
"v_mfma_f32_16x16x32_bf16 v[88:91], a[208:211], a[160:163], v[88:91]\n"
"ds_read_b128 a[56:59], v6 offset:6144\n"
"v_bfe_u32 v3, v1, 2, 2\n"
"v_mfma_f32_16x16x32_bf16 v[88:91], a[212:215], a[164:167], v[88:91]\n"
"ds_read_b128 a[60:63], v6 offset:7168\n"
"v_and_b32 v13, 3, v1\n"
"v_mfma_f32_16x16x32_bf16 v[92:95], a[220:223], a[164:167], v[92:95]\n"
"buffer_load_dwordx4 v5, s[56:59], s11 offen offset:3072 sc1 lds\n"
"v_lshl_add_u32 v3, v3, 3, v13\n"
"v_mfma_f32_16x16x32_bf16 v[92:95], a[216:219], a[160:163], v[92:95]\n"
"ds_read_b128 a[64:67], v6 offset:8192\n"
"v_add_u32 v5, 0x8000, v5\n"
"v_mfma_f32_16x16x32_bf16 v[116:119], a[216:219], a[168:171], v[116:119]\n"
"s_lshl_b32 s14, s4, 8\n"
"ds_read_b128 a[68:71], v6 offset:9216\n"
"v_mfma_f32_16x16x32_bf16 v[116:119], a[220:223], a[172:175], v[116:119]\n"
"s_and_b32 s15, s13, 2\n"
"s_add_u32 m0, s77, 0\n"
"s_lshl_b32 s15, s15, 6\n"
"v_mfma_f32_16x16x32_bf16 v[112:115], a[212:215], a[172:175], v[112:115]\n"
"ds_read_b128 a[72:75], v6 offset:10240\n"
"v_mfma_f32_16x16x32_bf16 v[112:115], a[208:211], a[168:171], v[112:115]\n"
"buffer_load_dwordx4 v11, s[60:63], 0 offen offset:0 lds\n"
"s_add_u32 s14, s14, s15\n"
"v_mfma_f32_16x16x32_bf16 v[108:111], a[200:203], a[168:171], v[108:111]\n"
"s_lshl_b32 s15, s83, 5\n"
"ds_read_b128 a[76:79], v6 offset:11264\n"
"v_mfma_f32_16x16x32_bf16 v[108:111], a[204:207], a[172:175], v[108:111]\n"
"s_add_u32 s14, s14, s15\n"
"v_add_u32 v3, s14, v3\n"
"v_mfma_f32_16x16x32_bf16 v[104:107], a[196:199], a[172:175], v[104:107]\n"
"ds_read_b128 a[80:83], v6 offset:12288\n"
"v_lshlrev_b32 v3, 11, v3\n"
"v_mfma_f32_16x16x32_bf16 v[104:107], a[192:195], a[168:171], v[104:107]\n"
"ds_read_b128 a[84:87], v6 offset:13312\n"
"v_bfe_u32 v13, v1, 4, 1\n"
"v_mfma_f32_16x16x32_bf16 v[120:123], a[192:195], a[176:179], v[120:123]\n"
"buffer_load_dwordx4 v15, s[60:63], 0 offen offset:1024 lds\n"
"v_lshl_add_u32 v3, v13, 5, v3\n"
"v_mfma_f32_16x16x32_bf16 v[120:123], a[196:199], a[180:183], v[120:123]\n"
"ds_read_b128 a[88:91], v6 offset:14336\n"
"v_lshrrev_b32 v13, 5, v1\n"
"v_mfma_f32_16x16x32_bf16 v[124:127], a[204:207], a[180:183], v[124:127]\n"
"ds_read_b128 a[92:95], v6 offset:15360\n"
"v_lshl_add_u32 v3, v13, 4, v3\n"
"v_mfma_f32_16x16x32_bf16 v[124:127], a[200:203], a[176:179], v[124:127]\n"
"s_lshl_b32 s14, s5, 8\n"
"s_and_b32 s15, s13, 1\n"
"buffer_load_dwordx4 v11, s[60:63], s68 offen offset:2048 lds\n"
"v_mfma_f32_16x16x32_bf16 v[136:139], a[208:211], a[176:179], v[136:139]\n"
"s_lshl_b32 s15, s15, 7\n"
"v_add_u32 v11, 0x20000, v11\n"
"s_add_u32 s14, s14, s15\n"
"v_mfma_f32_16x16x32_bf16 v[136:139], a[212:215], a[180:183], v[136:139]\n"
"v_add_u32 v3, s14, v3\n"
"v_mfma_f32_16x16x32_bf16 v[140:143], a[220:223], a[180:183], v[140:143]\n"
"s_mov_b32 s64, 8192\n"
"v_mfma_f32_16x16x32_bf16 v[140:143], a[216:219], a[176:179], v[140:143]\n"
"buffer_load_dwordx4 v15, s[60:63], s68 offen offset:3072 lds\n"
"v_mfma_f32_16x16x32_bf16 v[156:159], a[216:219], a[184:187], v[156:159]\n"
"v_add_u32 v15, 0x20000, v15\n"
"s_add_u32 s76, s76, s8\n"
"v_mfma_f32_16x16x32_bf16 v[156:159], a[220:223], a[188:191], v[156:159]\n"
"s_add_u32 s77, s77, s8\n"
"s_cmp_eq_u32 s7, 98304\n"
"s_cselect_b32 s8, 4294868992, s9\n"
"v_mfma_f32_16x16x32_bf16 v[152:155], a[212:215], a[188:191], v[152:155]\n"
"s_add_u32 s7, s7, s8\n"
"v_add_u32 v6, s8, v6\n"
"v_add_u32 v7, s8, v7\n"
"v_mfma_f32_16x16x32_bf16 v[152:155], a[208:211], a[184:187], v[152:155]\n"
"v_add_u32 v8, s8, v8\n"
"v_add_u32 v9, s8, v9\n"
"v_mfma_f32_16x16x32_bf16 v[148:151], a[200:203], a[184:187], v[148:151]\n"
"v_add_u32 v10, s8, v10\n"
"v_mfma_f32_16x16x32_bf16 v[148:151], a[204:207], a[188:191], v[148:151]\n"
"v_mfma_f32_16x16x32_bf16 v[144:147], a[196:199], a[188:191], v[144:147]\n"
"v_mfma_f32_16x16x32_bf16 v[144:147], a[192:195], a[184:187], v[144:147]\n"
"s_lshr_b32 s14, s83, 1\n"
"s_lshl_b32 s14, s14, 31\n"
"s_waitcnt lgkmcnt(14)\n"
"v_mfma_f32_16x16x32_bf16 v[32:35], a[96:99], a[32:35], 0\n"
"s_waitcnt vmcnt(12) lgkmcnt(0)\n"
"s_barrier\n"
"v_sub_f32 v17, 1.0, v16\n"
"v_mfma_f32_16x16x32_bf16 v[32:35], a[100:103], a[36:39], v[32:35]\n"
"ds_read_b128 a[128:131], v6 offset:0\n"
"v_sub_f32 v19, 1.0, v18\n"
"v_mfma_f32_16x16x32_bf16 v[36:39], a[108:111], a[36:39], 0\n"
"ds_read_b128 a[132:135], v6 offset:1024\n"
"s_add_u32 m0, s76, 0\n"
"v_mfma_f32_16x16x32_bf16 v[36:39], a[104:107], a[32:35], v[36:39]\n"
"buffer_load_dwordx4 v5, s[56:59], s10 offen offset:0 sc1 lds\n"
"v_sub_f32 v21, 1.0, v20\n"
"v_mfma_f32_16x16x32_bf16 v[64:67], a[112:115], a[32:35], 0\n"
"ds_read_b64_tr_b16 a[192:193], v7 offset:0\n"
"v_sub_f32 v23, 1.0, v22\n"
"v_mfma_f32_16x16x32_bf16 v[64:67], a[116:119], a[36:39], v[64:67]\n"
"ds_read_b64_tr_b16 a[194:195], v7 offset:4096\n"
"v_sub_f32 v25, 1.0, v24\n"
"v_mfma_f32_16x16x32_bf16 v[68:71], a[124:127], a[36:39], 0\n"
"v_sub_f32 v27, 1.0, v26\n"
"v_sub_f32 v29, 1.0, v28\n"
"v_mfma_f32_16x16x32_bf16 v[68:71], a[120:123], a[32:35], v[68:71]\n"
"ds_read_b64_tr_b16 a[196:197], v7 offset:8192\n"
"v_sub_f32 v31, 1.0, v30\n"
"v_mfma_f32_16x16x32_bf16 v[132:135], a[120:123], a[40:43], 0\n"
"buffer_load_dwordx4 v5, s[56:59], s10 offen offset:1024 sc1 lds\n"
"v_xor_b32 v16, s14, v16\n"
"v_mfma_f32_16x16x32_bf16 v[132:135], a[124:127], a[44:47], v[132:135]\n"
"ds_read_b64_tr_b16 a[198:199], v7 offset:12288\n"
"v_xor_b32 v18, s14, v18\n"
"v_mfma_f32_16x16x32_bf16 v[128:131], a[116:119], a[44:47], 0\n"
"ds_read_b64_tr_b16 a[200:201], v8 offset:0\n"
"v_xor_b32 v20, s14, v20\n"
"v_mfma_f32_16x16x32_bf16 v[128:131], a[112:115], a[40:43], v[128:131]\n"
"ds_read_b64_tr_b16 a[202:203], v8 offset:4096\n"
"v_xor_b32 v22, s14, v22\n"
"v_mfma_f32_16x16x32_bf16 v[100:103], a[104:107], a[40:43], 0\n"
"buffer_load_dwordx4 v5, s[56:59], s10 offen offset:2048 sc1 lds\n"
"v_xor_b32 v24, s14, v24\n"
"v_mfma_f32_16x16x32_bf16 v[100:103], a[108:111], a[44:47], v[100:103]\n"
"ds_read_b64_tr_b16 a[204:205], v8 offset:8192\n"
"v_xor_b32 v26, s14, v26\n"
"v_mfma_f32_16x16x32_bf16 v[96:99], a[100:103], a[44:47], 0\n"
"ds_read_b64_tr_b16 a[206:207], v8 offset:12288\n"
"v_xor_b32 v28, s14, v28\n"
"v_mfma_f32_16x16x32_bf16 v[96:99], a[96:99], a[40:43], v[96:99]\n"
"v_xor_b32 v30, s14, v30\n"
"v_mul_f32 v40, v40, v20\n"
"v_mfma_f32_16x16x32_bf16 v[160:163], a[96:99], a[48:51], 0\n"
"ds_read_b64_tr_b16 a[208:209], v9 offset:0\n"
"v_mul_f32 v41, v41, v20\n"
"v_mfma_f32_16x16x32_bf16 v[160:163], a[100:103], a[52:55], v[160:163]\n"
"ds_read_b64_tr_b16 a[210:211], v9 offset:4096\n"
"v_mul_f32 v42, v42, v20\n"
"v_mfma_f32_16x16x32_bf16 v[164:167], a[108:111], a[52:55], 0\n"
"buffer_load_dwordx4 v5, s[56:59], s10 offen offset:3072 sc1 lds\n"
"v_mul_f32 v43, v43, v20\n"
"v_mfma_f32_16x16x32_bf16 v[164:167], a[104:107], a[48:51], v[164:167]\n"
"ds_read_b64_tr_b16 a[212:213], v9 offset:8192\n"
"v_mul_f32 v44, v44, v20\n"
"v_mfma_f32_16x16x32_bf16 v[168:171], a[112:115], a[48:51], 0\n"
"ds_read_b64_tr_b16 a[214:215], v9 offset:12288\n"
"s_add_u32 m0, s76, 4096\n"
"v_mfma_f32_16x16x32_bf16 v[168:171], a[116:119], a[52:55], v[168:171]\n"
"buffer_load_dwordx4 v5, s[56:59], s11 offen offset:0 sc1 lds\n"
"v_mul_f32 v45, v45, v20\n"
"v_mfma_f32_16x16x32_bf16 v[172:175], a[124:127], a[52:55], 0\n"
"ds_read_b64_tr_b16 a[216:217], v10 offset:0\n"
"v_mul_f32 v46, v46, v20\n"
"v_mfma_f32_16x16x32_bf16 v[172:175], a[120:123], a[48:51], v[172:175]\n"
"ds_read_b64_tr_b16 a[218:219], v10 offset:4096\n"
"v_mul_f32 v47, v47, v20\n"
"v_mfma_f32_16x16x32_bf16 v[188:191], a[120:123], a[56:59], 0\n"
"v_mul_f32 v48, v48, v20\n"
"v_mul_f32 v49, v49, v20\n"
"v_mfma_f32_16x16x32_bf16 v[188:191], a[124:127], a[60:63], v[188:191]\n"
"ds_read_b64_tr_b16 a[220:221], v10 offset:8192\n"
"v_mul_f32 v50, v50, v20\n"
"v_mfma_f32_16x16x32_bf16 v[184:187], a[116:119], a[60:63], 0\n"
"ds_read_b64_tr_b16 a[222:223], v10 offset:12288\n"
"v_mul_f32 v51, v51, v20\n"
"v_mfma_f32_16x16x32_bf16 v[184:187], a[112:115], a[56:59], v[184:187]\n"
"buffer_load_dwordx4 v5, s[56:59], s11 offen offset:1024 sc1 lds\n"
"v_mul_f32 v52, v52, v20\n"
"v_mfma_f32_16x16x32_bf16 v[180:183], a[104:107], a[56:59], 0\n"
"ds_read_b128 a[136:139], v6 offset:2048\n"
"v_mul_f32 v53, v53, v20\n"
"v_mfma_f32_16x16x32_bf16 v[180:183], a[108:111], a[60:63], v[180:183]\n"
"ds_read_b128 a[140:143], v6 offset:3072\n"
"v_mul_f32 v54, v54, v20\n"
"v_mfma_f32_16x16x32_bf16 v[176:179], a[100:103], a[60:63], 0\n"
"v_mul_f32 v55, v55, v20\n"
"v_mul_f32 v56, v56, v22\n"
"v_mfma_f32_16x16x32_bf16 v[176:179], a[96:99], a[56:59], v[176:179]\n"
"ds_read_b128 a[144:147], v6 offset:4096\n"
"v_mul_f32 v57, v57, v22\n"
"v_mfma_f32_16x16x32_bf16 v[192:195], a[96:99], a[64:67], 0\n"
"buffer_load_dwordx4 v5, s[56:59], s11 offen offset:2048 sc1 lds\n"
"v_mul_f32 v58, v58, v22\n"
"v_mfma_f32_16x16x32_bf16 v[192:195], a[100:103], a[68:71], v[192:195]\n"
"ds_read_b128 a[148:151], v6 offset:5120\n"
"v_mul_f32 v59, v59, v22\n"
"v_mfma_f32_16x16x32_bf16 v[196:199], a[108:111], a[68:71], 0\n"
"ds_read_b128 a[152:155], v6 offset:6144\n"
"v_mul_f32 v60, v60, v22\n"
"v_mfma_f32_16x16x32_bf16 v[196:199], a[104:107], a[64:67], v[196:199]\n"
"ds_read_b128 a[156:159], v6 offset:7168\n"
"v_mul_f32 v61, v61, v22\n"
"v_mfma_f32_16x16x32_bf16 v[200:203], a[112:115], a[64:67], 0\n"
"buffer_load_dwordx4 v5, s[56:59], s11 offen offset:3072 sc1 lds\n"
"v_mul_f32 v62, v62, v22\n"
"v_mfma_f32_16x16x32_bf16 v[200:203], a[116:119], a[68:71], v[200:203]\n"
"ds_read_b128 a[160:163], v6 offset:8192\n"
"v_add_u32 v5, 0x8000, v5\n"
"v_mfma_f32_16x16x32_bf16 v[204:207], a[124:127], a[68:71], 0\n"
"ds_read_b128 a[164:167], v6 offset:9216\n"
"v_mul_f32 v63, v63, v22\n"
"v_mfma_f32_16x16x32_bf16 v[204:207], a[120:123], a[64:67], v[204:207]\n"
"s_add_u32 m0, s77, 0\n"
"v_mul_f32 v72, v72, v22\n"
"v_mul_f32 v73, v73, v22\n"
"v_mfma_f32_16x16x32_bf16 v[220:223], a[120:123], a[72:75], 0\n"
"ds_read_b128 a[168:171], v6 offset:10240\n"
"v_mul_f32 v74, v74, v22\n"
"v_mfma_f32_16x16x32_bf16 v[220:223], a[124:127], a[76:79], v[220:223]\n"
"buffer_load_dwordx4 v11, s[60:63], 0 offen offset:0 lds\n"
"v_mul_f32 v75, v75, v22\n"
"v_mfma_f32_16x16x32_bf16 v[216:219], a[116:119], a[76:79], 0\n"
"ds_read_b128 a[172:175], v6 offset:11264\n"
"v_mul_f32 v76, v76, v22\n"
"v_mfma_f32_16x16x32_bf16 v[216:219], a[112:115], a[72:75], v[216:219]\n"
"ds_read_b128 a[176:179], v6 offset:12288\n"
"v_mul_f32 v77, v77, v22\n"
"v_mfma_f32_16x16x32_bf16 v[212:215], a[104:107], a[72:75], 0\n"
"ds_read_b128 a[180:183], v6 offset:13312\n"
"v_mul_f32 v78, v78, v22\n"
"v_mfma_f32_16x16x32_bf16 v[212:215], a[108:111], a[76:79], v[212:215]\n"
"buffer_load_dwordx4 v15, s[60:63], 0 offen offset:1024 lds\n"
"v_mul_f32 v79, v79, v22\n"
"v_mfma_f32_16x16x32_bf16 v[208:211], a[100:103], a[76:79], 0\n"
"ds_read_b128 a[184:187], v6 offset:14336\n"
"v_mul_f32 v80, v80, v24\n"
"v_mfma_f32_16x16x32_bf16 v[208:211], a[96:99], a[72:75], v[208:211]\n"
"ds_read_b128 a[188:191], v6 offset:15360\n"
"v_mul_f32 v81, v81, v24\n"
"v_mfma_f32_16x16x32_bf16 v[224:227], a[96:99], a[80:83], 0\n"
"v_mul_f32 v82, v82, v24\n"
"v_mul_f32 v83, v83, v24\n"
"v_mfma_f32_16x16x32_bf16 v[224:227], a[100:103], a[84:87], v[224:227]\n"
"v_mul_f32 v84, v84, v24\n"
"v_mul_f32 v85, v85, v24\n"
"v_mfma_f32_16x16x32_bf16 v[228:231], a[108:111], a[84:87], 0\n"
"buffer_load_dwordx4 v11, s[60:63], s68 offen offset:2048 lds\n"
"v_mul_f32 v86, v86, v24\n"
"v_mfma_f32_16x16x32_bf16 v[228:231], a[104:107], a[80:83], v[228:231]\n"
"v_add_u32 v11, 0x20000, v11\n"
"v_mul_f32 v87, v87, v24\n"
"v_mfma_f32_16x16x32_bf16 v[232:235], a[112:115], a[80:83], 0\n"
"v_mul_f32 v88, v88, v24\n"
"v_mul_f32 v89, v89, v24\n"
"v_mfma_f32_16x16x32_bf16 v[232:235], a[116:119], a[84:87], v[232:235]\n"
"v_mul_f32 v90, v90, v24\n"
"v_mul_f32 v91, v91, v24\n"
"v_mfma_f32_16x16x32_bf16 v[236:239], a[124:127], a[84:87], 0\n"
"v_mul_f32 v92, v92, v24\n"
"v_mul_f32 v93, v93, v24\n"
"v_mfma_f32_16x16x32_bf16 v[236:239], a[120:123], a[80:83], v[236:239]\n"
"buffer_load_dwordx4 v15, s[60:63], s68 offen offset:3072 lds\n"
"v_mul_f32 v94, v94, v24\n"
"v_mfma_f32_16x16x32_bf16 v[252:255], a[120:123], a[88:91], 0\n"
"v_add_u32 v15, 0x20000, v15\n"
"s_add_u32 s76, s76, s8\n"
"v_mfma_f32_16x16x32_bf16 v[252:255], a[124:127], a[92:95], v[252:255]\n"
"s_add_u32 s77, s77, s8\n"
"s_cmp_eq_u32 s7, 98304\n"
"s_cselect_b32 s8, 4294868992, s9\n"
"v_mfma_f32_16x16x32_bf16 v[248:251], a[116:119], a[92:95], 0\n"
"s_add_u32 s7, s7, s8\n"
"v_add_u32 v6, s8, v6\n"
"v_add_u32 v7, s8, v7\n"
"v_mfma_f32_16x16x32_bf16 v[248:251], a[112:115], a[88:91], v[248:251]\n"
"v_add_u32 v8, s8, v8\n"
"v_add_u32 v9, s8, v9\n"
"v_mfma_f32_16x16x32_bf16 v[244:247], a[104:107], a[88:91], 0\n"
"v_add_u32 v10, s8, v10\n"
"v_mul_f32 v95, v95, v24\n"
"v_mfma_f32_16x16x32_bf16 v[244:247], a[108:111], a[92:95], v[244:247]\n"
"v_mul_f32 v104, v104, v26\n"
"v_mul_f32 v105, v105, v26\n"
"v_mfma_f32_16x16x32_bf16 v[240:243], a[100:103], a[92:95], 0\n"
"v_mul_f32 v106, v106, v26\n"
"v_mul_f32 v107, v107, v26\n"
"v_mfma_f32_16x16x32_bf16 v[240:243], a[96:99], a[88:91], v[240:243]\n"
"v_mul_f32 v108, v108, v26\n"
"v_mul_f32 v109, v109, v26\n"
"v_mul_f32 v110, v110, v26\n"
"v_mul_f32 v111, v111, v26\n"
"v_mul_f32 v112, v112, v26\n"
"v_mul_f32 v113, v113, v26\n"
"v_mul_f32 v114, v114, v26\n"
"v_mul_f32 v115, v115, v26\n"
"v_mul_f32 v116, v116, v26\n"
"v_mul_f32 v117, v117, v26\n"
"v_mul_f32 v118, v118, v26\n"
"v_mul_f32 v119, v119, v26\n"
"s_mov_b32 s78, 4\n"
"DL_%=:\n"
"s_waitcnt lgkmcnt(14)\n"
"v_mfma_f32_16x16x32_bf16 v[32:35], a[192:195], a[128:131], v[32:35]\n"
"s_waitcnt vmcnt(12) lgkmcnt(0)\n"
"s_barrier\n"
"v_mfma_f32_16x16x32_bf16 v[32:35], a[196:199], a[132:135], v[32:35]\n"
"ds_read_b128 a[32:35], v6 offset:0\n"
"v_mfma_f32_16x16x32_bf16 v[36:39], a[204:207], a[132:135], v[36:39]\n"
"ds_read_b128 a[36:39], v6 offset:1024\n"
"s_add_u32 m0, s76, 0\n"
"v_mfma_f32_16x16x32_bf16 v[36:39], a[200:203], a[128:131], v[36:39]\n"
"buffer_load_dwordx4 v5, s[56:59], s10 offen offset:0 sc1 lds\n"
"v_mfma_f32_16x16x32_bf16 v[64:67], a[208:211], a[128:131], v[64:67]\n"
"ds_read_b64_tr_b16 a[96:97], v7 offset:0\n"
"v_mfma_f32_16x16x32_bf16 v[64:67], a[212:215], a[132:135], v[64:67]\n"
"ds_read_b64_tr_b16 a[98:99], v7 offset:4096\n"
"v_mfma_f32_16x16x32_bf16 v[68:71], a[220:223], a[132:135], v[68:71]\n"
"v_mfma_f32_16x16x32_bf16 v[68:71], a[216:219], a[128:131], v[68:71]\n"
"ds_read_b64_tr_b16 a[100:101], v7 offset:8192\n"
"v_mfma_f32_16x16x32_bf16 v[132:135], a[216:219], a[136:139], v[132:135]\n"
"buffer_load_dwordx4 v5, s[56:59], s10 offen offset:1024 sc1 lds\n"
"v_mfma_f32_16x16x32_bf16 v[132:135], a[220:223], a[140:143], v[132:135]\n"
"ds_read_b64_tr_b16 a[102:103], v7 offset:12288\n"
"v_mfma_f32_16x16x32_bf16 v[128:131], a[212:215], a[140:143], v[128:131]\n"
"ds_read_b64_tr_b16 a[104:105], v8 offset:0\n"
"v_mfma_f32_16x16x32_bf16 v[128:131], a[208:211], a[136:139], v[128:131]\n"
"ds_read_b64_tr_b16 a[106:107], v8 offset:4096\n"
"v_mfma_f32_16x16x32_bf16 v[100:103], a[200:203], a[136:139], v[100:103]\n"
"buffer_load_dwordx4 v5, s[56:59], s10 offen offset:2048 sc1 lds\n"
"v_mfma_f32_16x16x32_bf16 v[100:103], a[204:207], a[140:143], v[100:103]\n"
"ds_read_b64_tr_b16 a[108:109], v8 offset:8192\n"
"v_mfma_f32_16x16x32_bf16 v[96:99], a[196:199], a[140:143], v[96:99]\n"
"ds_read_b64_tr_b16 a[110:111], v8 offset:12288\n"
"v_mfma_f32_16x16x32_bf16 v[96:99], a[192:195], a[136:139], v[96:99]\n"
"v_mfma_f32_16x16x32_bf16 v[160:163], a[192:195], a[144:147], v[160:163]\n"
"ds_read_b64_tr_b16 a[112:113], v9 offset:0\n"
"v_mfma_f32_16x16x32_bf16 v[160:163], a[196:199], a[148:151], v[160:163]\n"
"ds_read_b64_tr_b16 a[114:115], v9 offset:4096\n"
"v_mfma_f32_16x16x32_bf16 v[164:167], a[204:207], a[148:151], v[164:167]\n"
"buffer_load_dwordx4 v5, s[56:59], s10 offen offset:3072 sc1 lds\n"
"v_mfma_f32_16x16x32_bf16 v[164:167], a[200:203], a[144:147], v[164:167]\n"
"ds_read_b64_tr_b16 a[116:117], v9 offset:8192\n"
"v_mfma_f32_16x16x32_bf16 v[168:171], a[208:211], a[144:147], v[168:171]\n"
"ds_read_b64_tr_b16 a[118:119], v9 offset:12288\n"
"s_add_u32 m0, s76, 4096\n"
"v_mfma_f32_16x16x32_bf16 v[168:171], a[212:215], a[148:151], v[168:171]\n"
"buffer_load_dwordx4 v5, s[56:59], s11 offen offset:0 sc1 lds\n"
"v_mfma_f32_16x16x32_bf16 v[172:175], a[220:223], a[148:151], v[172:175]\n"
"ds_read_b64_tr_b16 a[120:121], v10 offset:0\n"
"v_mfma_f32_16x16x32_bf16 v[172:175], a[216:219], a[144:147], v[172:175]\n"
"ds_read_b64_tr_b16 a[122:123], v10 offset:4096\n"
"v_mfma_f32_16x16x32_bf16 v[188:191], a[216:219], a[152:155], v[188:191]\n"
"v_mfma_f32_16x16x32_bf16 v[188:191], a[220:223], a[156:159], v[188:191]\n"
"ds_read_b64_tr_b16 a[124:125], v10 offset:8192\n"
"v_mfma_f32_16x16x32_bf16 v[184:187], a[212:215], a[156:159], v[184:187]\n"
"ds_read_b64_tr_b16 a[126:127], v10 offset:12288\n"
"v_mfma_f32_16x16x32_bf16 v[184:187], a[208:211], a[152:155], v[184:187]\n"
"buffer_load_dwordx4 v5, s[56:59], s11 offen offset:1024 sc1 lds\n"
"v_mfma_f32_16x16x32_bf16 v[180:183], a[200:203], a[152:155], v[180:183]\n"
"ds_read_b128 a[40:43], v6 offset:2048\n"
"v_mfma_f32_16x16x32_bf16 v[180:183], a[204:207], a[156:159], v[180:183]\n"
"ds_read_b128 a[44:47], v6 offset:3072\n"
"v_mfma_f32_16x16x32_bf16 v[176:179], a[196:199], a[156:159], v[176:179]\n"
"v_mfma_f32_16x16x32_bf16 v[176:179], a[192:195], a[152:155], v[176:179]\n"
"ds_read_b128 a[48:51], v6 offset:4096\n"
"v_mfma_f32_16x16x32_bf16 v[192:195], a[192:195], a[160:163], v[192:195]\n"
"buffer_load_dwordx4 v5, s[56:59], s11 offen offset:2048 sc1 lds\n"
"v_mfma_f32_16x16x32_bf16 v[192:195], a[196:199], a[164:167], v[192:195]\n"
"ds_read_b128 a[52:55], v6 offset:5120\n"
"v_mfma_f32_16x16x32_bf16 v[196:199], a[204:207], a[164:167], v[196:199]\n"
"ds_read_b128 a[56:59], v6 offset:6144\n"
"v_mfma_f32_16x16x32_bf16 v[196:199], a[200:203], a[160:163], v[196:199]\n"
"ds_read_b128 a[60:63], v6 offset:7168\n"
"v_mfma_f32_16x16x32_bf16 v[200:203], a[208:211], a[160:163], v[200:203]\n"
"buffer_load_dwordx4 v5, s[56:59], s11 offen offset:3072 sc1 lds\n"
"v_mfma_f32_16x16x32_bf16 v[200:203], a[212:215], a[164:167], v[200:203]\n"
"ds_read_b128 a[64:67], v6 offset:8192\n"
"v_add_u32 v5, 0x8000, v5\n"
"v_mfma_f32_16x16x32_bf16 v[204:207], a[220:223], a[164:167], v[204:207]\n"
"ds_read_b128 a[68:71], v6 offset:9216\n"
"v_mfma_f32_16x16x32_bf16 v[204:207], a[216:219], a[160:163], v[204:207]\n"
"s_add_u32 m0, s77, 0\n"
"v_mfma_f32_16x16x32_bf16 v[220:223], a[216:219], a[168:171], v[220:223]\n"
"ds_read_b128 a[72:75], v6 offset:10240\n"
"v_mfma_f32_16x16x32_bf16 v[220:223], a[220:223], a[172:175], v[220:223]\n"
"buffer_load_dwordx4 v11, s[60:63], 0 offen offset:0 lds\n"
"v_mfma_f32_16x16x32_bf16 v[216:219], a[212:215], a[172:175], v[216:219]\n"
"ds_read_b128 a[76:79], v6 offset:11264\n"
"v_mfma_f32_16x16x32_bf16 v[216:219], a[208:211], a[168:171], v[216:219]\n"
"ds_read_b128 a[80:83], v6 offset:12288\n"
"v_mfma_f32_16x16x32_bf16 v[212:215], a[200:203], a[168:171], v[212:215]\n"
"ds_read_b128 a[84:87], v6 offset:13312\n"
"v_mfma_f32_16x16x32_bf16 v[212:215], a[204:207], a[172:175], v[212:215]\n"
"buffer_load_dwordx4 v15, s[60:63], 0 offen offset:1024 lds\n"
"v_mfma_f32_16x16x32_bf16 v[208:211], a[196:199], a[172:175], v[208:211]\n"
"ds_read_b128 a[88:91], v6 offset:14336\n"
"v_mfma_f32_16x16x32_bf16 v[208:211], a[192:195], a[168:171], v[208:211]\n"
"ds_read_b128 a[92:95], v6 offset:15360\n"
"v_mfma_f32_16x16x32_bf16 v[224:227], a[192:195], a[176:179], v[224:227]\n"
"v_mfma_f32_16x16x32_bf16 v[224:227], a[196:199], a[180:183], v[224:227]\n"
"v_mfma_f32_16x16x32_bf16 v[228:231], a[204:207], a[180:183], v[228:231]\n"
"buffer_load_dwordx4 v11, s[60:63], s68 offen offset:2048 lds\n"
"v_mfma_f32_16x16x32_bf16 v[228:231], a[200:203], a[176:179], v[228:231]\n"
"v_add_u32 v11, 0x20000, v11\n"
"v_mfma_f32_16x16x32_bf16 v[232:235], a[208:211], a[176:179], v[232:235]\n"
"v_mfma_f32_16x16x32_bf16 v[232:235], a[212:215], a[180:183], v[232:235]\n"
"v_mfma_f32_16x16x32_bf16 v[236:239], a[220:223], a[180:183], v[236:239]\n"
"v_mfma_f32_16x16x32_bf16 v[236:239], a[216:219], a[176:179], v[236:239]\n"
"buffer_load_dwordx4 v15, s[60:63], s68 offen offset:3072 lds\n"
"v_mfma_f32_16x16x32_bf16 v[252:255], a[216:219], a[184:187], v[252:255]\n"
"v_add_u32 v15, 0x20000, v15\n"
"s_add_u32 s76, s76, s8\n"
"v_mfma_f32_16x16x32_bf16 v[252:255], a[220:223], a[188:191], v[252:255]\n"
"s_add_u32 s77, s77, s8\n"
"s_cmp_eq_u32 s7, 98304\n"
"s_cselect_b32 s8, 4294868992, s9\n"
"v_mfma_f32_16x16x32_bf16 v[248:251], a[212:215], a[188:191], v[248:251]\n"
"s_add_u32 s7, s7, s8\n"
"v_add_u32 v6, s8, v6\n"
"v_add_u32 v7, s8, v7\n"
"v_mfma_f32_16x16x32_bf16 v[248:251], a[208:211], a[184:187], v[248:251]\n"
"v_add_u32 v8, s8, v8\n"
"v_add_u32 v9, s8, v9\n"
"v_mfma_f32_16x16x32_bf16 v[244:247], a[200:203], a[184:187], v[244:247]\n"
"v_add_u32 v10, s8, v10\n"
"v_mfma_f32_16x16x32_bf16 v[244:247], a[204:207], a[188:191], v[244:247]\n"
"v_mfma_f32_16x16x32_bf16 v[240:243], a[196:199], a[188:191], v[240:243]\n"
"v_mfma_f32_16x16x32_bf16 v[240:243], a[192:195], a[184:187], v[240:243]\n"
"s_waitcnt lgkmcnt(14)\n"
"v_mfma_f32_16x16x32_bf16 v[32:35], a[96:99], a[32:35], v[32:35]\n"
"s_waitcnt vmcnt(12) lgkmcnt(0)\n"
"s_barrier\n"
"v_mfma_f32_16x16x32_bf16 v[32:35], a[100:103], a[36:39], v[32:35]\n"
"ds_read_b128 a[128:131], v6 offset:0\n"
"v_mfma_f32_16x16x32_bf16 v[36:39], a[108:111], a[36:39], v[36:39]\n"
"ds_read_b128 a[132:135], v6 offset:1024\n"
"s_add_u32 m0, s76, 0\n"
"v_mfma_f32_16x16x32_bf16 v[36:39], a[104:107], a[32:35], v[36:39]\n"
"buffer_load_dwordx4 v5, s[56:59], s10 offen offset:0 sc1 lds\n"
"v_mfma_f32_16x16x32_bf16 v[64:67], a[112:115], a[32:35], v[64:67]\n"
"ds_read_b64_tr_b16 a[192:193], v7 offset:0\n"
"v_mfma_f32_16x16x32_bf16 v[64:67], a[116:119], a[36:39], v[64:67]\n"
"ds_read_b64_tr_b16 a[194:195], v7 offset:4096\n"
"v_mfma_f32_16x16x32_bf16 v[68:71], a[124:127], a[36:39], v[68:71]\n"
"v_mfma_f32_16x16x32_bf16 v[68:71], a[120:123], a[32:35], v[68:71]\n"
"ds_read_b64_tr_b16 a[196:197], v7 offset:8192\n"
"v_mfma_f32_16x16x32_bf16 v[132:135], a[120:123], a[40:43], v[132:135]\n"
"buffer_load_dwordx4 v5, s[56:59], s10 offen offset:1024 sc1 lds\n"
"v_mfma_f32_16x16x32_bf16 v[132:135], a[124:127], a[44:47], v[132:135]\n"
"ds_read_b64_tr_b16 a[198:199], v7 offset:12288\n"
"v_mfma_f32_16x16x32_bf16 v[128:131], a[116:119], a[44:47], v[128:131]\n"
"ds_read_b64_tr_b16 a[200:201], v8 offset:0\n"
"v_mfma_f32_16x16x32_bf16 v[128:131], a[112:115], a[40:43], v[128:131]\n"
"ds_read_b64_tr_b16 a[202:203], v8 offset:4096\n"
"v_mfma_f32_16x16x32_bf16 v[100:103], a[104:107], a[40:43], v[100:103]\n"
"buffer_load_dwordx4 v5, s[56:59], s10 offen offset:2048 sc1 lds\n"
"v_mfma_f32_16x16x32_bf16 v[100:103], a[108:111], a[44:47], v[100:103]\n"
"ds_read_b64_tr_b16 a[204:205], v8 offset:8192\n"
"v_mfma_f32_16x16x32_bf16 v[96:99], a[100:103], a[44:47], v[96:99]\n"
"ds_read_b64_tr_b16 a[206:207], v8 offset:12288\n"
"v_mfma_f32_16x16x32_bf16 v[96:99], a[96:99], a[40:43], v[96:99]\n"
"v_mfma_f32_16x16x32_bf16 v[160:163], a[96:99], a[48:51], v[160:163]\n"
"ds_read_b64_tr_b16 a[208:209], v9 offset:0\n"
"v_mfma_f32_16x16x32_bf16 v[160:163], a[100:103], a[52:55], v[160:163]\n"
"ds_read_b64_tr_b16 a[210:211], v9 offset:4096\n"
"v_mfma_f32_16x16x32_bf16 v[164:167], a[108:111], a[52:55], v[164:167]\n"
"buffer_load_dwordx4 v5, s[56:59], s10 offen offset:3072 sc1 lds\n"
"v_mfma_f32_16x16x32_bf16 v[164:167], a[104:107], a[48:51], v[164:167]\n"
"ds_read_b64_tr_b16 a[212:213], v9 offset:8192\n"
"v_mfma_f32_16x16x32_bf16 v[168:171], a[112:115], a[48:51], v[168:171]\n"
"ds_read_b64_tr_b16 a[214:215], v9 offset:12288\n"
"s_add_u32 m0, s76, 4096\n"
"v_mfma_f32_16x16x32_bf16 v[168:171], a[116:119], a[52:55], v[168:171]\n"
"buffer_load_dwordx4 v5, s[56:59], s11 offen offset:0 sc1 lds\n"
"v_mfma_f32_16x16x32_bf16 v[172:175], a[124:127], a[52:55], v[172:175]\n"
"ds_read_b64_tr_b16 a[216:217], v10 offset:0\n"
"v_mfma_f32_16x16x32_bf16 v[172:175], a[120:123], a[48:51], v[172:175]\n"
"ds_read_b64_tr_b16 a[218:219], v10 offset:4096\n"
"v_mfma_f32_16x16x32_bf16 v[188:191], a[120:123], a[56:59], v[188:191]\n"
"v_mfma_f32_16x16x32_bf16 v[188:191], a[124:127], a[60:63], v[188:191]\n"
"ds_read_b64_tr_b16 a[220:221], v10 offset:8192\n"
"v_mfma_f32_16x16x32_bf16 v[184:187], a[116:119], a[60:63], v[184:187]\n"
"ds_read_b64_tr_b16 a[222:223], v10 offset:12288\n"
"v_mfma_f32_16x16x32_bf16 v[184:187], a[112:115], a[56:59], v[184:187]\n"
"buffer_load_dwordx4 v5, s[56:59], s11 offen offset:1024 sc1 lds\n"
"v_mfma_f32_16x16x32_bf16 v[180:183], a[104:107], a[56:59], v[180:183]\n"
"ds_read_b128 a[136:139], v6 offset:2048\n"
"v_mfma_f32_16x16x32_bf16 v[180:183], a[108:111], a[60:63], v[180:183]\n"
"ds_read_b128 a[140:143], v6 offset:3072\n"
"v_mfma_f32_16x16x32_bf16 v[176:179], a[100:103], a[60:63], v[176:179]\n"
"v_mfma_f32_16x16x32_bf16 v[176:179], a[96:99], a[56:59], v[176:179]\n"
"ds_read_b128 a[144:147], v6 offset:4096\n"
"v_mfma_f32_16x16x32_bf16 v[192:195], a[96:99], a[64:67], v[192:195]\n"
"buffer_load_dwordx4 v5, s[56:59], s11 offen offset:2048 sc1 lds\n"
"v_mfma_f32_16x16x32_bf16 v[192:195], a[100:103], a[68:71], v[192:195]\n"
"ds_read_b128 a[148:151], v6 offset:5120\n"
"v_mfma_f32_16x16x32_bf16 v[196:199], a[108:111], a[68:71], v[196:199]\n"
"ds_read_b128 a[152:155], v6 offset:6144\n"
"v_mfma_f32_16x16x32_bf16 v[196:199], a[104:107], a[64:67], v[196:199]\n"
"ds_read_b128 a[156:159], v6 offset:7168\n"
"v_mfma_f32_16x16x32_bf16 v[200:203], a[112:115], a[64:67], v[200:203]\n"
"buffer_load_dwordx4 v5, s[56:59], s11 offen offset:3072 sc1 lds\n"
"v_mfma_f32_16x16x32_bf16 v[200:203], a[116:119], a[68:71], v[200:203]\n"
"ds_read_b128 a[160:163], v6 offset:8192\n"
"v_add_u32 v5, 0x8000, v5\n"
"v_mfma_f32_16x16x32_bf16 v[204:207], a[124:127], a[68:71], v[204:207]\n"
"ds_read_b128 a[164:167], v6 offset:9216\n"
"v_mfma_f32_16x16x32_bf16 v[204:207], a[120:123], a[64:67], v[204:207]\n"
"s_add_u32 m0, s77, 0\n"
"v_mfma_f32_16x16x32_bf16 v[220:223], a[120:123], a[72:75], v[220:223]\n"
"ds_read_b128 a[168:171], v6 offset:10240\n"
"v_mfma_f32_16x16x32_bf16 v[220:223], a[124:127], a[76:79], v[220:223]\n"
"buffer_load_dwordx4 v11, s[60:63], 0 offen offset:0 lds\n"
"v_mfma_f32_16x16x32_bf16 v[216:219], a[116:119], a[76:79], v[216:219]\n"
"ds_read_b128 a[172:175], v6 offset:11264\n"
"v_mfma_f32_16x16x32_bf16 v[216:219], a[112:115], a[72:75], v[216:219]\n"
"ds_read_b128 a[176:179], v6 offset:12288\n"
"v_mfma_f32_16x16x32_bf16 v[212:215], a[104:107], a[72:75], v[212:215]\n"
"ds_read_b128 a[180:183], v6 offset:13312\n"
"v_mfma_f32_16x16x32_bf16 v[212:215], a[108:111], a[76:79], v[212:215]\n"
"buffer_load_dwordx4 v15, s[60:63], 0 offen offset:1024 lds\n"
"v_mfma_f32_16x16x32_bf16 v[208:211], a[100:103], a[76:79], v[208:211]\n"
"ds_read_b128 a[184:187], v6 offset:14336\n"
"v_mfma_f32_16x16x32_bf16 v[208:211], a[96:99], a[72:75], v[208:211]\n"
"ds_read_b128 a[188:191], v6 offset:15360\n"
"v_mfma_f32_16x16x32_bf16 v[224:227], a[96:99], a[80:83], v[224:227]\n"
"v_mfma_f32_16x16x32_bf16 v[224:227], a[100:103], a[84:87], v[224:227]\n"
"v_mfma_f32_16x16x32_bf16 v[228:231], a[108:111], a[84:87], v[228:231]\n"
"buffer_load_dwordx4 v11, s[60:63], s68 offen offset:2048 lds\n"
"v_mfma_f32_16x16x32_bf16 v[228:231], a[104:107], a[80:83], v[228:231]\n"
"v_add_u32 v11, 0x20000, v11\n"
"v_mfma_f32_16x16x32_bf16 v[232:235], a[112:115], a[80:83], v[232:235]\n"
"v_mfma_f32_16x16x32_bf16 v[232:235], a[116:119], a[84:87], v[232:235]\n"
"v_mfma_f32_16x16x32_bf16 v[236:239], a[124:127], a[84:87], v[236:239]\n"
"v_mfma_f32_16x16x32_bf16 v[236:239], a[120:123], a[80:83], v[236:239]\n"
"buffer_load_dwordx4 v15, s[60:63], s68 offen offset:3072 lds\n"
"v_mfma_f32_16x16x32_bf16 v[252:255], a[120:123], a[88:91], v[252:255]\n"
"v_add_u32 v15, 0x20000, v15\n"
"s_add_u32 s76, s76, s8\n"
"v_mfma_f32_16x16x32_bf16 v[252:255], a[124:127], a[92:95], v[252:255]\n"
"s_add_u32 s77, s77, s8\n"
"s_cmp_eq_u32 s7, 98304\n"
"s_cselect_b32 s8, 4294868992, s9\n"
"v_mfma_f32_16x16x32_bf16 v[248:251], a[116:119], a[92:95], v[248:251]\n"
"s_add_u32 s7, s7, s8\n"
"v_add_u32 v6, s8, v6\n"
"v_add_u32 v7, s8, v7\n"
"v_mfma_f32_16x16x32_bf16 v[248:251], a[112:115], a[88:91], v[248:251]\n"
"v_add_u32 v8, s8, v8\n"
"v_add_u32 v9, s8, v9\n"
"v_mfma_f32_16x16x32_bf16 v[244:247], a[104:107], a[88:91], v[244:247]\n"
"v_add_u32 v10, s8, v10\n"
"v_mfma_f32_16x16x32_bf16 v[244:247], a[108:111], a[92:95], v[244:247]\n"
"s_sub_u32 s78, s78, 1\n"
"v_mfma_f32_16x16x32_bf16 v[240:243], a[100:103], a[92:95], v[240:243]\n"
"v_mfma_f32_16x16x32_bf16 v[240:243], a[96:99], a[88:91], v[240:243]\n"
"s_cbranch_scc0 DL_%=\n"
"s_waitcnt lgkmcnt(11)\n"
"v_mfma_f32_16x16x32_bf16 v[160:163], a[192:195], a[144:147], v[160:163]\n"
"s_waitcnt vmcnt(12)\n"
"s_barrier\n"
"s_waitcnt lgkmcnt(10)\n"
"v_mfma_f32_16x16x32_bf16 v[160:163], a[196:199], a[148:151], v[160:163]\n"
"ds_read_b64_tr_b16 a[96:97], v7 offset:0\n"
"v_mfma_f32_16x16x32_bf16 v[164:167], a[204:207], a[148:151], v[164:167]\n"
"ds_read_b64_tr_b16 a[98:99], v7 offset:4096\n"
"v_mfma_f32_16x16x32_bf16 v[164:167], a[200:203], a[144:147], v[164:167]\n"
"ds_read_b64_tr_b16 a[100:101], v7 offset:8192\n"
"v_mfma_f32_16x16x32_bf16 v[168:171], a[208:211], a[144:147], v[168:171]\n"
"ds_read_b64_tr_b16 a[102:103], v7 offset:12288\n"
"v_mfma_f32_16x16x32_bf16 v[168:171], a[212:215], a[148:151], v[168:171]\n"
"ds_read_b64_tr_b16 a[104:105], v8 offset:0\n"
"v_mfma_f32_16x16x32_bf16 v[172:175], a[220:223], a[148:151], v[172:175]\n"
"ds_read_b64_tr_b16 a[106:107], v8 offset:4096\n"
"v_mfma_f32_16x16x32_bf16 v[172:175], a[216:219], a[144:147], v[172:175]\n"
"ds_read_b64_tr_b16 a[108:109], v8 offset:8192\n"
"s_waitcnt lgkmcnt(14)\n"
"v_mfma_f32_16x16x32_bf16 v[188:191], a[216:219], a[152:155], v[188:191]\n"
"ds_read_b64_tr_b16 a[110:111], v8 offset:12288\n"
"v_mfma_f32_16x16x32_bf16 v[188:191], a[220:223], a[156:159], v[188:191]\n"
"ds_read_b64_tr_b16 a[112:113], v9 offset:0\n"
"v_mfma_f32_16x16x32_bf16 v[184:187], a[212:215], a[156:159], v[184:187]\n"
"ds_read_b64_tr_b16 a[114:115], v9 offset:4096\n"
"v_mfma_f32_16x16x32_bf16 v[184:187], a[208:211], a[152:155], v[184:187]\n"
"ds_read_b64_tr_b16 a[116:117], v9 offset:8192\n"
"v_mfma_f32_16x16x32_bf16 v[180:183], a[200:203], a[152:155], v[180:183]\n"
"ds_read_b64_tr_b16 a[118:119], v9 offset:12288\n"
"v_mfma_f32_16x16x32_bf16 v[180:183], a[204:207], a[156:159], v[180:183]\n"
"ds_read_b64_tr_b16 a[120:121], v10 offset:0\n"
"v_mfma_f32_16x16x32_bf16 v[176:179], a[196:199], a[156:159], v[176:179]\n"
"ds_read_b64_tr_b16 a[122:123], v10 offset:4096\n"
"v_mfma_f32_16x16x32_bf16 v[176:179], a[192:195], a[152:155], v[176:179]\n"
"ds_read_b64_tr_b16 a[124:125], v10 offset:8192\n"
"v_mfma_f32_16x16x32_bf16 v[192:195], a[192:195], a[160:163], v[192:195]\n"
"ds_read_b64_tr_b16 a[126:127], v10 offset:12288\n"
"s_waitcnt lgkmcnt(14)\n"
"v_mfma_f32_16x16x32_bf16 v[192:195], a[196:199], a[164:167], v[192:195]\n"
"ds_read_b128 a[44:47], v6 offset:7168\n"
"v_mfma_f32_16x16x32_bf16 v[196:199], a[204:207], a[164:167], v[196:199]\n"
"ds_read_b128 a[40:43], v6 offset:6144\n"
"v_mfma_f32_16x16x32_bf16 v[196:199], a[200:203], a[160:163], v[196:199]\n"
"ds_read_b128 a[36:39], v6 offset:5120\n"
"v_mfma_f32_16x16x32_bf16 v[200:203], a[208:211], a[160:163], v[200:203]\n"
"ds_read_b128 a[32:35], v6 offset:4096\n"
"v_mfma_f32_16x16x32_bf16 v[200:203], a[212:215], a[164:167], v[200:203]\n"
"ds_read_b128 a[60:63], v6 offset:11264\n"
"v_mfma_f32_16x16x32_bf16 v[204:207], a[220:223], a[164:167], v[204:207]\n"
"ds_read_b128 a[56:59], v6 offset:10240\n"
"v_mfma_f32_16x16x32_bf16 v[204:207], a[216:219], a[160:163], v[204:207]\n"
"ds_read_b128 a[52:55], v6 offset:9216\n"
"v_mfma_f32_16x16x32_bf16 v[220:223], a[216:219], a[168:171], v[220:223]\n"
"ds_read_b128 a[48:51], v6 offset:8192\n"
"v_mfma_f32_16x16x32_bf16 v[220:223], a[220:223], a[172:175], v[220:223]\n"
"v_mul_f32 v120, v120, v28\n"
"v_mul_f32 v121, v121, v28\n"
"v_mfma_f32_16x16x32_bf16 v[216:219], a[212:215], a[172:175], v[216:219]\n"
"v_mul_f32 v122, v122, v28\n"
"v_mul_f32 v123, v123, v28\n"
"v_mfma_f32_16x16x32_bf16 v[216:219], a[208:211], a[168:171], v[216:219]\n"
"v_mul_f32 v124, v124, v28\n"
"v_mul_f32 v125, v125, v28\n"
"v_mfma_f32_16x16x32_bf16 v[212:215], a[200:203], a[168:171], v[212:215]\n"
"v_mul_f32 v126, v126, v28\n"
"v_mul_f32 v127, v127, v28\n"
"v_mfma_f32_16x16x32_bf16 v[212:215], a[204:207], a[172:175], v[212:215]\n"
"v_mul_f32 v136, v136, v28\n"
"v_mfma_f32_16x16x32_bf16 v[208:211], a[196:199], a[172:175], v[208:211]\n"
"v_mul_f32 v137, v137, v28\n"
"v_mul_f32 v138, v138, v28\n"
"v_mfma_f32_16x16x32_bf16 v[208:211], a[192:195], a[168:171], v[208:211]\n"
"v_mul_f32 v139, v139, v28\n"
"v_mul_f32 v140, v140, v28\n"
"s_waitcnt lgkmcnt(4)\n"
"v_mfma_f32_16x16x32_bf16 v[160:163], a[96:99], a[32:35], v[160:163]\n"
"v_mul_f32 v141, v141, v28\n"
"v_mul_f32 v142, v142, v28\n"
"v_mfma_f32_16x16x32_bf16 v[160:163], a[100:103], a[36:39], v[160:163]\n"
"v_mul_f32 v143, v143, v28\n"
"v_mfma_f32_16x16x32_bf16 v[164:167], a[108:111], a[36:39], v[164:167]\n"
"v_mul_f32 v144, v144, v30\n"
"v_mul_f32 v145, v145, v30\n"
"v_mfma_f32_16x16x32_bf16 v[164:167], a[104:107], a[32:35], v[164:167]\n"
"v_mul_f32 v146, v146, v30\n"
"v_mul_f32 v147, v147, v30\n"
"v_mfma_f32_16x16x32_bf16 v[168:171], a[112:115], a[32:35], v[168:171]\n"
"v_mul_f32 v148, v148, v30\n"
"v_mul_f32 v149, v149, v30\n"
"v_mfma_f32_16x16x32_bf16 v[168:171], a[116:119], a[36:39], v[168:171]\n"
"v_mul_f32 v150, v150, v30\n"
"v_mul_f32 v151, v151, v30\n"
"v_mfma_f32_16x16x32_bf16 v[172:175], a[124:127], a[36:39], v[172:175]\n"
"v_mul_f32 v152, v152, v30\n"
"v_mfma_f32_16x16x32_bf16 v[172:175], a[120:123], a[32:35], v[172:175]\n"
"v_mul_f32 v153, v153, v30\n"
"v_mul_f32 v154, v154, v30\n"
"v_mfma_f32_16x16x32_bf16 v[188:191], a[120:123], a[40:43], v[188:191]\n"
"v_mul_f32 v155, v155, v30\n"
"v_mul_f32 v156, v156, v30\n"
"v_mfma_f32_16x16x32_bf16 v[188:191], a[124:127], a[44:47], v[188:191]\n"
"v_mul_f32 v157, v157, v30\n"
"v_mul_f32 v158, v158, v30\n"
"v_mfma_f32_16x16x32_bf16 v[184:187], a[116:119], a[44:47], v[184:187]\n"
"v_mul_f32 v159, v159, v30\n"
"v_mfma_f32_16x16x32_bf16 v[184:187], a[112:115], a[40:43], v[184:187]\n"
"s_waitcnt vmcnt(0)\n"
"s_barrier\n"
"v_mfma_f32_16x16x32_bf16 v[180:183], a[104:107], a[40:43], v[180:183]\n"
"ds_read_b64_tr_b16 a[64:65], v7 offset:49152\n"
"v_mfma_f32_16x16x32_bf16 v[180:183], a[108:111], a[44:47], v[180:183]\n"
"ds_read_b64_tr_b16 a[66:67], v7 offset:53248\n"
"v_mfma_f32_16x16x32_bf16 v[176:179], a[100:103], a[44:47], v[176:179]\n"
"ds_read_b64_tr_b16 a[68:69], v7 offset:57344\n"
"v_mfma_f32_16x16x32_bf16 v[176:179], a[96:99], a[40:43], v[176:179]\n"
"ds_read_b64_tr_b16 a[70:71], v7 offset:61440\n"
"s_waitcnt lgkmcnt(4)\n"
"v_mfma_f32_16x16x32_bf16 v[192:195], a[96:99], a[48:51], v[192:195]\n"
"ds_read_b64_tr_b16 a[72:73], v8 offset:49152\n"
"v_mfma_f32_16x16x32_bf16 v[192:195], a[100:103], a[52:55], v[192:195]\n"
"ds_read_b64_tr_b16 a[74:75], v8 offset:53248\n"
"v_mfma_f32_16x16x32_bf16 v[196:199], a[108:111], a[52:55], v[196:199]\n"
"ds_read_b64_tr_b16 a[76:77], v8 offset:57344\n"
"v_mfma_f32_16x16x32_bf16 v[196:199], a[104:107], a[48:51], v[196:199]\n"
"ds_read_b64_tr_b16 a[78:79], v8 offset:61440\n"
"v_mfma_f32_16x16x32_bf16 v[200:203], a[112:115], a[48:51], v[200:203]\n"
"ds_read_b64_tr_b16 a[80:81], v9 offset:49152\n"
"v_mfma_f32_16x16x32_bf16 v[200:203], a[116:119], a[52:55], v[200:203]\n"
"ds_read_b64_tr_b16 a[82:83], v9 offset:53248\n"
"v_mfma_f32_16x16x32_bf16 v[204:207], a[124:127], a[52:55], v[204:207]\n"
"ds_read_b64_tr_b16 a[84:85], v9 offset:57344\n"
"v_mfma_f32_16x16x32_bf16 v[204:207], a[120:123], a[48:51], v[204:207]\n"
"ds_read_b64_tr_b16 a[86:87], v9 offset:61440\n"
"v_mfma_f32_16x16x32_bf16 v[220:223], a[120:123], a[56:59], v[220:223]\n"
"ds_read_b64_tr_b16 a[88:89], v10 offset:49152\n"
"v_mfma_f32_16x16x32_bf16 v[220:223], a[124:127], a[60:63], v[220:223]\n"
"ds_read_b64_tr_b16 a[90:91], v10 offset:53248\n"
"v_mfma_f32_16x16x32_bf16 v[216:219], a[116:119], a[60:63], v[216:219]\n"
"ds_read_b64_tr_b16 a[92:93], v10 offset:57344\n"
"v_mfma_f32_16x16x32_bf16 v[216:219], a[112:115], a[56:59], v[216:219]\n"
"ds_read_b64_tr_b16 a[94:95], v10 offset:61440\n"
"v_mfma_f32_16x16x32_bf16 v[212:215], a[104:107], a[56:59], v[212:215]\n"
"ds_read_b128 a[156:159], v6 offset:56320\n"
"v_mfma_f32_16x16x32_bf16 v[212:215], a[108:111], a[60:63], v[212:215]\n"
"ds_read_b128 a[152:155], v6 offset:55296\n"
"v_mfma_f32_16x16x32_bf16 v[208:211], a[100:103], a[60:63], v[208:211]\n"
"ds_read_b128 a[148:151], v6 offset:54272\n"
"v_mfma_f32_16x16x32_bf16 v[208:211], a[96:99], a[56:59], v[208:211]\n"
"ds_read_b128 a[144:147], v6 offset:53248\n"
"v_mfma_f32_16x16x32_bf16 v[224:227], a[192:195], a[176:179], v[224:227]\n"
"ds_read_b128 a[172:175], v6 offset:60416\n"
"v_mfma_f32_16x16x32_bf16 v[224:227], a[196:199], a[180:183], v[224:227]\n"
"ds_read_b128 a[168:171], v6 offset:59392\n"
"v_mfma_f32_16x16x32_bf16 v[228:231], a[204:207], a[180:183], v[228:231]\n"
"ds_read_b128 a[164:167], v6 offset:58368\n"
"v_mfma_f32_16x16x32_bf16 v[228:231], a[200:203], a[176:179], v[228:231]\n"
"ds_read_b128 a[160:163], v6 offset:57344\n"
"v_mfma_f32_16x16x32_bf16 v[232:235], a[208:211], a[176:179], v[232:235]\n"
"ds_read_b128 a[44:47], v6 offset:15360\n"
"v_mfma_f32_16x16x32_bf16 v[232:235], a[212:215], a[180:183], v[232:235]\n"
"ds_read_b128 a[40:43], v6 offset:14336\n"
"v_mfma_f32_16x16x32_bf16 v[236:239], a[220:223], a[180:183], v[236:239]\n"
"ds_read_b128 a[36:39], v6 offset:13312\n"
"v_mfma_f32_16x16x32_bf16 v[236:239], a[216:219], a[176:179], v[236:239]\n"
"ds_read_b128 a[32:35], v6 offset:12288\n"
"v_mfma_f32_16x16x32_bf16 v[252:255], a[216:219], a[184:187], v[252:255]\n"
"ds_read_b128 a[60:63], v6 offset:64512\n"
"v_mfma_f32_16x16x32_bf16 v[252:255], a[220:223], a[188:191], v[252:255]\n"
"ds_read_b128 a[56:59], v6 offset:63488\n"
"v_mfma_f32_16x16x32_bf16 v[248:251], a[212:215], a[188:191], v[248:251]\n"
"ds_read_b128 a[52:55], v6 offset:62464\n"
"v_mfma_f32_16x16x32_bf16 v[248:251], a[208:211], a[184:187], v[248:251]\n"
"ds_read_b128 a[48:51], v6 offset:61440\n"
"v_mfma_f32_16x16x32_bf16 v[244:247], a[200:203], a[184:187], v[244:247]\n"
"v_mfma_f32_16x16x32_bf16 v[244:247], a[204:207], a[188:191], v[244:247]\n"
"v_mfma_f32_16x16x32_bf16 v[240:243], a[196:199], a[188:191], v[240:243]\n"
"v_mfma_f32_16x16x32_bf16 v[240:243], a[192:195], a[184:187], v[240:243]\n"
"s_waitcnt lgkmcnt(12)\n"
"v_mfma_f32_16x16x32_bf16 v[160:163], a[64:67], a[144:147], v[160:163]\n"
"v_mfma_f32_16x16x32_bf16 v[160:163], a[68:71], a[148:151], v[160:163]\n"
"v_mfma_f32_16x16x32_bf16 v[164:167], a[76:79], a[148:151], v[164:167]\n"
"v_mfma_f32_16x16x32_bf16 v[164:167], a[72:75], a[144:147], v[164:167]\n"
"ds_read_b128 a[188:191], v6 offset:3072\n"
"v_mfma_f32_16x16x32_bf16 v[168:171], a[80:83], a[144:147], v[168:171]\n"
"ds_read_b128 a[184:187], v6 offset:2048\n"
"v_mfma_f32_16x16x32_bf16 v[168:171], a[84:87], a[148:151], v[168:171]\n"
"ds_read_b128 a[180:183], v6 offset:1024\n"
"v_mfma_f32_16x16x32_bf16 v[172:175], a[92:95], a[148:151], v[172:175]\n"
"ds_read_b128 a[176:179], v6 offset:0\n"
"v_mfma_f32_16x16x32_bf16 v[172:175], a[88:91], a[144:147], v[172:175]\n"
"v_mfma_f32_16x16x32_bf16 v[188:191], a[88:91], a[152:155], v[188:191]\n"
"v_mfma_f32_16x16x32_bf16 v[188:191], a[92:95], a[156:159], v[188:191]\n"
"v_fma_f32 v160, v160, v21, v40\n"
"v_fma_f32 v161, v161, v21, v41\n"
"v_mfma_f32_16x16x32_bf16 v[184:187], a[84:87], a[156:159], v[184:187]\n"
"v_fma_f32 v162, v162, v21, v42\n"
"v_fma_f32 v163, v163, v21, v43\n"
"v_mfma_f32_16x16x32_bf16 v[184:187], a[80:83], a[152:155], v[184:187]\n"
"v_fma_f32 v164, v164, v21, v44\n"
"v_fma_f32 v165, v165, v21, v45\n"
"v_mfma_f32_16x16x32_bf16 v[180:183], a[72:75], a[152:155], v[180:183]\n"
"v_fma_f32 v166, v166, v21, v46\n"
"v_fma_f32 v167, v167, v21, v47\n"
"v_mfma_f32_16x16x32_bf16 v[180:183], a[76:79], a[156:159], v[180:183]\n"
"v_cvt_pk_bf16_f32 v160, v160, v161\n"
"v_cvt_pk_bf16_f32 v161, v162, v163\n"
"v_mfma_f32_16x16x32_bf16 v[176:179], a[68:71], a[156:159], v[176:179]\n"
"v_cvt_pk_bf16_f32 v162, v164, v165\n"
"v_cvt_pk_bf16_f32 v163, v166, v167\n"
"v_mfma_f32_16x16x32_bf16 v[176:179], a[64:67], a[152:155], v[176:179]\n"
"buffer_store_dwordx4 v[160:163], v2, s[56:59], s65 offen offset:0 sc1\n"
"v_fma_f32 v168, v168, v21, v48\n"
"v_fma_f32 v169, v169, v21, v49\n"
"v_fma_f32 v170, v170, v21, v50\n"
"s_waitcnt lgkmcnt(12)\n"
"v_mfma_f32_16x16x32_bf16 v[192:195], a[64:67], a[160:163], v[192:195]\n"
"v_fma_f32 v171, v171, v21, v51\n"
"v_fma_f32 v172, v172, v21, v52\n"
"v_mfma_f32_16x16x32_bf16 v[192:195], a[68:71], a[164:167], v[192:195]\n"
"ds_read_b128 a[156:159], v6 offset:52224\n"
"v_fma_f32 v173, v173, v21, v53\n"
"v_mfma_f32_16x16x32_bf16 v[196:199], a[76:79], a[164:167], v[196:199]\n"
"v_fma_f32 v174, v174, v21, v54\n"
"v_mfma_f32_16x16x32_bf16 v[196:199], a[72:75], a[160:163], v[196:199]\n"
"ds_read_b128 a[152:155], v6 offset:51200\n"
"v_fma_f32 v175, v175, v21, v55\n"
"v_cvt_pk_bf16_f32 v168, v168, v169\n"
"v_mfma_f32_16x16x32_bf16 v[200:203], a[80:83], a[160:163], v[200:203]\n"
"ds_read_b128 a[148:151], v6 offset:50176\n"
"v_cvt_pk_bf16_f32 v169, v170, v171\n"
"v_cvt_pk_bf16_f32 v170, v172, v173\n"
"v_mfma_f32_16x16x32_bf16 v[200:203], a[84:87], a[164:167], v[200:203]\n"
"ds_read_b128 a[144:147], v6 offset:49152\n"
"v_cvt_pk_bf16_f32 v171, v174, v175\n"
"v_mfma_f32_16x16x32_bf16 v[204:207], a[92:95], a[164:167], v[204:207]\n"
"buffer_store_dwordx4 v[168:171], v2, s[56:59], s65 offen offset:1024 sc1\n"
"v_fma_f32 v184, v184, v23, v72\n"
"v_fma_f32 v185, v185, v23, v73\n"
"v_fma_f32 v186, v186, v23, v74\n"
"v_mfma_f32_16x16x32_bf16 v[204:207], a[88:91], a[160:163], v[204:207]\n"
"v_fma_f32 v187, v187, v23, v75\n"
"v_fma_f32 v188, v188, v23, v76\n"
"v_mfma_f32_16x16x32_bf16 v[220:223], a[88:91], a[168:171], v[220:223]\n"
"v_fma_f32 v189, v189, v23, v77\n"
"v_fma_f32 v190, v190, v23, v78\n"
"v_mfma_f32_16x16x32_bf16 v[220:223], a[92:95], a[172:175], v[220:223]\n"
"v_fma_f32 v191, v191, v23, v79\n"
"v_cvt_pk_bf16_f32 v184, v184, v185\n"
"v_mfma_f32_16x16x32_bf16 v[216:219], a[84:87], a[172:175], v[216:219]\n"
"v_cvt_pk_bf16_f32 v185, v186, v187\n"
"v_cvt_pk_bf16_f32 v186, v188, v189\n"
"v_mfma_f32_16x16x32_bf16 v[216:219], a[80:83], a[168:171], v[216:219]\n"
"v_cvt_pk_bf16_f32 v187, v190, v191\n"
"v_mfma_f32_16x16x32_bf16 v[212:215], a[72:75], a[168:171], v[212:215]\n"
"buffer_store_dwordx4 v[184:187], v2, s[56:59], s65 offen offset:3072 sc1\n"
"v_fma_f32 v176, v176, v23, v56\n"
"v_fma_f32 v177, v177, v23, v57\n"
"v_fma_f32 v178, v178, v23, v58\n"
"v_mfma_f32_16x16x32_bf16 v[212:215], a[76:79], a[172:175], v[212:215]\n"
"v_fma_f32 v179, v179, v23, v59\n"
"v_fma_f32 v180, v180, v23, v60\n"
"v_mfma_f32_16x16x32_bf16 v[208:211], a[68:71], a[172:175], v[208:211]\n"
"v_fma_f32 v181, v181, v23, v61\n"
"v_fma_f32 v182, v182, v23, v62\n"
"v_mfma_f32_16x16x32_bf16 v[208:211], a[64:67], a[168:171], v[208:211]\n"
"v_fma_f32 v183, v183, v23, v63\n"
"v_cvt_pk_bf16_f32 v176, v176, v177\n"
"s_waitcnt lgkmcnt(12)\n"
"v_mfma_f32_16x16x32_bf16 v[224:227], a[96:99], a[32:35], v[224:227]\n"
"v_cvt_pk_bf16_f32 v177, v178, v179\n"
"v_cvt_pk_bf16_f32 v178, v180, v181\n"
"v_mfma_f32_16x16x32_bf16 v[224:227], a[100:103], a[36:39], v[224:227]\n"
"v_cvt_pk_bf16_f32 v179, v182, v183\n"
"v_mfma_f32_16x16x32_bf16 v[228:231], a[108:111], a[36:39], v[228:231]\n"
"buffer_store_dwordx4 v[176:179], v2, s[56:59], s65 offen offset:2048 sc1\n"
"v_fma_f32 v192, v192, v25, v80\n"
"v_fma_f32 v193, v193, v25, v81\n"
"v_fma_f32 v194, v194, v25, v82\n"
"v_mfma_f32_16x16x32_bf16 v[228:231], a[104:107], a[32:35], v[228:231]\n"
"v_fma_f32 v195, v195, v25, v83\n"
"v_fma_f32 v196, v196, v25, v84\n"
"v_mfma_f32_16x16x32_bf16 v[232:235], a[112:115], a[32:35], v[232:235]\n"
"v_fma_f32 v197, v197, v25, v85\n"
"v_fma_f32 v198, v198, v25, v86\n"
"v_mfma_f32_16x16x32_bf16 v[232:235], a[116:119], a[36:39], v[232:235]\n"
"v_fma_f32 v199, v199, v25, v87\n"
"v_cvt_pk_bf16_f32 v192, v192, v193\n"
"v_mfma_f32_16x16x32_bf16 v[236:239], a[124:127], a[36:39], v[236:239]\n"
"v_cvt_pk_bf16_f32 v193, v194, v195\n"
"v_cvt_pk_bf16_f32 v194, v196, v197\n"
"v_mfma_f32_16x16x32_bf16 v[236:239], a[120:123], a[32:35], v[236:239]\n"
"v_cvt_pk_bf16_f32 v195, v198, v199\n"
"v_mfma_f32_16x16x32_bf16 v[252:255], a[120:123], a[40:43], v[252:255]\n"
"buffer_store_dwordx4 v[192:195], v2, s[56:59], s66 offen offset:0 sc1\n"
"v_fma_f32 v200, v200, v25, v88\n"
"v_fma_f32 v201, v201, v25, v89\n"
"v_fma_f32 v202, v202, v25, v90\n"
"v_mfma_f32_16x16x32_bf16 v[252:255], a[124:127], a[44:47], v[252:255]\n"
"v_fma_f32 v203, v203, v25, v91\n"
"v_fma_f32 v204, v204, v25, v92\n"
"v_mfma_f32_16x16x32_bf16 v[248:251], a[116:119], a[44:47], v[248:251]\n"
"v_fma_f32 v205, v205, v25, v93\n"
"v_fma_f32 v206, v206, v25, v94\n"
"v_mfma_f32_16x16x32_bf16 v[248:251], a[112:115], a[40:43], v[248:251]\n"
"v_fma_f32 v207, v207, v25, v95\n"
"v_cvt_pk_bf16_f32 v200, v200, v201\n"
"v_mfma_f32_16x16x32_bf16 v[244:247], a[104:107], a[40:43], v[244:247]\n"
"v_cvt_pk_bf16_f32 v201, v202, v203\n"
"v_cvt_pk_bf16_f32 v202, v204, v205\n"
"v_mfma_f32_16x16x32_bf16 v[244:247], a[108:111], a[44:47], v[244:247]\n"
"v_cvt_pk_bf16_f32 v203, v206, v207\n"
"v_mfma_f32_16x16x32_bf16 v[240:243], a[100:103], a[44:47], v[240:243]\n"
"buffer_store_dwordx4 v[200:203], v2, s[56:59], s66 offen offset:1024 sc1\n"
"v_fma_f32 v216, v216, v27, v112\n"
"v_fma_f32 v217, v217, v27, v113\n"
"v_fma_f32 v218, v218, v27, v114\n"
"v_mfma_f32_16x16x32_bf16 v[240:243], a[96:99], a[40:43], v[240:243]\n"
"v_fma_f32 v219, v219, v27, v115\n"
"v_fma_f32 v220, v220, v27, v116\n"
"s_waitcnt lgkmcnt(8)\n"
"v_mfma_f32_16x16x32_bf16 v[224:227], a[64:67], a[48:51], v[224:227]\n"
"v_fma_f32 v221, v221, v27, v117\n"
"v_fma_f32 v222, v222, v27, v118\n"
"v_mfma_f32_16x16x32_bf16 v[224:227], a[68:71], a[52:55], v[224:227]\n"
"v_fma_f32 v223, v223, v27, v119\n"
"v_cvt_pk_bf16_f32 v216, v216, v217\n"
"v_mfma_f32_16x16x32_bf16 v[228:231], a[76:79], a[52:55], v[228:231]\n"
"v_cvt_pk_bf16_f32 v217, v218, v219\n"
"v_cvt_pk_bf16_f32 v218, v220, v221\n"
"v_mfma_f32_16x16x32_bf16 v[228:231], a[72:75], a[48:51], v[228:231]\n"
"v_cvt_pk_bf16_f32 v219, v222, v223\n"
"v_mfma_f32_16x16x32_bf16 v[232:235], a[80:83], a[48:51], v[232:235]\n"
"buffer_store_dwordx4 v[216:219], v2, s[56:59], s66 offen offset:3072 sc1\n"
"v_fma_f32 v208, v208, v27, v104\n"
"v_fma_f32 v209, v209, v27, v105\n"
"v_fma_f32 v210, v210, v27, v106\n"
"v_mfma_f32_16x16x32_bf16 v[232:235], a[84:87], a[52:55], v[232:235]\n"
"v_fma_f32 v211, v211, v27, v107\n"
"v_fma_f32 v212, v212, v27, v108\n"
"v_mfma_f32_16x16x32_bf16 v[236:239], a[92:95], a[52:55], v[236:239]\n"
"v_fma_f32 v213, v213, v27, v109\n"
"v_fma_f32 v214, v214, v27, v110\n"
"v_mfma_f32_16x16x32_bf16 v[236:239], a[88:91], a[48:51], v[236:239]\n"
"v_fma_f32 v215, v215, v27, v111\n"
"v_cvt_pk_bf16_f32 v208, v208, v209\n"
"v_mfma_f32_16x16x32_bf16 v[252:255], a[88:91], a[56:59], v[252:255]\n"
"v_cvt_pk_bf16_f32 v209, v210, v211\n"
"v_cvt_pk_bf16_f32 v210, v212, v213\n"
"v_mfma_f32_16x16x32_bf16 v[252:255], a[92:95], a[60:63], v[252:255]\n"
"v_cvt_pk_bf16_f32 v211, v214, v215\n"
"v_mfma_f32_16x16x32_bf16 v[248:251], a[84:87], a[60:63], v[248:251]\n"
"buffer_store_dwordx4 v[208:211], v2, s[56:59], s66 offen offset:2048 sc1\n"
"v_fma_f32 v224, v224, v29, v120\n"
"v_fma_f32 v225, v225, v29, v121\n"
"v_fma_f32 v226, v226, v29, v122\n"
"v_mfma_f32_16x16x32_bf16 v[248:251], a[80:83], a[56:59], v[248:251]\n"
"v_fma_f32 v227, v227, v29, v123\n"
"v_fma_f32 v228, v228, v29, v124\n"
"v_fma_f32 v229, v229, v29, v125\n"
"v_mfma_f32_16x16x32_bf16 v[244:247], a[72:75], a[56:59], v[244:247]\n"
"v_fma_f32 v230, v230, v29, v126\n"
"v_fma_f32 v231, v231, v29, v127\n"
"v_cvt_pk_bf16_f32 v224, v224, v225\n"
"v_mfma_f32_16x16x32_bf16 v[244:247], a[76:79], a[60:63], v[244:247]\n"
"v_cvt_pk_bf16_f32 v225, v226, v227\n"
"v_cvt_pk_bf16_f32 v226, v228, v229\n"
"v_cvt_pk_bf16_f32 v227, v230, v231\n"
"v_mfma_f32_16x16x32_bf16 v[240:243], a[68:71], a[60:63], v[240:243]\n"
"buffer_store_dwordx4 v[224:227], v2, s[56:59], s67 offen offset:0 sc1\n"
"v_fma_f32 v232, v232, v29, v136\n"
"v_fma_f32 v233, v233, v29, v137\n"
"v_fma_f32 v234, v234, v29, v138\n"
"v_mfma_f32_16x16x32_bf16 v[240:243], a[64:67], a[56:59], v[240:243]\n"
"v_fma_f32 v235, v235, v29, v139\n"
"v_fma_f32 v236, v236, v29, v140\n"
"v_mfma_f32_16x16x32_bf16 v[32:35], a[192:195], a[128:131], v[32:35]\n"
"v_fma_f32 v237, v237, v29, v141\n"
"v_fma_f32 v238, v238, v29, v142\n"
"v_fma_f32 v239, v239, v29, v143\n"
"v_mfma_f32_16x16x32_bf16 v[32:35], a[196:199], a[132:135], v[32:35]\n"
"v_cvt_pk_bf16_f32 v232, v232, v233\n"
"v_cvt_pk_bf16_f32 v233, v234, v235\n"
"v_cvt_pk_bf16_f32 v234, v236, v237\n"
"v_mfma_f32_16x16x32_bf16 v[36:39], a[204:207], a[132:135], v[36:39]\n"
"v_cvt_pk_bf16_f32 v235, v238, v239\n"
"v_mfma_f32_16x16x32_bf16 v[36:39], a[200:203], a[128:131], v[36:39]\n"
"buffer_store_dwordx4 v[232:235], v2, s[56:59], s67 offen offset:1024 sc1\n"
"v_fma_f32 v248, v248, v31, v152\n"
"v_fma_f32 v249, v249, v31, v153\n"
"v_fma_f32 v250, v250, v31, v154\n"
"v_mfma_f32_16x16x32_bf16 v[64:67], a[208:211], a[128:131], v[64:67]\n"
"v_fma_f32 v251, v251, v31, v155\n"
"v_fma_f32 v252, v252, v31, v156\n"
"v_fma_f32 v253, v253, v31, v157\n"
"v_mfma_f32_16x16x32_bf16 v[64:67], a[212:215], a[132:135], v[64:67]\n"
"v_fma_f32 v254, v254, v31, v158\n"
"v_fma_f32 v255, v255, v31, v159\n"
"v_cvt_pk_bf16_f32 v248, v248, v249\n"
"v_mfma_f32_16x16x32_bf16 v[68:71], a[220:223], a[132:135], v[68:71]\n"
"v_cvt_pk_bf16_f32 v249, v250, v251\n"
"v_cvt_pk_bf16_f32 v250, v252, v253\n"
"v_cvt_pk_bf16_f32 v251, v254, v255\n"
"v_mfma_f32_16x16x32_bf16 v[68:71], a[216:219], a[128:131], v[68:71]\n"
"buffer_store_dwordx4 v[248:251], v2, s[56:59], s67 offen offset:3072 sc1\n"
"v_fma_f32 v240, v240, v31, v144\n"
"v_fma_f32 v241, v241, v31, v145\n"
"v_fma_f32 v242, v242, v31, v146\n"
"v_mfma_f32_16x16x32_bf16 v[132:135], a[216:219], a[136:139], v[132:135]\n"
"v_fma_f32 v243, v243, v31, v147\n"
"v_fma_f32 v244, v244, v31, v148\n"
"v_mfma_f32_16x16x32_bf16 v[132:135], a[220:223], a[140:143], v[132:135]\n"
"v_fma_f32 v245, v245, v31, v149\n"
"v_fma_f32 v246, v246, v31, v150\n"
"v_fma_f32 v247, v247, v31, v151\n"
"v_mfma_f32_16x16x32_bf16 v[128:131], a[212:215], a[140:143], v[128:131]\n"
"v_cvt_pk_bf16_f32 v240, v240, v241\n"
"v_cvt_pk_bf16_f32 v241, v242, v243\n"
"v_cvt_pk_bf16_f32 v242, v244, v245\n"
"v_mfma_f32_16x16x32_bf16 v[128:131], a[208:211], a[136:139], v[128:131]\n"
"v_cvt_pk_bf16_f32 v243, v246, v247\n"
"v_mfma_f32_16x16x32_bf16 v[100:103], a[200:203], a[136:139], v[100:103]\n"
"buffer_store_dwordx4 v[240:243], v2, s[56:59], s67 offen offset:2048 sc1\n"
"v_accvgpr_read_b32 v40, a224\n"
"v_accvgpr_read_b32 v41, a225\n"
"v_mfma_f32_16x16x32_bf16 v[100:103], a[204:207], a[140:143], v[100:103]\n"
"s_waitcnt vmcnt(8)\n"
"v_mfma_f32_16x16x32_bf16 v[96:99], a[196:199], a[140:143], v[96:99]\n"
"s_mov_b64 exec, 2\n"
"buffer_store_dword v12, v4, s[56:59], 0 offen sc1\n"
"s_mov_b64 exec, -1\n"
"v_accvgpr_read_b32 v42, a226\n"
"v_accvgpr_read_b32 v43, a227\n"
"v_mfma_f32_16x16x32_bf16 v[96:99], a[192:195], a[136:139], v[96:99]\n"
"v_accvgpr_read_b32 v44, a228\n"
"v_accvgpr_read_b32 v45, a229\n"
"s_waitcnt lgkmcnt(4)\n"
"v_mfma_f32_16x16x32_bf16 v[32:35], a[96:99], a[176:179], v[32:35]\n"
"v_accvgpr_read_b32 v46, a230\n"
"v_accvgpr_read_b32 v47, a231\n"
"v_mfma_f32_16x16x32_bf16 v[32:35], a[100:103], a[180:183], v[32:35]\n"
"v_accvgpr_read_b32 v48, a232\n"
"v_accvgpr_read_b32 v49, a233\n"
"v_mfma_f32_16x16x32_bf16 v[36:39], a[108:111], a[180:183], v[36:39]\n"
"v_accvgpr_read_b32 v50, a234\n"
"v_accvgpr_read_b32 v51, a235\n"
"v_mfma_f32_16x16x32_bf16 v[36:39], a[104:107], a[176:179], v[36:39]\n"
"v_accvgpr_read_b32 v52, a236\n"
"v_accvgpr_read_b32 v53, a237\n"
"v_mfma_f32_16x16x32_bf16 v[64:67], a[112:115], a[176:179], v[64:67]\n"
"v_accvgpr_read_b32 v54, a238\n"
"v_accvgpr_read_b32 v55, a239\n"
"v_mfma_f32_16x16x32_bf16 v[64:67], a[116:119], a[180:183], v[64:67]\n"
"v_accvgpr_read_b32 v72, a240\n"
"v_accvgpr_read_b32 v73, a241\n"
"v_mfma_f32_16x16x32_bf16 v[68:71], a[124:127], a[180:183], v[68:71]\n"
"v_accvgpr_read_b32 v74, a242\n"
"v_accvgpr_read_b32 v75, a243\n"
"v_mfma_f32_16x16x32_bf16 v[68:71], a[120:123], a[176:179], v[68:71]\n"
"v_accvgpr_read_b32 v76, a244\n"
"v_accvgpr_read_b32 v77, a245\n"
"v_mfma_f32_16x16x32_bf16 v[132:135], a[120:123], a[184:187], v[132:135]\n"
"v_accvgpr_read_b32 v78, a246\n"
"v_accvgpr_read_b32 v79, a247\n"
"v_mfma_f32_16x16x32_bf16 v[132:135], a[124:127], a[188:191], v[132:135]\n"
"v_accvgpr_read_b32 v80, a248\n"
"v_accvgpr_read_b32 v81, a249\n"
"v_mfma_f32_16x16x32_bf16 v[128:131], a[116:119], a[188:191], v[128:131]\n"
"v_accvgpr_read_b32 v82, a250\n"
"v_accvgpr_read_b32 v83, a251\n"
"v_mfma_f32_16x16x32_bf16 v[128:131], a[112:115], a[184:187], v[128:131]\n"
"v_accvgpr_read_b32 v84, a252\n"
"v_accvgpr_read_b32 v85, a253\n"
"v_mfma_f32_16x16x32_bf16 v[100:103], a[104:107], a[184:187], v[100:103]\n"
"v_accvgpr_read_b32 v86, a254\n"
"v_accvgpr_read_b32 v87, a255\n"
"v_mfma_f32_16x16x32_bf16 v[100:103], a[108:111], a[188:191], v[100:103]\n"
"v_mul_f32 v40, v40, v16\n"
"v_mul_f32 v41, v41, v16\n"
"v_mfma_f32_16x16x32_bf16 v[96:99], a[100:103], a[188:191], v[96:99]\n"
"v_mul_f32 v42, v42, v16\n"
"v_mul_f32 v43, v43, v16\n"
"v_mfma_f32_16x16x32_bf16 v[96:99], a[96:99], a[184:187], v[96:99]\n"
"v_mul_f32 v44, v44, v16\n"
"s_waitcnt lgkmcnt(0)\n"
"v_mfma_f32_16x16x32_bf16 v[32:35], a[64:67], a[144:147], v[32:35]\n"
"buffer_load_dword v13, v14, s[56:59], 0 offen offset:0 sc1\n"
"v_mul_f32 v45, v45, v16\n"
"v_mul_f32 v46, v46, v16\n"
"v_mfma_f32_16x16x32_bf16 v[32:35], a[68:71], a[148:151], v[32:35]\n"
"v_mul_f32 v47, v47, v16\n"
"v_mul_f32 v48, v48, v16\n"
"v_mfma_f32_16x16x32_bf16 v[36:39], a[76:79], a[148:151], v[36:39]\n"
"v_mul_f32 v49, v49, v16\n"
"v_mul_f32 v50, v50, v16\n"
"v_mfma_f32_16x16x32_bf16 v[36:39], a[72:75], a[144:147], v[36:39]\n"
"v_mul_f32 v51, v51, v16\n"
"v_mul_f32 v52, v52, v16\n"
"v_mfma_f32_16x16x32_bf16 v[64:67], a[80:83], a[144:147], v[64:67]\n"
"s_waitcnt vmcnt(6)\n"
"v_mfma_f32_16x16x32_bf16 v[64:67], a[84:87], a[148:151], v[64:67]\n"
"s_mov_b64 exec, 4\n"
"buffer_store_dword v12, v4, s[56:59], 0 offen sc1\n"
"s_mov_b64 exec, -1\n"
"v_mul_f32 v53, v53, v16\n"
"v_mul_f32 v54, v54, v16\n"
"v_mul_f32 v55, v55, v16\n"
"v_mul_f32 v72, v72, v18\n"
"v_mfma_f32_16x16x32_bf16 v[68:71], a[92:95], a[148:151], v[68:71]\n"
"v_mul_f32 v73, v73, v18\n"
"v_mul_f32 v74, v74, v18\n"
"v_mfma_f32_16x16x32_bf16 v[68:71], a[88:91], a[144:147], v[68:71]\n"
"v_mul_f32 v75, v75, v18\n"
"v_mul_f32 v76, v76, v18\n"
"v_mfma_f32_16x16x32_bf16 v[132:135], a[88:91], a[152:155], v[132:135]\n"
"v_mul_f32 v77, v77, v18\n"
"v_mul_f32 v78, v78, v18\n"
"v_mfma_f32_16x16x32_bf16 v[132:135], a[92:95], a[156:159], v[132:135]\n"
"v_mul_f32 v79, v79, v18\n"
"v_mul_f32 v80, v80, v18\n"
"v_mfma_f32_16x16x32_bf16 v[128:131], a[84:87], a[156:159], v[128:131]\n"
"v_mul_f32 v81, v81, v18\n"
"v_mul_f32 v82, v82, v18\n"
"v_mfma_f32_16x16x32_bf16 v[128:131], a[80:83], a[152:155], v[128:131]\n"
"v_mul_f32 v83, v83, v18\n"
"v_mul_f32 v84, v84, v18\n"
"v_mfma_f32_16x16x32_bf16 v[100:103], a[72:75], a[152:155], v[100:103]\n"
"v_mul_f32 v85, v85, v18\n"
"v_mul_f32 v86, v86, v18\n"
"v_mfma_f32_16x16x32_bf16 v[100:103], a[76:79], a[156:159], v[100:103]\n"
"v_mul_f32 v87, v87, v18\n"
"v_mfma_f32_16x16x32_bf16 v[96:99], a[68:71], a[156:159], v[96:99]\n"
"v_mfma_f32_16x16x32_bf16 v[96:99], a[64:67], a[152:155], v[96:99]\n"
"v_pk_fma_f32 v[32:33], v[32:33], v[16:17], v[40:41] op_sel:[0,1,0] op_sel_hi:[1,1,1]\n"
"v_pk_fma_f32 v[34:35], v[34:35], v[16:17], v[42:43] op_sel:[0,1,0] op_sel_hi:[1,1,1]\n"
"v_pk_fma_f32 v[36:37], v[36:37], v[16:17], v[44:45] op_sel:[0,1,0] op_sel_hi:[1,1,1]\n"
"v_pk_fma_f32 v[38:39], v[38:39], v[16:17], v[46:47] op_sel:[0,1,0] op_sel_hi:[1,1,1]\n"
"v_pk_fma_f32 v[64:65], v[64:65], v[16:17], v[48:49] op_sel:[0,1,0] op_sel_hi:[1,1,1]\n"
"v_pk_fma_f32 v[66:67], v[66:67], v[16:17], v[50:51] op_sel:[0,1,0] op_sel_hi:[1,1,1]\n"
"v_pk_fma_f32 v[68:69], v[68:69], v[16:17], v[52:53] op_sel:[0,1,0] op_sel_hi:[1,1,1]\n"
"v_pk_fma_f32 v[70:71], v[70:71], v[16:17], v[54:55] op_sel:[0,1,0] op_sel_hi:[1,1,1]\n"
"v_pk_fma_f32 v[96:97], v[96:97], v[18:19], v[72:73] op_sel:[0,1,0] op_sel_hi:[1,1,1]\n"
"v_pk_fma_f32 v[98:99], v[98:99], v[18:19], v[74:75] op_sel:[0,1,0] op_sel_hi:[1,1,1]\n"
"v_pk_fma_f32 v[100:101], v[100:101], v[18:19], v[76:77] op_sel:[0,1,0] op_sel_hi:[1,1,1]\n"
"v_pk_fma_f32 v[102:103], v[102:103], v[18:19], v[78:79] op_sel:[0,1,0] op_sel_hi:[1,1,1]\n"
"v_pk_fma_f32 v[128:129], v[128:129], v[18:19], v[80:81] op_sel:[0,1,0] op_sel_hi:[1,1,1]\n"
"v_pk_fma_f32 v[130:131], v[130:131], v[18:19], v[82:83] op_sel:[0,1,0] op_sel_hi:[1,1,1]\n"
"v_pk_fma_f32 v[132:133], v[132:133], v[18:19], v[84:85] op_sel:[0,1,0] op_sel_hi:[1,1,1]\n"
"v_pk_fma_f32 v[134:135], v[134:135], v[18:19], v[86:87] op_sel:[0,1,0] op_sel_hi:[1,1,1]\n"
"s_waitcnt vmcnt(3)\n"
"s_mov_b64 exec, 8\n"
"buffer_store_dword v12, v4, s[56:59], 0 offen sc1\n"
"s_mov_b64 exec, -1\n"
"s_waitcnt vmcnt(2)\n"
"v_cmp_ne_u32 vcc, 1, v13\n"
"s_nop 4\n"
"s_bitcmp1_b32 vcc_lo, 1\n"
"s_cbranch_scc1 ST1_%=\n"
"ST1d_%=:\n"
"buffer_load_dwordx4 v[160:163], v2, s[56:59], s69 offen offset:0 sc1\n"
"buffer_load_dwordx4 v[164:167], v2, s[56:59], s69 offen offset:1024 sc1\n"
"buffer_load_dwordx4 v[168:171], v2, s[56:59], s69 offen offset:2048 sc1\n"
"buffer_load_dwordx4 v[172:175], v2, s[56:59], s69 offen offset:3072 sc1\n"
"s_bitcmp1_b32 vcc_lo, 2\n"
"s_cbranch_scc1 ST2_%=\n"
"ST2d_%=:\n"
"buffer_load_dwordx4 v[176:179], v2, s[56:59], s70 offen offset:0 sc1\n"
"buffer_load_dwordx4 v[180:183], v2, s[56:59], s70 offen offset:1024 sc1\n"
"buffer_load_dwordx4 v[184:187], v2, s[56:59], s70 offen offset:2048 sc1\n"
"buffer_load_dwordx4 v[188:191], v2, s[56:59], s70 offen offset:3072 sc1\n"
"s_bitcmp1_b32 vcc_lo, 3\n"
"s_cbranch_scc1 ST3_%=\n"
"ST3d_%=:\n"
"buffer_load_dwordx4 v[192:195], v2, s[56:59], s71 offen offset:0 sc1\n"
"buffer_load_dwordx4 v[196:199], v2, s[56:59], s71 offen offset:1024 sc1\n"
"buffer_load_dwordx4 v[200:203], v2, s[56:59], s71 offen offset:2048 sc1\n"
"buffer_load_dwordx4 v[204:207], v2, s[56:59], s71 offen offset:3072 sc1\n"
"s_waitcnt vmcnt(11)\n"
"v_lshlrev_b32 v16, 16, v160\n"
"v_and_b32 v17, 0xffff0000, v160\n"
"v_lshlrev_b32 v18, 16, v161\n"
"v_and_b32 v19, 0xffff0000, v161\n"
"v_lshlrev_b32 v20, 16, v162\n"
"v_and_b32 v21, 0xffff0000, v162\n"
"v_lshlrev_b32 v22, 16, v163\n"
"v_and_b32 v23, 0xffff0000, v163\n"
"v_pk_add_f32 v[32:33], v[32:33], v[16:17]\n"
"v_pk_add_f32 v[34:35], v[34:35], v[18:19]\n"
"v_pk_add_f32 v[36:37], v[36:37], v[20:21]\n"
"v_pk_add_f32 v[38:39], v[38:39], v[22:23]\n"
"s_waitcnt vmcnt(10)\n"
"v_lshlrev_b32 v24, 16, v164\n"
"v_and_b32 v25, 0xffff0000, v164\n"
"v_lshlrev_b32 v26, 16, v165\n"
"v_and_b32 v27, 0xffff0000, v165\n"
"v_lshlrev_b32 v28, 16, v166\n"
"v_and_b32 v29, 0xffff0000, v166\n"
"v_lshlrev_b32 v30, 16, v167\n"
"v_and_b32 v31, 0xffff0000, v167\n"
"v_pk_add_f32 v[64:65], v[64:65], v[24:25]\n"
"v_pk_add_f32 v[66:67], v[66:67], v[26:27]\n"
"v_pk_add_f32 v[68:69], v[68:69], v[28:29]\n"
"v_pk_add_f32 v[70:71], v[70:71], v[30:31]\n"
"s_waitcnt vmcnt(9)\n"
"v_lshlrev_b32 v16, 16, v168\n"
"v_and_b32 v17, 0xffff0000, v168\n"
"v_lshlrev_b32 v18, 16, v169\n"
"v_and_b32 v19, 0xffff0000, v169\n"
"v_lshlrev_b32 v20, 16, v170\n"
"v_and_b32 v21, 0xffff0000, v170\n"
"v_lshlrev_b32 v22, 16, v171\n"
"v_and_b32 v23, 0xffff0000, v171\n"
"v_pk_add_f32 v[96:97], v[96:97], v[16:17]\n"
"v_pk_add_f32 v[98:99], v[98:99], v[18:19]\n"
"v_pk_add_f32 v[100:101], v[100:101], v[20:21]\n"
"v_pk_add_f32 v[102:103], v[102:103], v[22:23]\n"
"s_waitcnt vmcnt(8)\n"
"v_lshlrev_b32 v24, 16, v172\n"
"v_and_b32 v25, 0xffff0000, v172\n"
"v_lshlrev_b32 v26, 16, v173\n"
"v_and_b32 v27, 0xffff0000, v173\n"
"v_lshlrev_b32 v28, 16, v174\n"
"v_and_b32 v29, 0xffff0000, v174\n"
"v_lshlrev_b32 v30, 16, v175\n"
"v_and_b32 v31, 0xffff0000, v175\n"
"v_pk_add_f32 v[128:129], v[128:129], v[24:25]\n"
"v_pk_add_f32 v[130:131], v[130:131], v[26:27]\n"
"v_pk_add_f32 v[132:133], v[132:133], v[28:29]\n"
"v_pk_add_f32 v[134:135], v[134:135], v[30:31]\n"
"s_waitcnt vmcnt(7)\n"
"v_lshlrev_b32 v16, 16, v176\n"
"v_and_b32 v17, 0xffff0000, v176\n"
"v_lshlrev_b32 v18, 16, v177\n"
"v_and_b32 v19, 0xffff0000, v177\n"
"v_lshlrev_b32 v20, 16, v178\n"
"v_and_b32 v21, 0xffff0000, v178\n"
"v_lshlrev_b32 v22, 16, v179\n"
"v_and_b32 v23, 0xffff0000, v179\n"
"v_pk_add_f32 v[32:33], v[32:33], v[16:17]\n"
"v_pk_add_f32 v[34:35], v[34:35], v[18:19]\n"
"v_pk_add_f32 v[36:37], v[36:37], v[20:21]\n"
"v_pk_add_f32 v[38:39], v[38:39], v[22:23]\n"
"s_waitcnt vmcnt(6)\n"
"v_lshlrev_b32 v24, 16, v180\n"
"v_and_b32 v25, 0xffff0000, v180\n"
"v_lshlrev_b32 v26, 16, v181\n"
"v_and_b32 v27, 0xffff0000, v181\n"
"v_lshlrev_b32 v28, 16, v182\n"
"v_and_b32 v29, 0xffff0000, v182\n"
"v_lshlrev_b32 v30, 16, v183\n"
"v_and_b32 v31, 0xffff0000, v183\n"
"v_pk_add_f32 v[64:65], v[64:65], v[24:25]\n"
"v_pk_add_f32 v[66:67], v[66:67], v[26:27]\n"
"v_pk_add_f32 v[68:69], v[68:69], v[28:29]\n"
"v_pk_add_f32 v[70:71], v[70:71], v[30:31]\n"
"s_waitcnt vmcnt(5)\n"
"v_lshlrev_b32 v16, 16, v184\n"
"v_and_b32 v17, 0xffff0000, v184\n"
"v_lshlrev_b32 v18, 16, v185\n"
"v_and_b32 v19, 0xffff0000, v185\n"
"v_lshlrev_b32 v20, 16, v186\n"
"v_and_b32 v21, 0xffff0000, v186\n"
"v_lshlrev_b32 v22, 16, v187\n"
"v_and_b32 v23, 0xffff0000, v187\n"
"v_pk_add_f32 v[96:97], v[96:97], v[16:17]\n"
"v_pk_add_f32 v[98:99], v[98:99], v[18:19]\n"
"v_pk_add_f32 v[100:101], v[100:101], v[20:21]\n"
"v_pk_add_f32 v[102:103], v[102:103], v[22:23]\n"
"s_waitcnt vmcnt(4)\n"
"v_lshlrev_b32 v24, 16, v188\n"
"v_and_b32 v25, 0xffff0000, v188\n"
"v_lshlrev_b32 v26, 16, v189\n"
"v_and_b32 v27, 0xffff0000, v189\n"
"v_lshlrev_b32 v28, 16, v190\n"
"v_and_b32 v29, 0xffff0000, v190\n"
"v_lshlrev_b32 v30, 16, v191\n"
"v_and_b32 v31, 0xffff0000, v191\n"
"v_pk_add_f32 v[128:129], v[128:129], v[24:25]\n"
"v_pk_add_f32 v[130:131], v[130:131], v[26:27]\n"
"v_pk_add_f32 v[132:133], v[132:133], v[28:29]\n"
"v_pk_add_f32 v[134:135], v[134:135], v[30:31]\n"
"STX_%=:\n"
"s_waitcnt vmcnt(3)\n"
"v_lshlrev_b32 v16, 16, v192\n"
"v_and_b32 v17, 0xffff0000, v192\n"
"v_lshlrev_b32 v18, 16, v193\n"
"v_and_b32 v19, 0xffff0000, v193\n"
"v_lshlrev_b32 v20, 16, v194\n"
"v_and_b32 v21, 0xffff0000, v194\n"
"v_lshlrev_b32 v22, 16, v195\n"
"v_and_b32 v23, 0xffff0000, v195\n"
"v_pk_add_f32 v[32:33], v[32:33], v[16:17]\n"
"v_pk_add_f32 v[34:35], v[34:35], v[18:19]\n"
"v_pk_add_f32 v[36:37], v[36:37], v[20:21]\n"
"v_pk_add_f32 v[38:39], v[38:39], v[22:23]\n"
"v_cvt_pk_bf16_f32 v208, v32, v33\n"
"v_cvt_pk_bf16_f32 v209, v34, v35\n"
"v_cvt_pk_bf16_f32 v210, v36, v37\n"
"v_cvt_pk_bf16_f32 v211, v38, v39\n"
"s_nop 1\n"
"v_permlane16_swap_b32 v208, v210\n"
"v_permlane16_swap_b32 v209, v211\n"
"buffer_store_dwordx4 v[208:211], v3, s[40:43], 0 offen offset:0 sc1\n"
"s_waitcnt vmcnt(3)\n"
"v_lshlrev_b32 v24, 16, v196\n"
"v_and_b32 v25, 0xffff0000, v196\n"
"v_lshlrev_b32 v26, 16, v197\n"
"v_and_b32 v27, 0xffff0000, v197\n"
"v_lshlrev_b32 v28, 16, v198\n"
"v_and_b32 v29, 0xffff0000, v198\n"
"v_lshlrev_b32 v30, 16, v199\n"
"v_and_b32 v31, 0xffff0000, v199\n"
"v_pk_add_f32 v[64:65], v[64:65], v[24:25]\n"
"v_pk_add_f32 v[66:67], v[66:67], v[26:27]\n"
"v_pk_add_f32 v[68:69], v[68:69], v[28:29]\n"
"v_pk_add_f32 v[70:71], v[70:71], v[30:31]\n"
"v_cvt_pk_bf16_f32 v212, v64, v65\n"
"v_cvt_pk_bf16_f32 v213, v66, v67\n"
"v_cvt_pk_bf16_f32 v214, v68, v69\n"
"v_cvt_pk_bf16_f32 v215, v70, v71\n"
"s_nop 1\n"
"v_permlane16_swap_b32 v212, v214\n"
"v_permlane16_swap_b32 v213, v215\n"
"buffer_store_dwordx4 v[212:215], v3, s[40:43], 0 offen offset:64 sc1\n"
"s_waitcnt vmcnt(3)\n"
"v_lshlrev_b32 v16, 16, v200\n"
"v_and_b32 v17, 0xffff0000, v200\n"
"v_lshlrev_b32 v18, 16, v201\n"
"v_and_b32 v19, 0xffff0000, v201\n"
"v_lshlrev_b32 v20, 16, v202\n"
"v_and_b32 v21, 0xffff0000, v202\n"
"v_lshlrev_b32 v22, 16, v203\n"
"v_and_b32 v23, 0xffff0000, v203\n"
"v_pk_add_f32 v[96:97], v[96:97], v[16:17]\n"
"v_pk_add_f32 v[98:99], v[98:99], v[18:19]\n"
"v_pk_add_f32 v[100:101], v[100:101], v[20:21]\n"
"v_pk_add_f32 v[102:103], v[102:103], v[22:23]\n"
"v_cvt_pk_bf16_f32 v216, v96, v97\n"
"v_cvt_pk_bf16_f32 v217, v98, v99\n"
"v_cvt_pk_bf16_f32 v218, v100, v101\n"
"v_cvt_pk_bf16_f32 v219, v102, v103\n"
"s_nop 1\n"
"v_permlane16_swap_b32 v216, v218\n"
"v_permlane16_swap_b32 v217, v219\n"
"buffer_store_dwordx4 v[216:219], v3, s[40:43], s64 offen offset:0 sc1\n"
"s_waitcnt vmcnt(3)\n"
"v_lshlrev_b32 v24, 16, v204\n"
"v_and_b32 v25, 0xffff0000, v204\n"
"v_lshlrev_b32 v26, 16, v205\n"
"v_and_b32 v27, 0xffff0000, v205\n"
"v_lshlrev_b32 v28, 16, v206\n"
"v_and_b32 v29, 0xffff0000, v206\n"
"v_lshlrev_b32 v30, 16, v207\n"
"v_and_b32 v31, 0xffff0000, v207\n"
"v_pk_add_f32 v[128:129], v[128:129], v[24:25]\n"
"v_pk_add_f32 v[130:131], v[130:131], v[26:27]\n"
"v_pk_add_f32 v[132:133], v[132:133], v[28:29]\n"
"v_pk_add_f32 v[134:135], v[134:135], v[30:31]\n"
"v_cvt_pk_bf16_f32 v220, v128, v129\n"
"v_cvt_pk_bf16_f32 v221, v130, v131\n"
"v_cvt_pk_bf16_f32 v222, v132, v133\n"
"v_cvt_pk_bf16_f32 v223, v134, v135\n"
"s_nop 1\n"
"v_permlane16_swap_b32 v220, v222\n"
"v_permlane16_swap_b32 v221, v223\n"
"buffer_store_dwordx4 v[220:223], v3, s[40:43], s64 offen offset:64 sc1\n"
"s_branch END_%=\n"
"EV1_%=:\n"
"s_lshl_b32 s14, s4, 3\n"
"s_add_u32 s14, s14, s5\n"
"s_lshl_b32 s14, s14, 2\n"
"s_add_u32 s14, s14, s83\n"
"s_mov_b64 exec, 0xfffff\n"
"v_and_b32 v16, 3, v1\n"
"v_xor_b32 v17, s14, v16\n"
"v_lshrrev_b32 v18, 2, v1\n"
"v_lshl_add_u32 v17, v17, 2, v18\n"
"v_lshlrev_b32 v17, 7, v17\n"
"v_lshl_add_u32 v17, v16, 4, v17\n"
"v_add_u32 v31, 262144, v17\n"
"s_lshl_b32 s14, s79, 5\n"
"s_add_u32 s14, s14, s80\n"
"s_lshl_b32 s15, s14, 7\n"
"s_mov_b64 exec, 0xf0000\n"
"v_lshl_add_u32 v31, v16, 2, s15\n"
"s_mov_b64 exec, -1\n"
"s_lshl_b32 s14, s14, 2\n"
"s_add_u32 s14, s14, 32768\n"
"v_writelane_b32 v31, s14, 20\n"
"s_lshl_b32 s14, s12, 2\n"
"s_add_u32 s15, s14, 36864\n"
"s_add_u32 s14, s14, 38912\n"
"v_writelane_b32 v22, s15, 0\n"
"v_writelane_b32 v21, s14, 0\n"
"s_mov_b32 s14, 40960\n"
"v_writelane_b32 v21, s14, 1\n"
"s_branch EV1r_%=\n"
"EV2_%=:\n"
"v_lshlrev_b32 v29, 4, v1\n"
"v_add_u32 v29, 36864, v29\n"
"s_mov_b32 s14, 40960\n"
"v_writelane_b32 v21, s14, 1\n"
"v_writelane_b32 v30, s14, 0\n"
"s_branch EV2r_%=\n"
"EV4_%=:\n"
"s_mov_b32 s15, 0x40000\n"
"s_waitcnt vmcnt(0)\n"
"HS3_%=:\n"
"buffer_store_dword v23, v22, s[56:59], 0 offen sc1\n"
"buffer_load_dword v23, v21, s[56:59], 0 offen sc1\n"
"v_xor_b32 v24, v24, v16\n"
"v_xor_b32 v25, v25, v17\n"
"v_xor_b32 v26, v26, v18\n"
"v_xor_b32 v27, v27, v19\n"
"v_or3_b32 v24, v24, v25, v26\n"
"v_or_b32 v24, v24, v27\n"
"v_cmp_ne_u32 vcc, 0, v24\n"
"s_nop 3\n"
"s_cmp_eq_u64 vcc, 0\n"
"s_cselect_b32 s14, 0, 1073741824\n"
"buffer_store_dword v28, v30, s[56:59], s14 offen sc1\n"
"buffer_load_dwordx4 v[24:27], v29, s[56:59], 0 offen sc1\n"
"s_waitcnt vmcnt(0)\n"
"v_readlane_b32 s14, v23, 1\n"
"s_nop 3\n"
"s_cmp_lg_u32 s14, s7\n"
"s_cselect_b32 s6, 2, s6\n"
"s_sub_u32 s15, s15, 1\n"
"s_cselect_b32 s6, 2, s6\n"
"s_cmp_lt_u32 s6, 2\n"
"s_cbranch_scc1 HS3_%=\n"
"s_branch EV4r_%=\n"
"EV5_%=:\n"
"s_lshl_b32 s14, s79, 5\n"
"s_add_u32 s14, s14, s80\n"
"s_lshl_b32 s14, s14, 7\n"
"s_add_u32 s14, s14, 0\n"
"s_mov_b64 exec, 1\n"
"v_mov_b32 v14, 1\n"
"v_lshlrev_b32 v15, 2, v1\n"
"buffer_store_dword v14, v15, s[56:59], s14 offen sc1\n"
"s_mov_b64 exec, -1\n"
"s_branch EV5r_%=\n"
"EV6_%=:\n"
"s_lshl_b32 s14, s79, 5\n"
"s_add_u32 s14, s14, s80\n"
"s_lshl_b32 s14, s14, 7\n"
"s_add_u32 s14, s14, 0\n"
"s_mov_b64 exec, 2\n"
"v_mov_b32 v14, 1\n"
"v_lshlrev_b32 v15, 2, v1\n"
"buffer_store_dword v14, v15, s[56:59], s14 offen sc1\n"
"s_mov_b64 exec, -1\n"
"s_branch EV6r_%=\n"
"PL7_%=:\n"
"s_mov_b32 s14, 0x40000\n"
"PL7a_%=:\n"
"buffer_load_dword v11, v10, s[56:59], 0 offen offset:0 sc1\n"
"s_sub_u32 s14, s14, 1\n"
"s_waitcnt vmcnt(0)\n"
"s_cbranch_scc1 PL7d_%=\n"
"v_cmp_ne_u32 vcc, 1, v11\n"
"s_nop 4\n"
"s_cbranch_vccnz PL7a_%=\n"
"s_branch PL7d_%=\n"
"EV8_%=:\n"
"s_lshl_b32 s14, s79, 5\n"
"s_add_u32 s14, s14, s80\n"
"s_lshl_b32 s14, s14, 7\n"
"s_add_u32 s14, s14, 0\n"
"s_mov_b64 exec, 4\n"
"v_mov_b32 v5, 1\n"
"v_lshlrev_b32 v15, 2, v1\n"
"buffer_store_dword v5, v15, s[56:59], s14 offen sc1\n"
"s_mov_b64 exec, -1\n"
"s_branch EV8r_%=\n"
"EV9_%=:\n"
"s_lshl_b32 s14, s79, 5\n"
"s_add_u32 s14, s14, s80\n"
"s_lshl_b32 s14, s14, 7\n"
"s_add_u32 s14, s14, 0\n"
"s_mov_b64 exec, 8\n"
"v_mov_b32 v5, 1\n"
"v_lshlrev_b32 v15, 2, v1\n"
"buffer_store_dword v5, v15, s[56:59], s14 offen sc1\n"
"s_mov_b64 exec, -1\n"
"s_branch EV9r_%=\n"
"PL10_%=:\n"
"s_mov_b32 s14, 0x40000\n"
"PL10a_%=:\n"
"buffer_load_dword v11, v10, s[56:59], 0 offen offset:4 sc1\n"
"s_sub_u32 s14, s14, 1\n"
"s_waitcnt vmcnt(0)\n"
"s_cbranch_scc1 PL10d_%=\n"
"v_cmp_ne_u32 vcc, 1, v11\n"
"s_nop 4\n"
"s_cbranch_vccnz PL10a_%=\n"
"s_branch PL10d_%=\n"
"PL11_%=:\n"
"s_mov_b32 s14, 0x40000\n"
"PL11a_%=:\n"
"buffer_load_dword v11, v10, s[56:59], 0 offen offset:8 sc1\n"
"s_sub_u32 s14, s14, 1\n"
"s_waitcnt vmcnt(0)\n"
"s_cbranch_scc1 PL11d_%=\n"
"v_cmp_ne_u32 vcc, 1, v11\n"
"s_nop 4\n"
"s_cbranch_vccnz PL11a_%=\n"
"s_branch PL11d_%=\n"
"PL12_%=:\n"
"s_mov_b32 s14, 0x40000\n"
"PL12a_%=:\n"
"buffer_load_dword v11, v10, s[56:59], 0 offen offset:12 sc1\n"
"s_sub_u32 s14, s14, 1\n"
"s_waitcnt vmcnt(0)\n"
"s_cbranch_scc1 PL12d_%=\n"
"v_cmp_ne_u32 vcc, 1, v11\n"
"s_nop 4\n"
"s_cbranch_vccnz PL12a_%=\n"
"s_branch PL12d_%=\n"
"TYP2_%=:\n"
"s_bfe_u32 s82, s80, 0x10001\n"
"s_and_b32 s14, s80, 1\n"
"s_mul_i32 s14, s14, 288\n"
"s_add_u32 s81, s14, 448\n"
"s_mov_b32 s68, 65536\n"
"s_mov_b32 s69, 131072\n"
"s_mov_b32 s70, 196608\n"
"s_mov_b32 s71, 262144\n"
"s_mov_b32 s72, 327680\n"
"s_mov_b32 s73, 393216\n"
"s_mov_b32 s74, 458752\n"
"s_mov_b32 s75, 524288\n"
"v_lshrrev_b32 v20, 4, v1\n"
"v_and_b32 v4, 7, v1\n"
"v_xor_b32 v4, v4, v20\n"
"v_lshlrev_b32 v4, 4, v4\n"
"s_and_b32 s14, s13, 1\n"
"s_lshl_b32 s14, s14, 6\n"
"v_xor_b32 v4, s14, v4\n"
"s_lshl_b32 s14, s13, 3\n"
"s_add_u32 s14, s14, s81\n"
"v_lshrrev_b32 v20, 3, v1\n"
"v_add_u32 v20, s14, v20\n"
"v_lshl_add_u32 v4, v20, 11, v4\n"
"s_and_b32 s78, s12, 7\n"
"s_lshl_b32 s78, s78, 1\n"
"s_min_u32 s78, s78, 13\n"
"s_waitcnt lgkmcnt(0)\n"
"s_cmp_eq_u32 s82, 0\n"
"s_cselect_b32 s38, s22, s24\n"
"s_cselect_b32 s39, s23, s25\n"
"s_mov_b32 s52, s38\n"
"s_and_b32 s53, s39, 0xffff\n"
"s_mov_b32 s54, 2097152\n"
"s_mov_b32 s55, 0x20000\n"
"s_mov_b32 s40, s16\n"
"s_and_b32 s41, s17, 0xffff\n"
"s_mov_b32 s42, 4194304\n"
"s_mov_b32 s43, 0x20000\n"
"s_mov_b32 s60, s32\n"
"s_and_b32 s61, s33, 0xffff\n"
"s_mov_b32 s62, 2048\n"
"s_mov_b32 s63, 0x20000\n"
"s_mov_b32 s56, s36\n"
"s_and_b32 s57, s37, 0xffff\n"
"s_mov_b32 s58, 57016320\n"
"s_mov_b32 s59, 0x20000\n"
"s_mov_b32 s16, s78\n"
"s_movk_i32 s17, 128\n"
"s_mov_b32 s28, 458752\n"
"s_add_u32 m0, s77, 65536\n"
"s_nop 0\n"
"buffer_load_dwordx4 v4, s[52:55], 0 offen lds\n"
"s_add_u32 m0, s77, 69632\n"
"s_nop 0\n"
"buffer_load_dwordx4 v4, s[52:55], s68 offen lds\n"
"s_add_u32 m0, s77, 73728\n"
"s_nop 0\n"
"buffer_load_dwordx4 v4, s[52:55], s69 offen lds\n"
"s_add_u32 m0, s77, 77824\n"
"s_nop 0\n"
"buffer_load_dwordx4 v4, s[52:55], s70 offen lds\n"
"s_add_u32 m0, s77, 81920\n"
"s_nop 0\n"
"buffer_load_dwordx4 v4, s[52:55], s71 offen lds\n"
"s_add_u32 m0, s77, 86016\n"
"s_nop 0\n"
"buffer_load_dwordx4 v4, s[52:55], s72 offen lds\n"
"s_add_u32 m0, s77, 90112\n"
"s_nop 0\n"
"buffer_load_dwordx4 v4, s[52:55], s73 offen lds\n"
"s_add_u32 m0, s77, 94208\n"
"s_nop 0\n"
"buffer_load_dwordx4 v4, s[52:55], s74 offen lds\n"
"s_add_u32 m0, s77, 98304\n"
"s_nop 0\n"
"buffer_load_dwordx4 v4, s[52:55], s75 offen lds\n"
"v_add_u32 v4, s17, v4\n"
"s_add_u32 m0, s76, 0\n"
"s_nop 0\n"
"buffer_load_dwordx4 v2, s[40:43], s64 offen lds\n"
"s_add_u32 m0, s76, 1024\n"
"s_nop 0\n"
"buffer_load_dwordx4 v3, s[40:43], s64 offen lds\n"
"s_add_u32 m0, s76, 2048\n"
"s_nop 0\n"
"buffer_load_dwordx4 v2, s[40:43], s65 offen lds\n"
"s_add_u32 m0, s76, 3072\n"
"s_nop 0\n"
"buffer_load_dwordx4 v3, s[40:43], s65 offen lds\n"
"s_add_u32 m0, s76, 8192\n"
"s_nop 0\n"
"buffer_load_dwordx4 v2, s[40:43], s66 offen lds\n"
"s_add_u32 m0, s76, 9216\n"
"s_nop 0\n"
"buffer_load_dwordx4 v3, s[40:43], s66 offen lds\n"
"s_add_u32 m0, s76, 10240\n"
"s_nop 0\n"
"buffer_load_dwordx4 v2, s[40:43], s67 offen lds\n"
"v_add_u32 v2, s17, v2\n"
"s_add_u32 m0, s76, 11264\n"
"s_nop 0\n"
"buffer_load_dwordx4 v3, s[40:43], s67 offen lds\n"
"v_add_u32 v3, s17, v3\n"
"s_add_u32 s16, s16, 1\n"
"s_mov_b32 s28, s16\n"
"s_lshl_b32 s17, s28, 7\n"
"s_mul_i32 s28, s28, 458752\n"
"s_add_u32 m0, s77, 102400\n"
"s_nop 0\n"
"buffer_load_dwordx4 v4, s[52:55], 0 offen lds\n"
"v_lshrrev_b32 v20, 4, v1\n"
"v_bfe_u32 v21, v1, 1, 3\n"
"v_xor_b32 v20, v20, v21\n"
"v_lshlrev_b32 v20, 4, v20\n"
"s_add_u32 m0, s77, 106496\n"
"s_nop 0\n"
"buffer_load_dwordx4 v4, s[52:55], s68 offen lds\n"
"v_and_b32 v21, 15, v1\n"
"v_lshl_add_u32 v6, v21, 7, v20\n"
"s_and_b32 s15, s13, 1\n"
"s_add_u32 m0, s77, 110592\n"
"s_nop 0\n"
"buffer_load_dwordx4 v4, s[52:55], s69 offen lds\n"
"s_mul_i32 s14, s15, 18432\n"
"s_add_u32 s14, s14, 65536\n"
"v_add_u32 v8, s14, v6\n"
"v_xor_b32 v9, 64, v8\n"
"s_add_u32 m0, s77, 114688\n"
"s_nop 0\n"
"buffer_load_dwordx4 v4, s[52:55], s70 offen lds\n"
"s_lshr_b32 s14, s13, 1\n"
"s_lshl_b32 s14, s14, 14\n"
"v_add_u32 v6, s14, v6\n"
"s_add_u32 m0, s77, 118784\n"
"s_nop 0\n"
"buffer_load_dwordx4 v4, s[52:55], s71 offen lds\n"
"v_xor_b32 v7, 64, v6\n"
"s_lshr_b32 s14, s12, 3\n"
"s_lshl_b32 s14, s14, 2\n"
"s_add_u32 s14, s14, s13\n"
"s_add_u32 m0, s77, 122880\n"
"s_nop 0\n"
"buffer_load_dwordx4 v4, s[52:55], s72 offen lds\n"
"s_mul_i32 s15, s14, 14336\n"
"s_mul_i32 s78, s83, 1835008\n"
"s_add_u32 s15, s15, s78\n"
"s_add_u32 m0, s77, 126976\n"
"s_nop 0\n"
"buffer_load_dwordx4 v4, s[52:55], s73 offen lds\n"
"v_lshlrev_b32 v14, 5, v1\n"
"v_add_u32 v15, s15, v14\n"
"s_lshl_b32 s14, s14, 15\n"
"s_and_b32 s15, s12, 1\n"
"s_add_u32 m0, s77, 131072\n"
"s_nop 0\n"
"buffer_load_dwordx4 v4, s[52:55], s74 offen lds\n"
"s_lshl_b32 s15, s15, 22\n"
"s_add_u32 s14, s14, s15\n"
"s_and_b32 s15, s83, 1\n"
"s_add_u32 m0, s77, 135168\n"
"s_nop 0\n"
"buffer_load_dwordx4 v4, s[52:55], s75 offen lds\n"
"v_add_u32 v4, s17, v4\n"
"s_lshl_b32 s15, s15, 11\n"
"s_add_u32 s14, s14, s15\n"
"v_add_u32 v14, s14, v14\n"
"v_mov_b32 v21, 1073741824\n"
"v_mov_b32 v22, 1073741824\n"
"v_mov_b32 v29, 1073741824\n"
"v_mov_b32 v30, 1073741824\n"
"v_mov_b32 v31, 1073741824\n"
"v_mov_b32 v20, 0\n"
"s_lshl_b32 s14, s12, 2\n"
"s_add_u32 s14, s14, s13\n"
"s_cmp_eq_u32 s14, 1\n"
"s_cselect_b32 s6, 1, 2\n"
"s_cmp_eq_u32 s13, 0\n"
"s_cselect_b32 s6, 0, s6\n"
"s_cmp_eq_u32 s6, 0\n"
"s_cbranch_scc1 EV1_%=\n"
"EV1r_%=:\n"
"s_cmp_eq_u32 s6, 1\n"
"s_cbranch_scc1 EV2_%=\n"
"EV2r_%=:\n"
"buffer_store_dword v20, v31, s[56:59], 0 offen sc1\n"
"buffer_load_dword v23, v21, s[56:59], 0 offen sc1\n"
"buffer_load_dwordx4 v[24:27], v29, s[56:59], 0 offen sc1\n"
"buffer_load_dwordx4 v[16:19], v29, s[56:59], 0 offen offset:2048 sc1\n"
"s_cmp_lt_u32 s83, 2\n"
"s_cselect_b32 s38, s18, s20\n"
"s_cselect_b32 s39, s19, s21\n"
"s_mov_b32 s24, s38\n"
"s_and_b32 s25, s39, 0xffff\n"
"s_mov_b32 s26, 8388608\n"
"s_mov_b32 s27, 0x20000\n"
"s_waitcnt vmcnt(17)\n"
"s_barrier\n"
"ds_read_b128 a[212:215], v7 offset:0\n"
"ds_read_b128 a[208:211], v6 offset:0\n"
"ds_read_b128 a[64:67], v8 offset:0\n"
"ds_read_b128 a[68:71], v9 offset:0\n"
"ds_read_b128 a[72:75], v8 offset:2048\n"
"ds_read_b128 a[76:79], v9 offset:2048\n"
"ds_read_b128 a[80:83], v8 offset:4096\n"
"ds_read_b128 a[84:87], v9 offset:4096\n"
"ds_read_b128 a[88:91], v8 offset:6144\n"
"ds_read_b128 a[92:95], v9 offset:6144\n"
"ds_read_b128 a[96:99], v8 offset:8192\n"
"ds_read_b128 a[100:103], v9 offset:8192\n"
"ds_read_b128 a[104:107], v8 offset:10240\n"
"ds_read_b128 a[108:111], v9 offset:10240\n"
"ds_read_b128 a[112:115], v8 offset:12288\n"
"ds_read_b128 a[116:119], v9 offset:12288\n"
"ds_read_b128 a[120:123], v8 offset:14336\n"
"ds_read_b128 a[124:127], v9 offset:14336\n"
"ds_read_b128 a[128:131], v8 offset:16384\n"
"ds_read_b128 a[132:135], v9 offset:16384\n"
"ds_read_b128 a[220:223], v7 offset:2048\n"
"ds_read_b128 a[216:219], v6 offset:2048\n"
"v_lshlrev_b32 v250, 4, v1\n"
"s_lshl_b32 s14, s79, 5\n"
"s_add_u32 s14, s14, s80\n"
"s_lshl_b32 s14, s14, 2\n"
"s_add_u32 s78, s14, s13\n"
"s_lshl_b32 s14, s78, 12\n"
"buffer_load_dwordx4 v[224:227], v250, s[60:63], 0 offen offset:0\n"
"buffer_load_dwordx4 v[228:231], v250, s[60:63], 0 offen offset:1024\n"
"buffer_load_dwordx4 v[232:235], v250, s[40:43], s14 offen offset:0\n"
"buffer_load_dwordx4 v[236:239], v250, s[40:43], s14 offen offset:1024\n"
"buffer_load_dwordx4 v[240:243], v250, s[40:43], s14 offen offset:2048\n"
"buffer_load_dwordx4 v[244:247], v250, s[40:43], s14 offen offset:3072\n"
"s_mov_b32 s60, s30\n"
"s_and_b32 s61, s31, 0xffff\n"
"s_mov_b32 s62, 7340032\n"
"s_mov_b32 s63, 0x20000\n"
"s_waitcnt lgkmcnt(14)\n"
"v_mfma_f32_16x16x32_bf16 v[32:35], a[208:211], a[64:67], 0\n"
"ds_read_b128 a[228:231], v7 offset:4096\n"
"v_mfma_f32_16x16x32_bf16 v[32:35], a[212:215], a[68:71], v[32:35]\n"
"ds_read_b128 a[224:227], v6 offset:4096\n"
"v_mfma_f32_16x16x32_bf16 v[36:39], a[212:215], a[76:79], 0\n"
"s_add_u32 m0, s76, 32768\n"
"v_mfma_f32_16x16x32_bf16 v[36:39], a[208:211], a[72:75], v[36:39]\n"
"buffer_load_dwordx4 v2, s[40:43], s64 offen lds\n"
"v_mfma_f32_16x16x32_bf16 v[40:43], a[208:211], a[80:83], 0\n"
"v_mfma_f32_16x16x32_bf16 v[40:43], a[212:215], a[84:87], v[40:43]\n"
"s_waitcnt lgkmcnt(14)\n"
"v_mfma_f32_16x16x32_bf16 v[44:47], a[212:215], a[92:95], 0\n"
"s_add_u32 m0, s76, 33792\n"
"v_mfma_f32_16x16x32_bf16 v[44:47], a[208:211], a[88:91], v[44:47]\n"
"buffer_load_dwordx4 v3, s[40:43], s64 offen lds\n"
"s_waitcnt lgkmcnt(13)\n"
"v_mfma_f32_16x16x32_bf16 v[48:51], a[208:211], a[96:99], 0\n"
"s_waitcnt lgkmcnt(12)\n"
"v_mfma_f32_16x16x32_bf16 v[48:51], a[212:215], a[100:103], v[48:51]\n"
"s_waitcnt lgkmcnt(10)\n"
"v_mfma_f32_16x16x32_bf16 v[52:55], a[212:215], a[108:111], 0\n"
"s_add_u32 m0, s76, 34816\n"
"v_mfma_f32_16x16x32_bf16 v[52:55], a[208:211], a[104:107], v[52:55]\n"
"buffer_load_dwordx4 v2, s[40:43], s65 offen lds\n"
"s_waitcnt lgkmcnt(9)\n"
"v_mfma_f32_16x16x32_bf16 v[56:59], a[208:211], a[112:115], 0\n"
"s_waitcnt lgkmcnt(8)\n"
"v_mfma_f32_16x16x32_bf16 v[56:59], a[212:215], a[116:119], v[56:59]\n"
"s_waitcnt lgkmcnt(6)\n"
"v_mfma_f32_16x16x32_bf16 v[60:63], a[212:215], a[124:127], 0\n"
"s_add_u32 m0, s76, 35840\n"
"v_mfma_f32_16x16x32_bf16 v[60:63], a[208:211], a[120:123], v[60:63]\n"
"buffer_load_dwordx4 v3, s[40:43], s65 offen lds\n"
"s_waitcnt lgkmcnt(5)\n"
"v_mfma_f32_16x16x32_bf16 a[32:35], a[208:211], a[128:131], 0\n"
"s_waitcnt lgkmcnt(4)\n"
"v_mfma_f32_16x16x32_bf16 a[32:35], a[212:215], a[132:135], a[32:35]\n"
"s_waitcnt lgkmcnt(2)\n"
"v_mfma_f32_16x16x32_bf16 a[36:39], a[216:219], a[128:131], 0\n"
"ds_read_b128 a[236:239], v7 offset:6144\n"
"s_add_u32 m0, s76, 40960\n"
"v_mfma_f32_16x16x32_bf16 a[36:39], a[220:223], a[132:135], a[36:39]\n"
"ds_read_b128 a[232:235], v6 offset:6144\n"
"v_mfma_f32_16x16x32_bf16 v[92:95], a[220:223], a[124:127], 0\n"
"buffer_load_dwordx4 v2, s[40:43], s66 offen lds\n"
"v_mfma_f32_16x16x32_bf16 v[92:95], a[216:219], a[120:123], v[92:95]\n"
"v_mfma_f32_16x16x32_bf16 v[88:91], a[216:219], a[112:115], 0\n"
"s_add_u32 m0, s76, 41984\n"
"v_mfma_f32_16x16x32_bf16 v[88:91], a[220:223], a[116:119], v[88:91]\n"
"buffer_load_dwordx4 v3, s[40:43], s66 offen lds\n"
"v_mfma_f32_16x16x32_bf16 v[84:87], a[220:223], a[108:111], 0\n"
"v_mfma_f32_16x16x32_bf16 v[84:87], a[216:219], a[104:107], v[84:87]\n"
"v_mfma_f32_16x16x32_bf16 v[80:83], a[216:219], a[96:99], 0\n"
"s_add_u32 m0, s76, 43008\n"
"v_mfma_f32_16x16x32_bf16 v[80:83], a[220:223], a[100:103], v[80:83]\n"
"buffer_load_dwordx4 v2, s[40:43], s67 offen lds\n"
"v_mfma_f32_16x16x32_bf16 v[76:79], a[220:223], a[92:95], 0\n"
"v_add_u32 v2, s17, v2\n"
"v_mfma_f32_16x16x32_bf16 v[76:79], a[216:219], a[88:91], v[76:79]\n"
"v_mfma_f32_16x16x32_bf16 v[72:75], a[216:219], a[80:83], 0\n"
"s_add_u32 m0, s76, 44032\n"
"v_mfma_f32_16x16x32_bf16 v[72:75], a[220:223], a[84:87], v[72:75]\n"
"buffer_load_dwordx4 v3, s[40:43], s67 offen lds\n"
"v_mfma_f32_16x16x32_bf16 v[68:71], a[220:223], a[76:79], 0\n"
"v_add_u32 v3, s17, v3\n"
"s_add_u32 s16, s16, 1\n"
"v_mfma_f32_16x16x32_bf16 v[68:71], a[216:219], a[72:75], v[68:71]\n"
"s_cmp_eq_u32 s16, 15\n"
"s_cselect_b32 s28, -13, 1\n"
"s_lshl_b32 s17, s28, 7\n"
"v_mfma_f32_16x16x32_bf16 v[64:67], a[216:219], a[64:67], 0\n"
"s_mul_i32 s28, s28, 458752\n"
"v_mfma_f32_16x16x32_bf16 v[64:67], a[220:223], a[68:71], v[64:67]\n"
"s_waitcnt vmcnt(18) lgkmcnt(0)\n"
"s_barrier\n"
"v_mfma_f32_16x16x32_bf16 v[96:99], a[224:227], a[64:67], 0\n"
"ds_read_b128 a[212:215], v7 offset:8192\n"
"s_waitcnt vmcnt(8)\n"
"v_mfma_f32_16x16x32_bf16 v[96:99], a[228:231], a[68:71], v[96:99]\n"
"v_readlane_b32 s7, v23, 1\n"
"v_mfma_f32_16x16x32_bf16 v[100:103], a[228:231], a[76:79], 0\n"
"ds_read_b128 a[208:211], v6 offset:8192\n"
"v_add_u32 v16, 1, v16\n"
"v_mfma_f32_16x16x32_bf16 v[100:103], a[224:227], a[72:75], v[100:103]\n"
"s_add_u32 m0, s77, 65536\n"
"ds_read_b128 a[136:139], v8 offset:36864\n"
"v_mfma_f32_16x16x32_bf16 v[104:107], a[224:227], a[80:83], 0\n"
"v_cmp_eq_u32 vcc, v16, v24\n"
"v_mfma_f32_16x16x32_bf16 v[104:107], a[228:231], a[84:87], v[104:107]\n"
"buffer_load_dwordx4 v4, s[52:55], 0 offen lds\n"
"s_nop 1\n"
"v_mfma_f32_16x16x32_bf16 v[108:111], a[228:231], a[92:95], 0\n"
"v_addc_co_u32 v16, vcc, 0, v16, vcc\n"
"v_add_u32 v17, 1, v17\n"
"v_mfma_f32_16x16x32_bf16 v[108:111], a[224:227], a[88:91], v[108:111]\n"
"ds_read_b128 a[140:143], v9 offset:36864\n"
"v_cmp_eq_u32 vcc, v17, v25\n"
"v_mfma_f32_16x16x32_bf16 v[112:115], a[224:227], a[96:99], 0\n"
"s_add_u32 m0, s77, 69632\n"
"s_nop 1\n"
"buffer_load_dwordx4 v4, s[52:55], s68 offen lds\n"
"v_mfma_f32_16x16x32_bf16 v[112:115], a[228:231], a[100:103], v[112:115]\n"
"v_addc_co_u32 v17, vcc, 0, v17, vcc\n"
"v_mfma_f32_16x16x32_bf16 v[116:119], a[228:231], a[108:111], 0\n"
"ds_read_b128 a[144:147], v8 offset:38912\n"
"v_add_u32 v18, 1, v18\n"
"v_mfma_f32_16x16x32_bf16 v[116:119], a[224:227], a[104:107], v[116:119]\n"
"v_cmp_eq_u32 vcc, v18, v26\n"
"s_nop 1\n"
"v_mfma_f32_16x16x32_bf16 v[120:123], a[224:227], a[112:115], 0\n"
"s_add_u32 m0, s77, 73728\n"
"v_addc_co_u32 v18, vcc, 0, v18, vcc\n"
"v_mfma_f32_16x16x32_bf16 v[120:123], a[228:231], a[116:119], v[120:123]\n"
"buffer_load_dwordx4 v4, s[52:55], s69 offen lds\n"
"v_add_u32 v19, 1, v19\n"
"v_mfma_f32_16x16x32_bf16 v[124:127], a[228:231], a[124:127], 0\n"
"ds_read_b128 a[148:151], v9 offset:38912\n"
"v_cmp_eq_u32 vcc, v19, v27\n"
"v_mfma_f32_16x16x32_bf16 v[124:127], a[224:227], a[120:123], v[124:127]\n"
"s_nop 1\n"
"v_addc_co_u32 v19, vcc, 0, v19, vcc\n"
"s_add_u32 m0, s77, 77824\n"
"v_mfma_f32_16x16x32_bf16 a[40:43], a[224:227], a[128:131], 0\n"
"v_mov_b32 v28, s7\n"
"v_mfma_f32_16x16x32_bf16 a[40:43], a[228:231], a[132:135], a[40:43]\n"
"buffer_load_dwordx4 v4, s[52:55], s70 offen lds\n"
"v_mfma_f32_16x16x32_bf16 a[44:47], a[232:235], a[128:131], 0\n"
"ds_read_b128 a[152:155], v8 offset:40960\n"
"v_add_u32 v28, 1, v28\n"
"v_mfma_f32_16x16x32_bf16 a[44:47], a[236:239], a[132:135], a[44:47]\n"
"buffer_store_dwordx4 v[16:19], v29, s[56:59], 0 offen offset:2048 sc1\n"
"v_mov_b32 v248, 0\n"
"v_mov_b32 v249, 0\n"
"v_mfma_f32_16x16x32_bf16 v[156:159], a[236:239], a[124:127], 0\n"
"s_add_u32 m0, s77, 81920\n"
"ds_read_b128 a[220:223], v7 offset:10240\n"
"v_mfma_f32_16x16x32_bf16 v[156:159], a[232:235], a[120:123], v[156:159]\n"
"ds_read_b128 a[156:159], v9 offset:40960\n"
"v_dot2c_f32_bf16 v248, v224, v232\n"
"v_mfma_f32_16x16x32_bf16 v[152:155], a[232:235], a[112:115], 0\n"
"buffer_load_dwordx4 v4, s[52:55], s71 offen lds\n"
"v_mfma_f32_16x16x32_bf16 v[152:155], a[236:239], a[116:119], v[152:155]\n"
"ds_read_b128 a[216:219], v6 offset:10240\n"
"v_dot2c_f32_bf16 v249, v224, v240\n"
"v_mfma_f32_16x16x32_bf16 v[148:151], a[236:239], a[108:111], 0\n"
"v_dot2c_f32_bf16 v248, v225, v233\n"
"v_dot2c_f32_bf16 v249, v225, v241\n"
"v_mfma_f32_16x16x32_bf16 v[148:151], a[232:235], a[104:107], v[148:151]\n"
"s_add_u32 m0, s77, 86016\n"
"ds_read_b128 a[160:163], v8 offset:43008\n"
"v_mfma_f32_16x16x32_bf16 v[144:147], a[232:235], a[96:99], 0\n"
"v_dot2c_f32_bf16 v248, v226, v234\n"
"v_dot2c_f32_bf16 v249, v226, v242\n"
"v_mfma_f32_16x16x32_bf16 v[144:147], a[236:239], a[100:103], v[144:147]\n"
"buffer_load_dwordx4 v4, s[52:55], s72 offen lds\n"
"v_dot2c_f32_bf16 v248, v227, v235\n"
"v_mfma_f32_16x16x32_bf16 v[140:143], a[236:239], a[92:95], 0\n"
"v_dot2c_f32_bf16 v249, v227, v243\n"
"v_mfma_f32_16x16x32_bf16 v[140:143], a[232:235], a[88:91], v[140:143]\n"
"ds_read_b128 a[164:167], v9 offset:43008\n"
"v_dot2c_f32_bf16 v248, v228, v236\n"
"v_mfma_f32_16x16x32_bf16 v[136:139], a[232:235], a[80:83], 0\n"
"s_add_u32 m0, s77, 90112\n"
"v_dot2c_f32_bf16 v249, v228, v244\n"
"v_dot2c_f32_bf16 v248, v229, v237\n"
"v_mfma_f32_16x16x32_bf16 v[136:139], a[236:239], a[84:87], v[136:139]\n"
"buffer_load_dwordx4 v4, s[52:55], s73 offen lds\n"
"v_dot2c_f32_bf16 v249, v229, v245\n"
"v_mfma_f32_16x16x32_bf16 v[132:135], a[236:239], a[76:79], 0\n"
"ds_read_b128 a[168:171], v8 offset:45056\n"
"v_dot2c_f32_bf16 v248, v230, v238\n"
"v_mfma_f32_16x16x32_bf16 v[132:135], a[232:235], a[72:75], v[132:135]\n"
"v_dot2c_f32_bf16 v249, v230, v246\n"
"s_add_u32 m0, s77, 94208\n"
"v_mfma_f32_16x16x32_bf16 v[128:131], a[232:235], a[64:67], 0\n"
"v_dot2c_f32_bf16 v248, v231, v239\n"
"v_mfma_f32_16x16x32_bf16 v[128:131], a[236:239], a[68:71], v[128:131]\n"
"buffer_load_dwordx4 v4, s[52:55], s74 offen lds\n"
"s_waitcnt lgkmcnt(11)\n"
"v_mfma_f32_16x16x32_bf16 v[160:163], a[208:211], a[64:67], 0\n"
"v_dot2c_f32_bf16 v249, v231, v247\n"
"s_nop 1\n"
"v_mfma_f32_16x16x32_bf16 v[160:163], a[212:215], a[68:71], v[160:163]\n"
"ds_read_b128 a[172:175], v9 offset:45056\n"
"s_nop 1\n"
"v_mfma_f32_16x16x32_bf16 v[164:167], a[212:215], a[76:79], 0\n"
"v_add_f32_dpp v248, v248, v248 row_ror:8 row_mask:0xf bank_mask:0xf\n"
"s_add_u32 m0, s77, 98304\n"
"v_mfma_f32_16x16x32_bf16 v[164:167], a[208:211], a[72:75], v[164:167]\n"
"v_add_f32_dpp v249, v249, v249 row_ror:8 row_mask:0xf bank_mask:0xf\n"
"v_mfma_f32_16x16x32_bf16 v[168:171], a[208:211], a[80:83], 0\n"
"buffer_load_dwordx4 v4, s[52:55], s75 offen lds\n"
"v_mfma_f32_16x16x32_bf16 v[168:171], a[212:215], a[84:87], v[168:171]\n"
"ds_read_b128 a[176:179], v8 offset:47104\n"
"s_nop 1\n"
"v_add_f32_dpp v248, v248, v248 row_ror:4 row_mask:0xf bank_mask:0xf\n"
"v_add_u32 v4, s17, v4\n"
"v_mfma_f32_16x16x32_bf16 v[172:175], a[212:215], a[92:95], 0\n"
"ds_read_b128 a[228:231], v7 offset:12288\n"
"v_add_f32_dpp v249, v249, v249 row_ror:4 row_mask:0xf bank_mask:0xf\n"
"v_mfma_f32_16x16x32_bf16 v[172:175], a[208:211], a[88:91], v[172:175]\n"
"ds_read_b128 a[224:227], v6 offset:12288\n"
"s_nop 1\n"
"s_add_u32 m0, s76, 0\n"
"v_mfma_f32_16x16x32_bf16 v[176:179], a[208:211], a[96:99], 0\n"
"ds_read_b128 a[180:183], v9 offset:47104\n"
"v_add_f32_dpp v248, v248, v248 row_ror:2 row_mask:0xf bank_mask:0xf\n"
"v_mfma_f32_16x16x32_bf16 v[176:179], a[212:215], a[100:103], v[176:179]\n"
"buffer_load_dwordx4 v2, s[40:43], s64 offen lds\n"
"v_add_f32_dpp v249, v249, v249 row_ror:2 row_mask:0xf bank_mask:0xf\n"
"v_mfma_f32_16x16x32_bf16 v[180:183], a[212:215], a[108:111], 0\n"
"s_nop 1\n"
"v_add_f32_dpp v248, v248, v248 row_ror:1 row_mask:0xf bank_mask:0xf\n"
"v_add_f32_dpp v249, v249, v249 row_ror:1 row_mask:0xf bank_mask:0xf\n"
"v_mfma_f32_16x16x32_bf16 v[180:183], a[208:211], a[104:107], v[180:183]\n"
"ds_read_b128 a[184:187], v8 offset:49152\n"
"s_nop 1\n"
"v_mfma_f32_16x16x32_bf16 v[184:187], a[208:211], a[112:115], 0\n"
"v_readlane_b32 s14, v248, 16\n"
"v_readlane_b32 s15, v248, 32\n"
"v_mfma_f32_16x16x32_bf16 v[184:187], a[212:215], a[116:119], v[184:187]\n"
"v_readlane_b32 s38, v248, 48\n"
"v_mfma_f32_16x16x32_bf16 v[188:191], a[212:215], a[124:127], 0\n"
"ds_read_b128 a[188:191], v9 offset:49152\n"
"s_nop 1\n"
"v_mfma_f32_16x16x32_bf16 v[188:191], a[208:211], a[120:123], v[188:191]\n"
"v_add_f32 v248, s14, v248\n"
"s_add_u32 m0, s76, 1024\n"
"v_mfma_f32_16x16x32_bf16 a[48:51], a[208:211], a[128:131], 0\n"
"v_add_f32 v248, s15, v248\n"
"v_mfma_f32_16x16x32_bf16 a[48:51], a[212:215], a[132:135], a[48:51]\n"
"buffer_load_dwordx4 v3, s[40:43], s64 offen lds\n"
"s_waitcnt lgkmcnt(10)\n"
"v_mfma_f32_16x16x32_bf16 a[52:55], a[216:219], a[128:131], 0\n"
"ds_read_b128 a[192:195], v8 offset:51200\n"
"v_add_f32 v248, s38, v248\n"
"v_mfma_f32_16x16x32_bf16 a[52:55], a[220:223], a[132:135], a[52:55]\n"
"v_mul_f32 v248, 0xbfb8aa3b, v248\n"
"v_exp_f32 v248, v248\n"
"v_mfma_f32_16x16x32_bf16 v[220:223], a[220:223], a[124:127], 0\n"
"s_nop 0\n"
"v_add_f32 v248, 1.0, v248\n"
"v_mfma_f32_16x16x32_bf16 v[220:223], a[216:219], a[120:123], v[220:223]\n"
"ds_read_b128 a[196:199], v9 offset:51200\n"
"v_mfma_f32_16x16x32_bf16 v[216:219], a[216:219], a[112:115], 0\n"
"v_rcp_f32 v252, v248\n"
"v_mfma_f32_16x16x32_bf16 v[216:219], a[220:223], a[116:119], v[216:219]\n"
"s_nop 1\n"
"v_readlane_b32 s14, v249, 16\n"
"v_mfma_f32_16x16x32_bf16 v[212:215], a[220:223], a[108:111], 0\n"
"ds_read_b128 a[236:239], v7 offset:14336\n"
"v_readlane_b32 s15, v249, 32\n"
"v_mfma_f32_16x16x32_bf16 v[212:215], a[216:219], a[104:107], v[212:215]\n"
"ds_read_b128 a[232:235], v6 offset:14336\n"
"v_mfma_f32_16x16x32_bf16 v[208:211], a[216:219], a[96:99], 0\n"
"ds_read_b128 a[200:203], v8 offset:53248\n"
"v_readlane_b32 s38, v249, 48\n"
"s_add_u32 m0, s76, 2048\n"
"s_nop 1\n"
"v_mfma_f32_16x16x32_bf16 v[208:211], a[220:223], a[100:103], v[208:211]\n"
"buffer_load_dwordx4 v2, s[40:43], s65 offen lds\n"
"v_add_f32 v249, s14, v249\n"
"v_mfma_f32_16x16x32_bf16 v[204:207], a[220:223], a[92:95], 0\n"
"ds_read_b128 a[204:207], v9 offset:53248\n"
"v_add_f32 v249, s15, v249\n"
"v_add_f32 v249, s38, v249\n"
"v_mfma_f32_16x16x32_bf16 v[204:207], a[216:219], a[88:91], v[204:207]\n"
"v_mul_f32 v249, 0xbfb8aa3b, v249\n"
"v_mfma_f32_16x16x32_bf16 v[200:203], a[216:219], a[80:83], 0\n"
"v_exp_f32 v249, v249\n"
"v_mfma_f32_16x16x32_bf16 v[200:203], a[220:223], a[84:87], v[200:203]\n"
"s_nop 0\n"
"v_add_f32 v249, 1.0, v249\n"
"v_mfma_f32_16x16x32_bf16 v[196:199], a[220:223], a[76:79], 0\n"
"v_rcp_f32 v253, v249\n"
"v_mfma_f32_16x16x32_bf16 v[196:199], a[216:219], a[72:75], v[196:199]\n"
"s_lshl_b32 s14, s78, 3\n"
"s_add_u32 s14, s14, 65536\n"
"s_add_u32 m0, s76, 3072\n"
"v_mfma_f32_16x16x32_bf16 v[192:195], a[216:219], a[64:67], 0\n"
"s_mov_b64 exec, 1\n"
"buffer_store_dwordx2 v[252:253], v250, s[56:59], s14 offen sc1\n"
"s_mov_b64 exec, -1\n"
"v_mfma_f32_16x16x32_bf16 v[192:195], a[220:223], a[68:71], v[192:195]\n"
"buffer_load_dwordx4 v3, s[40:43], s65 offen lds\n"
"s_mov_b32 s78, 5\n"
"s_waitcnt lgkmcnt(9)\n"
"v_mfma_f32_16x16x32_bf16 v[224:227], a[224:227], a[64:67], 0\n"
"s_waitcnt vmcnt(19) lgkmcnt(0)\n"
"s_barrier\n"
"v_mfma_f32_16x16x32_bf16 v[224:227], a[228:231], a[68:71], v[224:227]\n"
"ds_read_b128 a[212:215], v7 offset:32768\n"
"v_mfma_f32_16x16x32_bf16 v[228:231], a[228:231], a[76:79], 0\n"
"ds_read_b128 a[208:211], v6 offset:32768\n"
"s_add_u32 m0, s76, 8192\n"
"v_mfma_f32_16x16x32_bf16 v[228:231], a[224:227], a[72:75], v[228:231]\n"
"buffer_load_dwordx4 v2, s[40:43], s66 offen lds\n"
"v_mfma_f32_16x16x32_bf16 v[232:235], a[224:227], a[80:83], 0\n"
"buffer_load_dword a255, v14, s[24:27], 0 offen\n"
"v_add_u32 v14, 4096, v14\n"
"v_mfma_f32_16x16x32_bf16 v[232:235], a[228:231], a[84:87], v[232:235]\n"
"v_mfma_f32_16x16x32_bf16 v[236:239], a[228:231], a[92:95], 0\n"
"v_mfma_f32_16x16x32_bf16 v[236:239], a[224:227], a[88:91], v[236:239]\n"
"v_mfma_f32_16x16x32_bf16 v[240:243], a[224:227], a[96:99], 0\n"
"v_mfma_f32_16x16x32_bf16 v[240:243], a[228:231], a[100:103], v[240:243]\n"
"v_mfma_f32_16x16x32_bf16 v[244:247], a[228:231], a[108:111], 0\n"
"s_add_u32 m0, s76, 9216\n"
"v_mfma_f32_16x16x32_bf16 v[244:247], a[224:227], a[104:107], v[244:247]\n"
"buffer_load_dwordx4 v3, s[40:43], s66 offen lds\n"
"v_mfma_f32_16x16x32_bf16 v[248:251], a[224:227], a[112:115], 0\n"
"v_mfma_f32_16x16x32_bf16 v[248:251], a[228:231], a[116:119], v[248:251]\n"
"v_mfma_f32_16x16x32_bf16 v[252:255], a[228:231], a[124:127], 0\n"
"v_mfma_f32_16x16x32_bf16 v[252:255], a[224:227], a[120:123], v[252:255]\n"
"v_mfma_f32_16x16x32_bf16 a[56:59], a[224:227], a[128:131], 0\n"
"v_mfma_f32_16x16x32_bf16 a[56:59], a[228:231], a[132:135], a[56:59]\n"
"v_mfma_f32_16x16x32_bf16 a[60:63], a[232:235], a[128:131], 0\n"
"ds_read_b128 a[220:223], v7 offset:34816\n"
"v_mfma_f32_16x16x32_bf16 a[60:63], a[236:239], a[132:135], a[60:63]\n"
"ds_read_b128 a[216:219], v6 offset:34816\n"
"s_add_u32 m0, s76, 10240\n"
"v_mfma_f32_16x16x32_bf16 a[28:31], a[236:239], a[124:127], 0\n"
"buffer_load_dwordx4 v2, s[40:43], s67 offen lds\n"
"v_mfma_f32_16x16x32_bf16 a[28:31], a[232:235], a[120:123], a[28:31]\n"
"v_add_u32 v2, s17, v2\n"
"v_mfma_f32_16x16x32_bf16 a[24:27], a[232:235], a[112:115], 0\n"
"v_mfma_f32_16x16x32_bf16 a[24:27], a[236:239], a[116:119], a[24:27]\n"
"v_mfma_f32_16x16x32_bf16 a[20:23], a[236:239], a[108:111], 0\n"
"v_mfma_f32_16x16x32_bf16 a[20:23], a[232:235], a[104:107], a[20:23]\n"
"v_mfma_f32_16x16x32_bf16 a[16:19], a[232:235], a[96:99], 0\n"
"v_mfma_f32_16x16x32_bf16 a[16:19], a[236:239], a[100:103], a[16:19]\n"
"v_mfma_f32_16x16x32_bf16 a[12:15], a[236:239], a[92:95], 0\n"
"s_add_u32 m0, s76, 11264\n"
"v_mfma_f32_16x16x32_bf16 a[12:15], a[232:235], a[88:91], a[12:15]\n"
"buffer_load_dwordx4 v3, s[40:43], s67 offen lds\n"
"v_mfma_f32_16x16x32_bf16 a[8:11], a[232:235], a[80:83], 0\n"
"v_add_u32 v3, s17, v3\n"
"s_add_u32 s16, s16, 1\n"
"v_mfma_f32_16x16x32_bf16 a[8:11], a[236:239], a[84:87], a[8:11]\n"
"s_cmp_eq_u32 s16, 15\n"
"s_cselect_b32 s28, -13, 1\n"
"s_lshl_b32 s17, s28, 7\n"
"v_mfma_f32_16x16x32_bf16 a[4:7], a[236:239], a[76:79], 0\n"
"s_mul_i32 s28, s28, 458752\n"
"v_mfma_f32_16x16x32_bf16 a[4:7], a[232:235], a[72:75], a[4:7]\n"
"v_mfma_f32_16x16x32_bf16 a[0:3], a[232:235], a[64:67], 0\n"
"v_mfma_f32_16x16x32_bf16 a[0:3], a[236:239], a[68:71], a[0:3]\n"
"s_waitcnt lgkmcnt(2)\n"
"v_mfma_f32_16x16x32_bf16 v[32:35], a[208:211], a[136:139], v[32:35]\n"
"ds_read_b128 a[228:231], v7 offset:36864\n"
"v_mfma_f32_16x16x32_bf16 v[32:35], a[212:215], a[140:143], v[32:35]\n"
"ds_read_b128 a[224:227], v6 offset:36864\n"
"s_add_u32 m0, s77, 102400\n"
"v_mfma_f32_16x16x32_bf16 v[36:39], a[212:215], a[148:151], v[36:39]\n"
"buffer_load_dwordx4 v4, s[52:55], 0 offen lds\n"
"v_mfma_f32_16x16x32_bf16 v[36:39], a[208:211], a[144:147], v[36:39]\n"
"v_mfma_f32_16x16x32_bf16 v[40:43], a[208:211], a[152:155], v[40:43]\n"
"v_mfma_f32_16x16x32_bf16 v[40:43], a[212:215], a[156:159], v[40:43]\n"
"v_mfma_f32_16x16x32_bf16 v[44:47], a[212:215], a[164:167], v[44:47]\n"
"v_mfma_f32_16x16x32_bf16 v[44:47], a[208:211], a[160:163], v[44:47]\n"
"v_mfma_f32_16x16x32_bf16 v[48:51], a[208:211], a[168:171], v[48:51]\n"
"s_add_u32 m0, s77, 106496\n"
"v_mfma_f32_16x16x32_bf16 v[48:51], a[212:215], a[172:175], v[48:51]\n"
"buffer_load_dwordx4 v4, s[52:55], s68 offen lds\n"
"v_mfma_f32_16x16x32_bf16 v[52:55], a[212:215], a[180:183], v[52:55]\n"
"v_mfma_f32_16x16x32_bf16 v[52:55], a[208:211], a[176:179], v[52:55]\n"
"v_mfma_f32_16x16x32_bf16 v[56:59], a[208:211], a[184:187], v[56:59]\n"
"v_mfma_f32_16x16x32_bf16 v[56:59], a[212:215], a[188:191], v[56:59]\n"
"v_mfma_f32_16x16x32_bf16 v[60:63], a[212:215], a[196:199], v[60:63]\n"
"v_mfma_f32_16x16x32_bf16 v[60:63], a[208:211], a[192:195], v[60:63]\n"
"s_add_u32 m0, s77, 110592\n"
"v_mfma_f32_16x16x32_bf16 a[32:35], a[208:211], a[200:203], a[32:35]\n"
"buffer_load_dwordx4 v4, s[52:55], s69 offen lds\n"
"v_mfma_f32_16x16x32_bf16 a[32:35], a[212:215], a[204:207], a[32:35]\n"
"s_waitcnt lgkmcnt(2)\n"
"v_mfma_f32_16x16x32_bf16 a[36:39], a[216:219], a[200:203], a[36:39]\n"
"ds_read_b128 a[236:239], v7 offset:38912\n"
"v_mfma_f32_16x16x32_bf16 a[36:39], a[220:223], a[204:207], a[36:39]\n"
"ds_read_b128 a[232:235], v6 offset:38912\n"
"v_mfma_f32_16x16x32_bf16 v[92:95], a[220:223], a[196:199], v[92:95]\n"
"v_mfma_f32_16x16x32_bf16 v[92:95], a[216:219], a[192:195], v[92:95]\n"
"v_mfma_f32_16x16x32_bf16 v[88:91], a[216:219], a[184:187], v[88:91]\n"
"s_add_u32 m0, s77, 114688\n"
"v_mfma_f32_16x16x32_bf16 v[88:91], a[220:223], a[188:191], v[88:91]\n"
"buffer_load_dwordx4 v4, s[52:55], s70 offen lds\n"
"v_mfma_f32_16x16x32_bf16 v[84:87], a[220:223], a[180:183], v[84:87]\n"
"v_mfma_f32_16x16x32_bf16 v[84:87], a[216:219], a[176:179], v[84:87]\n"
"v_mfma_f32_16x16x32_bf16 v[80:83], a[216:219], a[168:171], v[80:83]\n"
"v_mfma_f32_16x16x32_bf16 v[80:83], a[220:223], a[172:175], v[80:83]\n"
"v_mfma_f32_16x16x32_bf16 v[76:79], a[220:223], a[164:167], v[76:79]\n"
"v_mfma_f32_16x16x32_bf16 v[76:79], a[216:219], a[160:163], v[76:79]\n"
"s_add_u32 m0, s77, 118784\n"
"v_mfma_f32_16x16x32_bf16 v[72:75], a[216:219], a[152:155], v[72:75]\n"
"buffer_load_dwordx4 v4, s[52:55], s71 offen lds\n"
"v_mfma_f32_16x16x32_bf16 v[72:75], a[220:223], a[156:159], v[72:75]\n"
"v_mfma_f32_16x16x32_bf16 v[68:71], a[220:223], a[148:151], v[68:71]\n"
"v_mfma_f32_16x16x32_bf16 v[68:71], a[216:219], a[144:147], v[68:71]\n"
"v_mfma_f32_16x16x32_bf16 v[64:67], a[216:219], a[136:139], v[64:67]\n"
"v_mfma_f32_16x16x32_bf16 v[64:67], a[220:223], a[140:143], v[64:67]\n"
"s_waitcnt vmcnt(15) lgkmcnt(0)\n"
"s_barrier\n"
"v_mfma_f32_16x16x32_bf16 v[96:99], a[224:227], a[136:139], v[96:99]\n"
"ds_read_b128 a[212:215], v7 offset:40960\n"
"s_add_u32 m0, s77, 122880\n"
"v_mfma_f32_16x16x32_bf16 v[96:99], a[228:231], a[140:143], v[96:99]\n"
"ds_read_b128 a[208:211], v6 offset:40960\n"
"v_mfma_f32_16x16x32_bf16 v[100:103], a[228:231], a[148:151], v[100:103]\n"
"buffer_load_dwordx4 v4, s[52:55], s72 offen lds\n"
"v_mfma_f32_16x16x32_bf16 v[100:103], a[224:227], a[144:147], v[100:103]\n"
"ds_read_b128 a[64:67], v8 offset:0\n"
"v_mfma_f32_16x16x32_bf16 v[104:107], a[224:227], a[152:155], v[104:107]\n"
"v_mfma_f32_16x16x32_bf16 v[104:107], a[228:231], a[156:159], v[104:107]\n"
"ds_read_b128 a[68:71], v9 offset:0\n"
"v_mfma_f32_16x16x32_bf16 v[108:111], a[228:231], a[164:167], v[108:111]\n"
"v_mfma_f32_16x16x32_bf16 v[108:111], a[224:227], a[160:163], v[108:111]\n"
"s_add_u32 m0, s77, 126976\n"
"v_mfma_f32_16x16x32_bf16 v[112:115], a[224:227], a[168:171], v[112:115]\n"
"ds_read_b128 a[72:75], v8 offset:2048\n"
"v_mfma_f32_16x16x32_bf16 v[112:115], a[228:231], a[172:175], v[112:115]\n"
"buffer_load_dwordx4 v4, s[52:55], s73 offen lds\n"
"v_mfma_f32_16x16x32_bf16 v[116:119], a[228:231], a[180:183], v[116:119]\n"
"v_mfma_f32_16x16x32_bf16 v[116:119], a[224:227], a[176:179], v[116:119]\n"
"v_mfma_f32_16x16x32_bf16 v[120:123], a[224:227], a[184:187], v[120:123]\n"
"ds_read_b128 a[76:79], v9 offset:2048\n"
"v_mfma_f32_16x16x32_bf16 v[120:123], a[228:231], a[188:191], v[120:123]\n"
"v_mfma_f32_16x16x32_bf16 v[124:127], a[228:231], a[196:199], v[124:127]\n"
"s_add_u32 m0, s77, 131072\n"
"v_mfma_f32_16x16x32_bf16 v[124:127], a[224:227], a[192:195], v[124:127]\n"
"ds_read_b128 a[80:83], v8 offset:4096\n"
"v_mfma_f32_16x16x32_bf16 a[40:43], a[224:227], a[200:203], a[40:43]\n"
"buffer_load_dwordx4 v4, s[52:55], s74 offen lds\n"
"v_mfma_f32_16x16x32_bf16 a[40:43], a[228:231], a[204:207], a[40:43]\n"
"v_mfma_f32_16x16x32_bf16 a[44:47], a[232:235], a[200:203], a[44:47]\n"
"ds_read_b128 a[220:223], v7 offset:43008\n"
"v_mfma_f32_16x16x32_bf16 a[44:47], a[236:239], a[204:207], a[44:47]\n"
"ds_read_b128 a[84:87], v9 offset:4096\n"
"v_mfma_f32_16x16x32_bf16 v[156:159], a[236:239], a[196:199], v[156:159]\n"
"ds_read_b128 a[216:219], v6 offset:43008\n"
"v_mfma_f32_16x16x32_bf16 v[156:159], a[232:235], a[192:195], v[156:159]\n"
"s_add_u32 m0, s77, 135168\n"
"v_mfma_f32_16x16x32_bf16 v[152:155], a[232:235], a[184:187], v[152:155]\n"
"ds_read_b128 a[88:91], v8 offset:6144\n"
"v_mfma_f32_16x16x32_bf16 v[152:155], a[236:239], a[188:191], v[152:155]\n"
"buffer_load_dwordx4 v4, s[52:55], s75 offen lds\n"
"v_add_u32 v4, s17, v4\n"
"v_mfma_f32_16x16x32_bf16 v[148:151], a[236:239], a[180:183], v[148:151]\n"
"v_mfma_f32_16x16x32_bf16 v[148:151], a[232:235], a[176:179], v[148:151]\n"
"ds_read_b128 a[92:95], v9 offset:6144\n"
"v_mfma_f32_16x16x32_bf16 v[144:147], a[232:235], a[168:171], v[144:147]\n"
"v_mfma_f32_16x16x32_bf16 v[144:147], a[236:239], a[172:175], v[144:147]\n"
"v_mfma_f32_16x16x32_bf16 v[140:143], a[236:239], a[164:167], v[140:143]\n"
"ds_read_b128 a[96:99], v8 offset:8192\n"
"v_mfma_f32_16x16x32_bf16 v[140:143], a[232:235], a[160:163], v[140:143]\n"
"v_mfma_f32_16x16x32_bf16 v[136:139], a[232:235], a[152:155], v[136:139]\n"
"v_mfma_f32_16x16x32_bf16 v[136:139], a[236:239], a[156:159], v[136:139]\n"
"v_mfma_f32_16x16x32_bf16 v[132:135], a[236:239], a[148:151], v[132:135]\n"
"ds_read_b128 a[100:103], v9 offset:8192\n"
"v_mfma_f32_16x16x32_bf16 v[132:135], a[232:235], a[144:147], v[132:135]\n"
"v_mfma_f32_16x16x32_bf16 v[128:131], a[232:235], a[136:139], v[128:131]\n"
"v_mfma_f32_16x16x32_bf16 v[128:131], a[236:239], a[140:143], v[128:131]\n"
"ds_read_b128 a[104:107], v8 offset:10240\n"
"s_waitcnt lgkmcnt(13)\n"
"v_mfma_f32_16x16x32_bf16 v[160:163], a[208:211], a[136:139], v[160:163]\n"
"ds_read_b128 a[228:231], v7 offset:45056\n"
"v_mfma_f32_16x16x32_bf16 v[160:163], a[212:215], a[140:143], v[160:163]\n"
"ds_read_b128 a[224:227], v6 offset:45056\n"
"s_add_u32 m0, s76, 32768\n"
"v_mfma_f32_16x16x32_bf16 v[164:167], a[212:215], a[148:151], v[164:167]\n"
"ds_read_b128 a[108:111], v9 offset:10240\n"
"v_mfma_f32_16x16x32_bf16 v[164:167], a[208:211], a[144:147], v[164:167]\n"
"buffer_load_dwordx4 v2, s[40:43], s64 offen lds\n"
"v_mfma_f32_16x16x32_bf16 v[168:171], a[208:211], a[152:155], v[168:171]\n"
"v_mfma_f32_16x16x32_bf16 v[168:171], a[212:215], a[156:159], v[168:171]\n"
"v_mfma_f32_16x16x32_bf16 v[172:175], a[212:215], a[164:167], v[172:175]\n"
"ds_read_b128 a[112:115], v8 offset:12288\n"
"v_mfma_f32_16x16x32_bf16 v[172:175], a[208:211], a[160:163], v[172:175]\n"
"v_mfma_f32_16x16x32_bf16 v[176:179], a[208:211], a[168:171], v[176:179]\n"
"v_mfma_f32_16x16x32_bf16 v[176:179], a[212:215], a[172:175], v[176:179]\n"
"ds_read_b128 a[116:119], v9 offset:12288\n"
"v_mfma_f32_16x16x32_bf16 v[180:183], a[212:215], a[180:183], v[180:183]\n"
"s_add_u32 m0, s76, 33792\n"
"v_mfma_f32_16x16x32_bf16 v[180:183], a[208:211], a[176:179], v[180:183]\n"
"buffer_load_dwordx4 v3, s[40:43], s64 offen lds\n"
"v_mfma_f32_16x16x32_bf16 v[184:187], a[208:211], a[184:187], v[184:187]\n"
"ds_read_b128 a[120:123], v8 offset:14336\n"
"v_mfma_f32_16x16x32_bf16 v[184:187], a[212:215], a[188:191], v[184:187]\n"
"v_mfma_f32_16x16x32_bf16 v[188:191], a[212:215], a[196:199], v[188:191]\n"
"v_mfma_f32_16x16x32_bf16 v[188:191], a[208:211], a[192:195], v[188:191]\n"
"v_mfma_f32_16x16x32_bf16 a[48:51], a[208:211], a[200:203], a[48:51]\n"
"ds_read_b128 a[124:127], v9 offset:14336\n"
"v_mfma_f32_16x16x32_bf16 a[48:51], a[212:215], a[204:207], a[48:51]\n"
"s_waitcnt lgkmcnt(12)\n"
"v_mfma_f32_16x16x32_bf16 a[52:55], a[216:219], a[200:203], a[52:55]\n"
"ds_read_b128 a[236:239], v7 offset:47104\n"
"v_mfma_f32_16x16x32_bf16 a[52:55], a[220:223], a[204:207], a[52:55]\n"
"ds_read_b128 a[232:235], v6 offset:47104\n"
"v_mfma_f32_16x16x32_bf16 v[220:223], a[220:223], a[196:199], v[220:223]\n"
"ds_read_b128 a[128:131], v8 offset:16384\n"
"s_add_u32 m0, s76, 34816\n"
"v_mfma_f32_16x16x32_bf16 v[220:223], a[216:219], a[192:195], v[220:223]\n"
"buffer_load_dwordx4 v2, s[40:43], s65 offen lds\n"
"v_mfma_f32_16x16x32_bf16 v[216:219], a[216:219], a[184:187], v[216:219]\n"
"ds_read_b128 a[132:135], v9 offset:16384\n"
"v_mfma_f32_16x16x32_bf16 v[216:219], a[220:223], a[188:191], v[216:219]\n"
"v_mfma_f32_16x16x32_bf16 v[212:215], a[220:223], a[180:183], v[212:215]\n"
"v_mfma_f32_16x16x32_bf16 v[212:215], a[216:219], a[176:179], v[212:215]\n"
"v_mfma_f32_16x16x32_bf16 v[208:211], a[216:219], a[168:171], v[208:211]\n"
"v_mfma_f32_16x16x32_bf16 v[208:211], a[220:223], a[172:175], v[208:211]\n"
"v_mfma_f32_16x16x32_bf16 v[204:207], a[220:223], a[164:167], v[204:207]\n"
"s_add_u32 m0, s76, 35840\n"
"v_mfma_f32_16x16x32_bf16 v[204:207], a[216:219], a[160:163], v[204:207]\n"
"buffer_load_dwordx4 v3, s[40:43], s65 offen lds\n"
"v_mfma_f32_16x16x32_bf16 v[200:203], a[216:219], a[152:155], v[200:203]\n"
"v_mfma_f32_16x16x32_bf16 v[200:203], a[220:223], a[156:159], v[200:203]\n"
"v_mfma_f32_16x16x32_bf16 v[196:199], a[220:223], a[148:151], v[196:199]\n"
"v_mfma_f32_16x16x32_bf16 v[196:199], a[216:219], a[144:147], v[196:199]\n"
"v_mfma_f32_16x16x32_bf16 v[192:195], a[216:219], a[136:139], v[192:195]\n"
"v_mfma_f32_16x16x32_bf16 v[192:195], a[220:223], a[140:143], v[192:195]\n"
"s_waitcnt vmcnt(18) lgkmcnt(0)\n"
"s_barrier\n"
"v_mfma_f32_16x16x32_bf16 v[224:227], a[224:227], a[136:139], v[224:227]\n"
"ds_read_b128 a[212:215], v7 offset:0\n"
"v_mfma_f32_16x16x32_bf16 v[224:227], a[228:231], a[140:143], v[224:227]\n"
"ds_read_b128 a[208:211], v6 offset:0\n"
"s_add_u32 m0, s76, 40960\n"
"v_mfma_f32_16x16x32_bf16 v[228:231], a[228:231], a[148:151], v[228:231]\n"
"buffer_load_dwordx4 v2, s[40:43], s66 offen lds\n"
"v_mfma_f32_16x16x32_bf16 v[228:231], a[224:227], a[144:147], v[228:231]\n"
"v_mfma_f32_16x16x32_bf16 v[232:235], a[224:227], a[152:155], v[232:235]\n"
"buffer_load_dword a255, v15, s[60:63], 0 offen\n"
"v_add_u32 v15, 2048, v15\n"
"v_mfma_f32_16x16x32_bf16 v[232:235], a[228:231], a[156:159], v[232:235]\n"
"v_mfma_f32_16x16x32_bf16 v[236:239], a[228:231], a[164:167], v[236:239]\n"
"v_mfma_f32_16x16x32_bf16 v[236:239], a[224:227], a[160:163], v[236:239]\n"
"v_mfma_f32_16x16x32_bf16 v[240:243], a[224:227], a[168:171], v[240:243]\n"
"v_mfma_f32_16x16x32_bf16 v[240:243], a[228:231], a[172:175], v[240:243]\n"
"v_mfma_f32_16x16x32_bf16 v[244:247], a[228:231], a[180:183], v[244:247]\n"
"s_add_u32 m0, s76, 41984\n"
"v_mfma_f32_16x16x32_bf16 v[244:247], a[224:227], a[176:179], v[244:247]\n"
"buffer_store_dword v23, v22, s[56:59], 0 offen sc1\n"
"v_mfma_f32_16x16x32_bf16 v[248:251], a[224:227], a[184:187], v[248:251]\n"
"buffer_load_dwordx4 v3, s[40:43], s66 offen lds\n"
"v_mfma_f32_16x16x32_bf16 v[248:251], a[228:231], a[188:191], v[248:251]\n"
"v_mfma_f32_16x16x32_bf16 v[252:255], a[228:231], a[196:199], v[252:255]\n"
"v_mfma_f32_16x16x32_bf16 v[252:255], a[224:227], a[192:195], v[252:255]\n"
"v_mfma_f32_16x16x32_bf16 a[56:59], a[224:227], a[200:203], a[56:59]\n"
"v_mfma_f32_16x16x32_bf16 a[56:59], a[228:231], a[204:207], a[56:59]\n"
"v_mfma_f32_16x16x32_bf16 a[60:63], a[232:235], a[200:203], a[60:63]\n"
"ds_read_b128 a[220:223], v7 offset:2048\n"
"v_mfma_f32_16x16x32_bf16 a[60:63], a[236:239], a[204:207], a[60:63]\n"
"buffer_load_dword v23, v21, s[56:59], 0 offen sc1\n"
"v_mfma_f32_16x16x32_bf16 a[28:31], a[236:239], a[196:199], a[28:31]\n"
"ds_read_b128 a[216:219], v6 offset:2048\n"
"s_add_u32 m0, s76, 43008\n"
"v_mfma_f32_16x16x32_bf16 a[28:31], a[232:235], a[192:195], a[28:31]\n"
"buffer_load_dwordx4 v2, s[40:43], s67 offen lds\n"
"v_add_u32 v2, s17, v2\n"
"v_mfma_f32_16x16x32_bf16 a[24:27], a[232:235], a[184:187], a[24:27]\n"
"v_mfma_f32_16x16x32_bf16 a[24:27], a[236:239], a[188:191], a[24:27]\n"
"v_mfma_f32_16x16x32_bf16 a[20:23], a[236:239], a[180:183], a[20:23]\n"
"v_mfma_f32_16x16x32_bf16 a[20:23], a[232:235], a[176:179], a[20:23]\n"
"v_mfma_f32_16x16x32_bf16 a[16:19], a[232:235], a[168:171], a[16:19]\n"
"v_mfma_f32_16x16x32_bf16 a[16:19], a[236:239], a[172:175], a[16:19]\n"
"v_mfma_f32_16x16x32_bf16 a[12:15], a[236:239], a[164:167], a[12:15]\n"
"s_add_u32 m0, s76, 44032\n"
"v_mfma_f32_16x16x32_bf16 a[12:15], a[232:235], a[160:163], a[12:15]\n"
"buffer_load_dwordx4 v3, s[40:43], s67 offen lds\n"
"v_mfma_f32_16x16x32_bf16 a[8:11], a[232:235], a[152:155], a[8:11]\n"
"v_add_u32 v3, s17, v3\n"
"s_add_u32 s16, s16, 1\n"
"v_mfma_f32_16x16x32_bf16 a[8:11], a[236:239], a[156:159], a[8:11]\n"
"s_cmp_eq_u32 s16, 15\n"
"s_cselect_b32 s28, -13, 1\n"
"s_lshl_b32 s17, s28, 7\n"
"v_mfma_f32_16x16x32_bf16 a[4:7], a[236:239], a[148:151], a[4:7]\n"
"s_mul_i32 s28, s28, 458752\n"
"v_mfma_f32_16x16x32_bf16 a[4:7], a[232:235], a[144:147], a[4:7]\n"
"v_mfma_f32_16x16x32_bf16 a[0:3], a[232:235], a[136:139], a[0:3]\n"
"v_mfma_f32_16x16x32_bf16 a[0:3], a[236:239], a[140:143], a[0:3]\n"
"T2loop_%=:\n"
"s_waitcnt lgkmcnt(2)\n"
"v_mfma_f32_16x16x32_bf16 v[32:35], a[208:211], a[64:67], v[32:35]\n"
"ds_read_b128 a[228:231], v7 offset:4096\n"
"v_mfma_f32_16x16x32_bf16 v[32:35], a[212:215], a[68:71], v[32:35]\n"
"ds_read_b128 a[224:227], v6 offset:4096\n"
"s_add_u32 m0, s77, 65536\n"
"v_mfma_f32_16x16x32_bf16 v[36:39], a[212:215], a[76:79], v[36:39]\n"
"buffer_load_dwordx4 v4, s[52:55], 0 offen lds\n"
"v_mfma_f32_16x16x32_bf16 v[36:39], a[208:211], a[72:75], v[36:39]\n"
"v_mfma_f32_16x16x32_bf16 v[40:43], a[208:211], a[80:83], v[40:43]\n"
"v_mfma_f32_16x16x32_bf16 v[40:43], a[212:215], a[84:87], v[40:43]\n"
"v_mfma_f32_16x16x32_bf16 v[44:47], a[212:215], a[92:95], v[44:47]\n"
"v_mfma_f32_16x16x32_bf16 v[44:47], a[208:211], a[88:91], v[44:47]\n"
"v_mfma_f32_16x16x32_bf16 v[48:51], a[208:211], a[96:99], v[48:51]\n"
"s_add_u32 m0, s77, 69632\n"
"v_mfma_f32_16x16x32_bf16 v[48:51], a[212:215], a[100:103], v[48:51]\n"
"buffer_load_dwordx4 v4, s[52:55], s68 offen lds\n"
"v_mfma_f32_16x16x32_bf16 v[52:55], a[212:215], a[108:111], v[52:55]\n"
"v_mfma_f32_16x16x32_bf16 v[52:55], a[208:211], a[104:107], v[52:55]\n"
"v_mfma_f32_16x16x32_bf16 v[56:59], a[208:211], a[112:115], v[56:59]\n"
"v_mfma_f32_16x16x32_bf16 v[56:59], a[212:215], a[116:119], v[56:59]\n"
"v_mfma_f32_16x16x32_bf16 v[60:63], a[212:215], a[124:127], v[60:63]\n"
"v_mfma_f32_16x16x32_bf16 v[60:63], a[208:211], a[120:123], v[60:63]\n"
"s_add_u32 m0, s77, 73728\n"
"v_mfma_f32_16x16x32_bf16 a[32:35], a[208:211], a[128:131], a[32:35]\n"
"buffer_load_dwordx4 v4, s[52:55], s69 offen lds\n"
"v_mfma_f32_16x16x32_bf16 a[32:35], a[212:215], a[132:135], a[32:35]\n"
"s_waitcnt lgkmcnt(2)\n"
"v_mfma_f32_16x16x32_bf16 a[36:39], a[216:219], a[128:131], a[36:39]\n"
"ds_read_b128 a[236:239], v7 offset:6144\n"
"v_mfma_f32_16x16x32_bf16 a[36:39], a[220:223], a[132:135], a[36:39]\n"
"ds_read_b128 a[232:235], v6 offset:6144\n"
"v_mfma_f32_16x16x32_bf16 v[92:95], a[220:223], a[124:127], v[92:95]\n"
"v_mfma_f32_16x16x32_bf16 v[92:95], a[216:219], a[120:123], v[92:95]\n"
"v_mfma_f32_16x16x32_bf16 v[88:91], a[216:219], a[112:115], v[88:91]\n"
"s_add_u32 m0, s77, 77824\n"
"v_mfma_f32_16x16x32_bf16 v[88:91], a[220:223], a[116:119], v[88:91]\n"
"buffer_load_dwordx4 v4, s[52:55], s70 offen lds\n"
"v_mfma_f32_16x16x32_bf16 v[84:87], a[220:223], a[108:111], v[84:87]\n"
"v_mfma_f32_16x16x32_bf16 v[84:87], a[216:219], a[104:107], v[84:87]\n"
"v_mfma_f32_16x16x32_bf16 v[80:83], a[216:219], a[96:99], v[80:83]\n"
"v_mfma_f32_16x16x32_bf16 v[80:83], a[220:223], a[100:103], v[80:83]\n"
"v_mfma_f32_16x16x32_bf16 v[76:79], a[220:223], a[92:95], v[76:79]\n"
"v_mfma_f32_16x16x32_bf16 v[76:79], a[216:219], a[88:91], v[76:79]\n"
"s_add_u32 m0, s77, 81920\n"
"v_mfma_f32_16x16x32_bf16 v[72:75], a[216:219], a[80:83], v[72:75]\n"
"buffer_load_dwordx4 v4, s[52:55], s71 offen lds\n"
"v_mfma_f32_16x16x32_bf16 v[72:75], a[220:223], a[84:87], v[72:75]\n"
"v_mfma_f32_16x16x32_bf16 v[68:71], a[220:223], a[76:79], v[68:71]\n"
"v_mfma_f32_16x16x32_bf16 v[68:71], a[216:219], a[72:75], v[68:71]\n"
"v_mfma_f32_16x16x32_bf16 v[64:67], a[216:219], a[64:67], v[64:67]\n"
"v_mfma_f32_16x16x32_bf16 v[64:67], a[220:223], a[68:71], v[64:67]\n"
"s_waitcnt vmcnt(16) lgkmcnt(0)\n"
"s_barrier\n"
"v_mfma_f32_16x16x32_bf16 v[96:99], a[224:227], a[64:67], v[96:99]\n"
"ds_read_b128 a[212:215], v7 offset:8192\n"
"s_add_u32 m0, s77, 86016\n"
"v_mfma_f32_16x16x32_bf16 v[96:99], a[228:231], a[68:71], v[96:99]\n"
"ds_read_b128 a[208:211], v6 offset:8192\n"
"v_mfma_f32_16x16x32_bf16 v[100:103], a[228:231], a[76:79], v[100:103]\n"
"buffer_load_dwordx4 v4, s[52:55], s72 offen lds\n"
"v_mfma_f32_16x16x32_bf16 v[100:103], a[224:227], a[72:75], v[100:103]\n"
"ds_read_b128 a[136:139], v8 offset:36864\n"
"v_mfma_f32_16x16x32_bf16 v[104:107], a[224:227], a[80:83], v[104:107]\n"
"v_mfma_f32_16x16x32_bf16 v[104:107], a[228:231], a[84:87], v[104:107]\n"
"ds_read_b128 a[140:143], v9 offset:36864\n"
"v_mfma_f32_16x16x32_bf16 v[108:111], a[228:231], a[92:95], v[108:111]\n"
"v_mfma_f32_16x16x32_bf16 v[108:111], a[224:227], a[88:91], v[108:111]\n"
"s_add_u32 m0, s77, 90112\n"
"v_mfma_f32_16x16x32_bf16 v[112:115], a[224:227], a[96:99], v[112:115]\n"
"ds_read_b128 a[144:147], v8 offset:38912\n"
"v_mfma_f32_16x16x32_bf16 v[112:115], a[228:231], a[100:103], v[112:115]\n"
"buffer_load_dwordx4 v4, s[52:55], s73 offen lds\n"
"v_mfma_f32_16x16x32_bf16 v[116:119], a[228:231], a[108:111], v[116:119]\n"
"v_mfma_f32_16x16x32_bf16 v[116:119], a[224:227], a[104:107], v[116:119]\n"
"v_mfma_f32_16x16x32_bf16 v[120:123], a[224:227], a[112:115], v[120:123]\n"
"ds_read_b128 a[148:151], v9 offset:38912\n"
"v_mfma_f32_16x16x32_bf16 v[120:123], a[228:231], a[116:119], v[120:123]\n"
"v_mfma_f32_16x16x32_bf16 v[124:127], a[228:231], a[124:127], v[124:127]\n"
"s_add_u32 m0, s77, 94208\n"
"v_mfma_f32_16x16x32_bf16 v[124:127], a[224:227], a[120:123], v[124:127]\n"
"ds_read_b128 a[152:155], v8 offset:40960\n"
"v_mfma_f32_16x16x32_bf16 a[40:43], a[224:227], a[128:131], a[40:43]\n"
"buffer_load_dwordx4 v4, s[52:55], s74 offen lds\n"
"v_mfma_f32_16x16x32_bf16 a[40:43], a[228:231], a[132:135], a[40:43]\n"
"v_mfma_f32_16x16x32_bf16 a[44:47], a[232:235], a[128:131], a[44:47]\n"
"ds_read_b128 a[220:223], v7 offset:10240\n"
"v_mfma_f32_16x16x32_bf16 a[44:47], a[236:239], a[132:135], a[44:47]\n"
"ds_read_b128 a[156:159], v9 offset:40960\n"
"v_mfma_f32_16x16x32_bf16 v[156:159], a[236:239], a[124:127], v[156:159]\n"
"ds_read_b128 a[216:219], v6 offset:10240\n"
"v_mfma_f32_16x16x32_bf16 v[156:159], a[232:235], a[120:123], v[156:159]\n"
"s_add_u32 m0, s77, 98304\n"
"v_mfma_f32_16x16x32_bf16 v[152:155], a[232:235], a[112:115], v[152:155]\n"
"ds_read_b128 a[160:163], v8 offset:43008\n"
"v_mfma_f32_16x16x32_bf16 v[152:155], a[236:239], a[116:119], v[152:155]\n"
"buffer_load_dwordx4 v4, s[52:55], s75 offen lds\n"
"v_add_u32 v4, s17, v4\n"
"v_mfma_f32_16x16x32_bf16 v[148:151], a[236:239], a[108:111], v[148:151]\n"
"v_mfma_f32_16x16x32_bf16 v[148:151], a[232:235], a[104:107], v[148:151]\n"
"ds_read_b128 a[164:167], v9 offset:43008\n"
"v_mfma_f32_16x16x32_bf16 v[144:147], a[232:235], a[96:99], v[144:147]\n"
"v_mfma_f32_16x16x32_bf16 v[144:147], a[236:239], a[100:103], v[144:147]\n"
"v_mfma_f32_16x16x32_bf16 v[140:143], a[236:239], a[92:95], v[140:143]\n"
"ds_read_b128 a[168:171], v8 offset:45056\n"
"v_mfma_f32_16x16x32_bf16 v[140:143], a[232:235], a[88:91], v[140:143]\n"
"v_mfma_f32_16x16x32_bf16 v[136:139], a[232:235], a[80:83], v[136:139]\n"
"v_mfma_f32_16x16x32_bf16 v[136:139], a[236:239], a[84:87], v[136:139]\n"
"v_mfma_f32_16x16x32_bf16 v[132:135], a[236:239], a[76:79], v[132:135]\n"
"ds_read_b128 a[172:175], v9 offset:45056\n"
"v_mfma_f32_16x16x32_bf16 v[132:135], a[232:235], a[72:75], v[132:135]\n"
"v_mfma_f32_16x16x32_bf16 v[128:131], a[232:235], a[64:67], v[128:131]\n"
"v_mfma_f32_16x16x32_bf16 v[128:131], a[236:239], a[68:71], v[128:131]\n"
"ds_read_b128 a[176:179], v8 offset:47104\n"
"s_waitcnt lgkmcnt(13)\n"
"v_mfma_f32_16x16x32_bf16 v[160:163], a[208:211], a[64:67], v[160:163]\n"
"ds_read_b128 a[228:231], v7 offset:12288\n"
"v_mfma_f32_16x16x32_bf16 v[160:163], a[212:215], a[68:71], v[160:163]\n"
"ds_read_b128 a[224:227], v6 offset:12288\n"
"s_add_u32 m0, s76, 0\n"
"v_mfma_f32_16x16x32_bf16 v[164:167], a[212:215], a[76:79], v[164:167]\n"
"ds_read_b128 a[180:183], v9 offset:47104\n"
"v_mfma_f32_16x16x32_bf16 v[164:167], a[208:211], a[72:75], v[164:167]\n"
"buffer_load_dwordx4 v2, s[40:43], s64 offen lds\n"
"v_mfma_f32_16x16x32_bf16 v[168:171], a[208:211], a[80:83], v[168:171]\n"
"v_mfma_f32_16x16x32_bf16 v[168:171], a[212:215], a[84:87], v[168:171]\n"
"v_mfma_f32_16x16x32_bf16 v[172:175], a[212:215], a[92:95], v[172:175]\n"
"ds_read_b128 a[184:187], v8 offset:49152\n"
"v_mfma_f32_16x16x32_bf16 v[172:175], a[208:211], a[88:91], v[172:175]\n"
"v_mfma_f32_16x16x32_bf16 v[176:179], a[208:211], a[96:99], v[176:179]\n"
"v_mfma_f32_16x16x32_bf16 v[176:179], a[212:215], a[100:103], v[176:179]\n"
"ds_read_b128 a[188:191], v9 offset:49152\n"
"v_mfma_f32_16x16x32_bf16 v[180:183], a[212:215], a[108:111], v[180:183]\n"
"s_add_u32 m0, s76, 1024\n"
"v_mfma_f32_16x16x32_bf16 v[180:183], a[208:211], a[104:107], v[180:183]\n"
"buffer_load_dwordx4 v3, s[40:43], s64 offen lds\n"
"v_mfma_f32_16x16x32_bf16 v[184:187], a[208:211], a[112:115], v[184:187]\n"
"ds_read_b128 a[192:195], v8 offset:51200\n"
"v_mfma_f32_16x16x32_bf16 v[184:187], a[212:215], a[116:119], v[184:187]\n"
"v_mfma_f32_16x16x32_bf16 v[188:191], a[212:215], a[124:127], v[188:191]\n"
"v_mfma_f32_16x16x32_bf16 v[188:191], a[208:211], a[120:123], v[188:191]\n"
"v_mfma_f32_16x16x32_bf16 a[48:51], a[208:211], a[128:131], a[48:51]\n"
"ds_read_b128 a[196:199], v9 offset:51200\n"
"v_mfma_f32_16x16x32_bf16 a[48:51], a[212:215], a[132:135], a[48:51]\n"
"s_waitcnt lgkmcnt(12)\n"
"v_mfma_f32_16x16x32_bf16 a[52:55], a[216:219], a[128:131], a[52:55]\n"
"ds_read_b128 a[236:239], v7 offset:14336\n"
"v_mfma_f32_16x16x32_bf16 a[52:55], a[220:223], a[132:135], a[52:55]\n"
"ds_read_b128 a[232:235], v6 offset:14336\n"
"v_mfma_f32_16x16x32_bf16 v[220:223], a[220:223], a[124:127], v[220:223]\n"
"ds_read_b128 a[200:203], v8 offset:53248\n"
"s_add_u32 m0, s76, 2048\n"
"v_mfma_f32_16x16x32_bf16 v[220:223], a[216:219], a[120:123], v[220:223]\n"
"buffer_load_dwordx4 v2, s[40:43], s65 offen lds\n"
"v_mfma_f32_16x16x32_bf16 v[216:219], a[216:219], a[112:115], v[216:219]\n"
"ds_read_b128 a[204:207], v9 offset:53248\n"
"v_mfma_f32_16x16x32_bf16 v[216:219], a[220:223], a[116:119], v[216:219]\n"
"v_mfma_f32_16x16x32_bf16 v[212:215], a[220:223], a[108:111], v[212:215]\n"
"v_mfma_f32_16x16x32_bf16 v[212:215], a[216:219], a[104:107], v[212:215]\n"
"v_mfma_f32_16x16x32_bf16 v[208:211], a[216:219], a[96:99], v[208:211]\n"
"v_mfma_f32_16x16x32_bf16 v[208:211], a[220:223], a[100:103], v[208:211]\n"
"v_mfma_f32_16x16x32_bf16 v[204:207], a[220:223], a[92:95], v[204:207]\n"
"s_add_u32 m0, s76, 3072\n"
"v_mfma_f32_16x16x32_bf16 v[204:207], a[216:219], a[88:91], v[204:207]\n"
"buffer_load_dwordx4 v3, s[40:43], s65 offen lds\n"
"v_mfma_f32_16x16x32_bf16 v[200:203], a[216:219], a[80:83], v[200:203]\n"
"v_mfma_f32_16x16x32_bf16 v[200:203], a[220:223], a[84:87], v[200:203]\n"
"v_mfma_f32_16x16x32_bf16 v[196:199], a[220:223], a[76:79], v[196:199]\n"
"v_mfma_f32_16x16x32_bf16 v[196:199], a[216:219], a[72:75], v[196:199]\n"
"v_mfma_f32_16x16x32_bf16 v[192:195], a[216:219], a[64:67], v[192:195]\n"
"v_mfma_f32_16x16x32_bf16 v[192:195], a[220:223], a[68:71], v[192:195]\n"
"s_waitcnt vmcnt(20) lgkmcnt(0)\n"
"s_barrier\n"
"v_mfma_f32_16x16x32_bf16 v[224:227], a[224:227], a[64:67], v[224:227]\n"
"ds_read_b128 a[212:215], v7 offset:32768\n"
"v_mfma_f32_16x16x32_bf16 v[224:227], a[228:231], a[68:71], v[224:227]\n"
"ds_read_b128 a[208:211], v6 offset:32768\n"
"s_add_u32 m0, s76, 8192\n"
"v_mfma_f32_16x16x32_bf16 v[228:231], a[228:231], a[76:79], v[228:231]\n"
"buffer_load_dwordx4 v2, s[40:43], s66 offen lds\n"
"v_mfma_f32_16x16x32_bf16 v[228:231], a[224:227], a[72:75], v[228:231]\n"
"v_mfma_f32_16x16x32_bf16 v[232:235], a[224:227], a[80:83], v[232:235]\n"
"buffer_load_dword a255, v14, s[24:27], 0 offen\n"
"v_add_u32 v14, 4096, v14\n"
"v_mfma_f32_16x16x32_bf16 v[232:235], a[228:231], a[84:87], v[232:235]\n"
"v_mfma_f32_16x16x32_bf16 v[236:239], a[228:231], a[92:95], v[236:239]\n"
"v_xor_b32 v24, v24, v16\n"
"v_mfma_f32_16x16x32_bf16 v[236:239], a[224:227], a[88:91], v[236:239]\n"
"v_mfma_f32_16x16x32_bf16 v[240:243], a[224:227], a[96:99], v[240:243]\n"
"v_xor_b32 v25, v25, v17\n"
"v_mfma_f32_16x16x32_bf16 v[240:243], a[228:231], a[100:103], v[240:243]\n"
"v_mfma_f32_16x16x32_bf16 v[244:247], a[228:231], a[108:111], v[244:247]\n"
"v_xor_b32 v26, v26, v18\n"
"s_add_u32 m0, s76, 9216\n"
"v_mfma_f32_16x16x32_bf16 v[244:247], a[224:227], a[104:107], v[244:247]\n"
"v_xor_b32 v27, v27, v19\n"
"v_mfma_f32_16x16x32_bf16 v[248:251], a[224:227], a[112:115], v[248:251]\n"
"buffer_load_dwordx4 v3, s[40:43], s66 offen lds\n"
"v_mfma_f32_16x16x32_bf16 v[248:251], a[228:231], a[116:119], v[248:251]\n"
"v_or3_b32 v24, v24, v25, v26\n"
"v_mfma_f32_16x16x32_bf16 v[252:255], a[228:231], a[124:127], v[252:255]\n"
"v_mfma_f32_16x16x32_bf16 v[252:255], a[224:227], a[120:123], v[252:255]\n"
"v_or_b32 v24, v24, v27\n"
"v_mfma_f32_16x16x32_bf16 a[56:59], a[224:227], a[128:131], a[56:59]\n"
"v_cmp_ne_u32 vcc, 0, v24\n"
"v_mfma_f32_16x16x32_bf16 a[56:59], a[228:231], a[132:135], a[56:59]\n"
"v_mfma_f32_16x16x32_bf16 a[60:63], a[232:235], a[128:131], a[60:63]\n"
"ds_read_b128 a[220:223], v7 offset:34816\n"
"s_nop 3\n"
"v_mfma_f32_16x16x32_bf16 a[60:63], a[236:239], a[132:135], a[60:63]\n"
"ds_read_b128 a[216:219], v6 offset:34816\n"
"s_add_u32 m0, s76, 10240\n"
"v_mfma_f32_16x16x32_bf16 a[28:31], a[236:239], a[124:127], a[28:31]\n"
"s_cmp_eq_u64 vcc, 0\n"
"s_cselect_b32 s14, 0, 1073741824\n"
"buffer_load_dwordx4 v2, s[40:43], s67 offen lds\n"
"v_mfma_f32_16x16x32_bf16 a[28:31], a[232:235], a[120:123], a[28:31]\n"
"buffer_store_dword v28, v30, s[56:59], s14 offen sc1\n"
"v_add_u32 v2, s17, v2\n"
"v_mfma_f32_16x16x32_bf16 a[24:27], a[232:235], a[112:115], a[24:27]\n"
"v_mfma_f32_16x16x32_bf16 a[24:27], a[236:239], a[116:119], a[24:27]\n"
"buffer_load_dwordx4 v[24:27], v29, s[56:59], 0 offen sc1\n"
"v_mfma_f32_16x16x32_bf16 a[20:23], a[236:239], a[108:111], a[20:23]\n"
"v_mfma_f32_16x16x32_bf16 a[20:23], a[232:235], a[104:107], a[20:23]\n"
"v_mfma_f32_16x16x32_bf16 a[16:19], a[232:235], a[96:99], a[16:19]\n"
"v_mfma_f32_16x16x32_bf16 a[16:19], a[236:239], a[100:103], a[16:19]\n"
"v_mfma_f32_16x16x32_bf16 a[12:15], a[236:239], a[92:95], a[12:15]\n"
"s_add_u32 m0, s76, 11264\n"
"v_mfma_f32_16x16x32_bf16 a[12:15], a[232:235], a[88:91], a[12:15]\n"
"buffer_load_dwordx4 v3, s[40:43], s67 offen lds\n"
"v_mfma_f32_16x16x32_bf16 a[8:11], a[232:235], a[80:83], a[8:11]\n"
"v_add_u32 v3, s17, v3\n"
"s_add_u32 s16, s16, 1\n"
"v_mfma_f32_16x16x32_bf16 a[8:11], a[236:239], a[84:87], a[8:11]\n"
"s_cmp_eq_u32 s16, 15\n"
"s_cselect_b32 s28, -13, 1\n"
"s_lshl_b32 s17, s28, 7\n"
"v_mfma_f32_16x16x32_bf16 a[4:7], a[236:239], a[76:79], a[4:7]\n"
"s_mul_i32 s28, s28, 458752\n"
"v_mfma_f32_16x16x32_bf16 a[4:7], a[232:235], a[72:75], a[4:7]\n"
"v_mfma_f32_16x16x32_bf16 a[0:3], a[232:235], a[64:67], a[0:3]\n"
"v_mfma_f32_16x16x32_bf16 a[0:3], a[236:239], a[68:71], a[0:3]\n"
"s_waitcnt lgkmcnt(2)\n"
"v_mfma_f32_16x16x32_bf16 v[32:35], a[208:211], a[136:139], v[32:35]\n"
"ds_read_b128 a[228:231], v7 offset:36864\n"
"v_mfma_f32_16x16x32_bf16 v[32:35], a[212:215], a[140:143], v[32:35]\n"
"ds_read_b128 a[224:227], v6 offset:36864\n"
"s_add_u32 m0, s77, 102400\n"
"v_mfma_f32_16x16x32_bf16 v[36:39], a[212:215], a[148:151], v[36:39]\n"
"buffer_load_dwordx4 v4, s[52:55], 0 offen lds\n"
"v_mfma_f32_16x16x32_bf16 v[36:39], a[208:211], a[144:147], v[36:39]\n"
"v_mfma_f32_16x16x32_bf16 v[40:43], a[208:211], a[152:155], v[40:43]\n"
"v_mfma_f32_16x16x32_bf16 v[40:43], a[212:215], a[156:159], v[40:43]\n"
"v_mfma_f32_16x16x32_bf16 v[44:47], a[212:215], a[164:167], v[44:47]\n"
"v_mfma_f32_16x16x32_bf16 v[44:47], a[208:211], a[160:163], v[44:47]\n"
"v_mfma_f32_16x16x32_bf16 v[48:51], a[208:211], a[168:171], v[48:51]\n"
"s_add_u32 m0, s77, 106496\n"
"v_mfma_f32_16x16x32_bf16 v[48:51], a[212:215], a[172:175], v[48:51]\n"
"buffer_load_dwordx4 v4, s[52:55], s68 offen lds\n"
"v_mfma_f32_16x16x32_bf16 v[52:55], a[212:215], a[180:183], v[52:55]\n"
"v_mfma_f32_16x16x32_bf16 v[52:55], a[208:211], a[176:179], v[52:55]\n"
"v_mfma_f32_16x16x32_bf16 v[56:59], a[208:211], a[184:187], v[56:59]\n"
"v_mfma_f32_16x16x32_bf16 v[56:59], a[212:215], a[188:191], v[56:59]\n"
"v_mfma_f32_16x16x32_bf16 v[60:63], a[212:215], a[196:199], v[60:63]\n"
"v_mfma_f32_16x16x32_bf16 v[60:63], a[208:211], a[192:195], v[60:63]\n"
"s_add_u32 m0, s77, 110592\n"
"v_mfma_f32_16x16x32_bf16 a[32:35], a[208:211], a[200:203], a[32:35]\n"
"buffer_load_dwordx4 v4, s[52:55], s69 offen lds\n"
"v_mfma_f32_16x16x32_bf16 a[32:35], a[212:215], a[204:207], a[32:35]\n"
"s_waitcnt lgkmcnt(2)\n"
"v_mfma_f32_16x16x32_bf16 a[36:39], a[216:219], a[200:203], a[36:39]\n"
"ds_read_b128 a[236:239], v7 offset:38912\n"
"v_mfma_f32_16x16x32_bf16 a[36:39], a[220:223], a[204:207], a[36:39]\n"
"ds_read_b128 a[232:235], v6 offset:38912\n"
"v_mfma_f32_16x16x32_bf16 v[92:95], a[220:223], a[196:199], v[92:95]\n"
"v_mfma_f32_16x16x32_bf16 v[92:95], a[216:219], a[192:195], v[92:95]\n"
"v_mfma_f32_16x16x32_bf16 v[88:91], a[216:219], a[184:187], v[88:91]\n"
"s_add_u32 m0, s77, 114688\n"
"v_mfma_f32_16x16x32_bf16 v[88:91], a[220:223], a[188:191], v[88:91]\n"
"buffer_load_dwordx4 v4, s[52:55], s70 offen lds\n"
"v_mfma_f32_16x16x32_bf16 v[84:87], a[220:223], a[180:183], v[84:87]\n"
"v_mfma_f32_16x16x32_bf16 v[84:87], a[216:219], a[176:179], v[84:87]\n"
"v_mfma_f32_16x16x32_bf16 v[80:83], a[216:219], a[168:171], v[80:83]\n"
"v_mfma_f32_16x16x32_bf16 v[80:83], a[220:223], a[172:175], v[80:83]\n"
"v_mfma_f32_16x16x32_bf16 v[76:79], a[220:223], a[164:167], v[76:79]\n"
"v_mfma_f32_16x16x32_bf16 v[76:79], a[216:219], a[160:163], v[76:79]\n"
"s_add_u32 m0, s77, 118784\n"
"v_mfma_f32_16x16x32_bf16 v[72:75], a[216:219], a[152:155], v[72:75]\n"
"buffer_load_dwordx4 v4, s[52:55], s71 offen lds\n"
"v_mfma_f32_16x16x32_bf16 v[72:75], a[220:223], a[156:159], v[72:75]\n"
"v_mfma_f32_16x16x32_bf16 v[68:71], a[220:223], a[148:151], v[68:71]\n"
"v_mfma_f32_16x16x32_bf16 v[68:71], a[216:219], a[144:147], v[68:71]\n"
"v_mfma_f32_16x16x32_bf16 v[64:67], a[216:219], a[136:139], v[64:67]\n"
"v_mfma_f32_16x16x32_bf16 v[64:67], a[220:223], a[140:143], v[64:67]\n"
"s_waitcnt vmcnt(16) lgkmcnt(0)\n"
"s_barrier\n"
"v_mfma_f32_16x16x32_bf16 v[96:99], a[224:227], a[136:139], v[96:99]\n"
"ds_read_b128 a[212:215], v7 offset:40960\n"
"s_add_u32 m0, s77, 122880\n"
"v_mfma_f32_16x16x32_bf16 v[96:99], a[228:231], a[140:143], v[96:99]\n"
"ds_read_b128 a[208:211], v6 offset:40960\n"
"v_mfma_f32_16x16x32_bf16 v[100:103], a[228:231], a[148:151], v[100:103]\n"
"buffer_load_dwordx4 v4, s[52:55], s72 offen lds\n"
"v_mfma_f32_16x16x32_bf16 v[100:103], a[224:227], a[144:147], v[100:103]\n"
"ds_read_b128 a[64:67], v8 offset:0\n"
"v_mfma_f32_16x16x32_bf16 v[104:107], a[224:227], a[152:155], v[104:107]\n"
"v_mfma_f32_16x16x32_bf16 v[104:107], a[228:231], a[156:159], v[104:107]\n"
"ds_read_b128 a[68:71], v9 offset:0\n"
"v_mfma_f32_16x16x32_bf16 v[108:111], a[228:231], a[164:167], v[108:111]\n"
"v_mfma_f32_16x16x32_bf16 v[108:111], a[224:227], a[160:163], v[108:111]\n"
"s_add_u32 m0, s77, 126976\n"
"v_mfma_f32_16x16x32_bf16 v[112:115], a[224:227], a[168:171], v[112:115]\n"
"ds_read_b128 a[72:75], v8 offset:2048\n"
"v_mfma_f32_16x16x32_bf16 v[112:115], a[228:231], a[172:175], v[112:115]\n"
"buffer_load_dwordx4 v4, s[52:55], s73 offen lds\n"
"v_mfma_f32_16x16x32_bf16 v[116:119], a[228:231], a[180:183], v[116:119]\n"
"v_mfma_f32_16x16x32_bf16 v[116:119], a[224:227], a[176:179], v[116:119]\n"
"v_mfma_f32_16x16x32_bf16 v[120:123], a[224:227], a[184:187], v[120:123]\n"
"ds_read_b128 a[76:79], v9 offset:2048\n"
"v_mfma_f32_16x16x32_bf16 v[120:123], a[228:231], a[188:191], v[120:123]\n"
"v_mfma_f32_16x16x32_bf16 v[124:127], a[228:231], a[196:199], v[124:127]\n"
"s_add_u32 m0, s77, 131072\n"
"v_mfma_f32_16x16x32_bf16 v[124:127], a[224:227], a[192:195], v[124:127]\n"
"ds_read_b128 a[80:83], v8 offset:4096\n"
"v_mfma_f32_16x16x32_bf16 a[40:43], a[224:227], a[200:203], a[40:43]\n"
"buffer_load_dwordx4 v4, s[52:55], s74 offen lds\n"
"v_mfma_f32_16x16x32_bf16 a[40:43], a[228:231], a[204:207], a[40:43]\n"
"v_mfma_f32_16x16x32_bf16 a[44:47], a[232:235], a[200:203], a[44:47]\n"
"ds_read_b128 a[220:223], v7 offset:43008\n"
"v_mfma_f32_16x16x32_bf16 a[44:47], a[236:239], a[204:207], a[44:47]\n"
"ds_read_b128 a[84:87], v9 offset:4096\n"
"v_mfma_f32_16x16x32_bf16 v[156:159], a[236:239], a[196:199], v[156:159]\n"
"ds_read_b128 a[216:219], v6 offset:43008\n"
"v_mfma_f32_16x16x32_bf16 v[156:159], a[232:235], a[192:195], v[156:159]\n"
"s_add_u32 m0, s77, 135168\n"
"v_mfma_f32_16x16x32_bf16 v[152:155], a[232:235], a[184:187], v[152:155]\n"
"ds_read_b128 a[88:91], v8 offset:6144\n"
"v_mfma_f32_16x16x32_bf16 v[152:155], a[236:239], a[188:191], v[152:155]\n"
"buffer_load_dwordx4 v4, s[52:55], s75 offen lds\n"
"v_add_u32 v4, s17, v4\n"
"v_mfma_f32_16x16x32_bf16 v[148:151], a[236:239], a[180:183], v[148:151]\n"
"v_mfma_f32_16x16x32_bf16 v[148:151], a[232:235], a[176:179], v[148:151]\n"
"ds_read_b128 a[92:95], v9 offset:6144\n"
"v_mfma_f32_16x16x32_bf16 v[144:147], a[232:235], a[168:171], v[144:147]\n"
"v_mfma_f32_16x16x32_bf16 v[144:147], a[236:239], a[172:175], v[144:147]\n"
"v_mfma_f32_16x16x32_bf16 v[140:143], a[236:239], a[164:167], v[140:143]\n"
"ds_read_b128 a[96:99], v8 offset:8192\n"
"v_mfma_f32_16x16x32_bf16 v[140:143], a[232:235], a[160:163], v[140:143]\n"
"v_mfma_f32_16x16x32_bf16 v[136:139], a[232:235], a[152:155], v[136:139]\n"
"v_mfma_f32_16x16x32_bf16 v[136:139], a[236:239], a[156:159], v[136:139]\n"
"v_mfma_f32_16x16x32_bf16 v[132:135], a[236:239], a[148:151], v[132:135]\n"
"ds_read_b128 a[100:103], v9 offset:8192\n"
"v_mfma_f32_16x16x32_bf16 v[132:135], a[232:235], a[144:147], v[132:135]\n"
"v_mfma_f32_16x16x32_bf16 v[128:131], a[232:235], a[136:139], v[128:131]\n"
"v_mfma_f32_16x16x32_bf16 v[128:131], a[236:239], a[140:143], v[128:131]\n"
"ds_read_b128 a[104:107], v8 offset:10240\n"
"s_waitcnt lgkmcnt(13)\n"
"v_mfma_f32_16x16x32_bf16 v[160:163], a[208:211], a[136:139], v[160:163]\n"
"ds_read_b128 a[228:231], v7 offset:45056\n"
"v_mfma_f32_16x16x32_bf16 v[160:163], a[212:215], a[140:143], v[160:163]\n"
"ds_read_b128 a[224:227], v6 offset:45056\n"
"s_add_u32 m0, s76, 32768\n"
"v_mfma_f32_16x16x32_bf16 v[164:167], a[212:215], a[148:151], v[164:167]\n"
"ds_read_b128 a[108:111], v9 offset:10240\n"
"v_mfma_f32_16x16x32_bf16 v[164:167], a[208:211], a[144:147], v[164:167]\n"
"buffer_load_dwordx4 v2, s[40:43], s64 offen lds\n"
"v_mfma_f32_16x16x32_bf16 v[168:171], a[208:211], a[152:155], v[168:171]\n"
"v_mfma_f32_16x16x32_bf16 v[168:171], a[212:215], a[156:159], v[168:171]\n"
"v_mfma_f32_16x16x32_bf16 v[172:175], a[212:215], a[164:167], v[172:175]\n"
"ds_read_b128 a[112:115], v8 offset:12288\n"
"v_mfma_f32_16x16x32_bf16 v[172:175], a[208:211], a[160:163], v[172:175]\n"
"v_mfma_f32_16x16x32_bf16 v[176:179], a[208:211], a[168:171], v[176:179]\n"
"v_mfma_f32_16x16x32_bf16 v[176:179], a[212:215], a[172:175], v[176:179]\n"
"ds_read_b128 a[116:119], v9 offset:12288\n"
"v_mfma_f32_16x16x32_bf16 v[180:183], a[212:215], a[180:183], v[180:183]\n"
"s_add_u32 m0, s76, 33792\n"
"v_mfma_f32_16x16x32_bf16 v[180:183], a[208:211], a[176:179], v[180:183]\n"
"buffer_load_dwordx4 v3, s[40:43], s64 offen lds\n"
"v_mfma_f32_16x16x32_bf16 v[184:187], a[208:211], a[184:187], v[184:187]\n"
"ds_read_b128 a[120:123], v8 offset:14336\n"
"v_mfma_f32_16x16x32_bf16 v[184:187], a[212:215], a[188:191], v[184:187]\n"
"v_mfma_f32_16x16x32_bf16 v[188:191], a[212:215], a[196:199], v[188:191]\n"
"v_mfma_f32_16x16x32_bf16 v[188:191], a[208:211], a[192:195], v[188:191]\n"
"v_mfma_f32_16x16x32_bf16 a[48:51], a[208:211], a[200:203], a[48:51]\n"
"ds_read_b128 a[124:127], v9 offset:14336\n"
"v_mfma_f32_16x16x32_bf16 a[48:51], a[212:215], a[204:207], a[48:51]\n"
"s_waitcnt lgkmcnt(12)\n"
"v_mfma_f32_16x16x32_bf16 a[52:55], a[216:219], a[200:203], a[52:55]\n"
"ds_read_b128 a[236:239], v7 offset:47104\n"
"v_mfma_f32_16x16x32_bf16 a[52:55], a[220:223], a[204:207], a[52:55]\n"
"ds_read_b128 a[232:235], v6 offset:47104\n"
"v_mfma_f32_16x16x32_bf16 v[220:223], a[220:223], a[196:199], v[220:223]\n"
"ds_read_b128 a[128:131], v8 offset:16384\n"
"s_add_u32 m0, s76, 34816\n"
"v_mfma_f32_16x16x32_bf16 v[220:223], a[216:219], a[192:195], v[220:223]\n"
"buffer_load_dwordx4 v2, s[40:43], s65 offen lds\n"
"v_mfma_f32_16x16x32_bf16 v[216:219], a[216:219], a[184:187], v[216:219]\n"
"ds_read_b128 a[132:135], v9 offset:16384\n"
"v_mfma_f32_16x16x32_bf16 v[216:219], a[220:223], a[188:191], v[216:219]\n"
"v_mfma_f32_16x16x32_bf16 v[212:215], a[220:223], a[180:183], v[212:215]\n"
"v_mfma_f32_16x16x32_bf16 v[212:215], a[216:219], a[176:179], v[212:215]\n"
"v_mfma_f32_16x16x32_bf16 v[208:211], a[216:219], a[168:171], v[208:211]\n"
"v_mfma_f32_16x16x32_bf16 v[208:211], a[220:223], a[172:175], v[208:211]\n"
"v_mfma_f32_16x16x32_bf16 v[204:207], a[220:223], a[164:167], v[204:207]\n"
"s_add_u32 m0, s76, 35840\n"
"v_mfma_f32_16x16x32_bf16 v[204:207], a[216:219], a[160:163], v[204:207]\n"
"buffer_load_dwordx4 v3, s[40:43], s65 offen lds\n"
"v_mfma_f32_16x16x32_bf16 v[200:203], a[216:219], a[152:155], v[200:203]\n"
"v_mfma_f32_16x16x32_bf16 v[200:203], a[220:223], a[156:159], v[200:203]\n"
"v_mfma_f32_16x16x32_bf16 v[196:199], a[220:223], a[148:151], v[196:199]\n"
"v_mfma_f32_16x16x32_bf16 v[196:199], a[216:219], a[144:147], v[196:199]\n"
"v_mfma_f32_16x16x32_bf16 v[192:195], a[216:219], a[136:139], v[192:195]\n"
"v_mfma_f32_16x16x32_bf16 v[192:195], a[220:223], a[140:143], v[192:195]\n"
"s_waitcnt vmcnt(20) lgkmcnt(0)\n"
"s_barrier\n"
"v_mfma_f32_16x16x32_bf16 v[224:227], a[224:227], a[136:139], v[224:227]\n"
"ds_read_b128 a[212:215], v7 offset:0\n"
"v_mfma_f32_16x16x32_bf16 v[224:227], a[228:231], a[140:143], v[224:227]\n"
"ds_read_b128 a[208:211], v6 offset:0\n"
"s_add_u32 m0, s76, 40960\n"
"v_mfma_f32_16x16x32_bf16 v[228:231], a[228:231], a[148:151], v[228:231]\n"
"buffer_load_dwordx4 v2, s[40:43], s66 offen lds\n"
"v_mfma_f32_16x16x32_bf16 v[228:231], a[224:227], a[144:147], v[228:231]\n"
"v_mfma_f32_16x16x32_bf16 v[232:235], a[224:227], a[152:155], v[232:235]\n"
"buffer_load_dword a255, v15, s[60:63], 0 offen\n"
"v_add_u32 v15, 2048, v15\n"
"v_mfma_f32_16x16x32_bf16 v[232:235], a[228:231], a[156:159], v[232:235]\n"
"v_mfma_f32_16x16x32_bf16 v[236:239], a[228:231], a[164:167], v[236:239]\n"
"v_mfma_f32_16x16x32_bf16 v[236:239], a[224:227], a[160:163], v[236:239]\n"
"v_mfma_f32_16x16x32_bf16 v[240:243], a[224:227], a[168:171], v[240:243]\n"
"v_mfma_f32_16x16x32_bf16 v[240:243], a[228:231], a[172:175], v[240:243]\n"
"v_mfma_f32_16x16x32_bf16 v[244:247], a[228:231], a[180:183], v[244:247]\n"
"s_add_u32 m0, s76, 41984\n"
"v_mfma_f32_16x16x32_bf16 v[244:247], a[224:227], a[176:179], v[244:247]\n"
"buffer_store_dword v23, v22, s[56:59], 0 offen sc1\n"
"v_mfma_f32_16x16x32_bf16 v[248:251], a[224:227], a[184:187], v[248:251]\n"
"buffer_load_dwordx4 v3, s[40:43], s66 offen lds\n"
"v_mfma_f32_16x16x32_bf16 v[248:251], a[228:231], a[188:191], v[248:251]\n"
"v_mfma_f32_16x16x32_bf16 v[252:255], a[228:231], a[196:199], v[252:255]\n"
"v_mfma_f32_16x16x32_bf16 v[252:255], a[224:227], a[192:195], v[252:255]\n"
"v_mfma_f32_16x16x32_bf16 a[56:59], a[224:227], a[200:203], a[56:59]\n"
"v_mfma_f32_16x16x32_bf16 a[56:59], a[228:231], a[204:207], a[56:59]\n"
"v_mfma_f32_16x16x32_bf16 a[60:63], a[232:235], a[200:203], a[60:63]\n"
"ds_read_b128 a[220:223], v7 offset:2048\n"
"v_mfma_f32_16x16x32_bf16 a[60:63], a[236:239], a[204:207], a[60:63]\n"
"buffer_load_dword v23, v21, s[56:59], 0 offen sc1\n"
"v_mfma_f32_16x16x32_bf16 a[28:31], a[236:239], a[196:199], a[28:31]\n"
"ds_read_b128 a[216:219], v6 offset:2048\n"
"s_add_u32 m0, s76, 43008\n"
"v_mfma_f32_16x16x32_bf16 a[28:31], a[232:235], a[192:195], a[28:31]\n"
"buffer_load_dwordx4 v2, s[40:43], s67 offen lds\n"
"v_add_u32 v2, s17, v2\n"
"v_mfma_f32_16x16x32_bf16 a[24:27], a[232:235], a[184:187], a[24:27]\n"
"v_mfma_f32_16x16x32_bf16 a[24:27], a[236:239], a[188:191], a[24:27]\n"
"v_mfma_f32_16x16x32_bf16 a[20:23], a[236:239], a[180:183], a[20:23]\n"
"v_mfma_f32_16x16x32_bf16 a[20:23], a[232:235], a[176:179], a[20:23]\n"
"v_mfma_f32_16x16x32_bf16 a[16:19], a[232:235], a[168:171], a[16:19]\n"
"v_mfma_f32_16x16x32_bf16 a[16:19], a[236:239], a[172:175], a[16:19]\n"
"v_mfma_f32_16x16x32_bf16 a[12:15], a[236:239], a[164:167], a[12:15]\n"
"s_add_u32 m0, s76, 44032\n"
"v_mfma_f32_16x16x32_bf16 a[12:15], a[232:235], a[160:163], a[12:15]\n"
"buffer_load_dwordx4 v3, s[40:43], s67 offen lds\n"
"v_mfma_f32_16x16x32_bf16 a[8:11], a[232:235], a[152:155], a[8:11]\n"
"v_add_u32 v3, s17, v3\n"
"s_add_u32 s16, s16, 1\n"
"v_mfma_f32_16x16x32_bf16 a[8:11], a[236:239], a[156:159], a[8:11]\n"
"s_cmp_eq_u32 s16, 15\n"
"s_cselect_b32 s28, -13, 1\n"
"s_lshl_b32 s17, s28, 7\n"
"v_mfma_f32_16x16x32_bf16 a[4:7], a[236:239], a[148:151], a[4:7]\n"
"s_mul_i32 s28, s28, 458752\n"
"v_mfma_f32_16x16x32_bf16 a[4:7], a[232:235], a[144:147], a[4:7]\n"
"s_sub_u32 s78, s78, 1\n"
"v_mfma_f32_16x16x32_bf16 a[0:3], a[232:235], a[136:139], a[0:3]\n"
"v_mfma_f32_16x16x32_bf16 a[0:3], a[236:239], a[140:143], a[0:3]\n"
"s_cbranch_scc0 T2loop_%=\n"
"s_waitcnt lgkmcnt(2)\n"
"v_mfma_f32_16x16x32_bf16 v[32:35], a[208:211], a[64:67], v[32:35]\n"
"ds_read_b128 a[228:231], v7 offset:4096\n"
"v_mfma_f32_16x16x32_bf16 v[32:35], a[212:215], a[68:71], v[32:35]\n"
"ds_read_b128 a[224:227], v6 offset:4096\n"
"v_mfma_f32_16x16x32_bf16 v[36:39], a[212:215], a[76:79], v[36:39]\n"
"v_mfma_f32_16x16x32_bf16 v[36:39], a[208:211], a[72:75], v[36:39]\n"
"v_mfma_f32_16x16x32_bf16 v[40:43], a[208:211], a[80:83], v[40:43]\n"
"v_mfma_f32_16x16x32_bf16 v[40:43], a[212:215], a[84:87], v[40:43]\n"
"v_mfma_f32_16x16x32_bf16 v[44:47], a[212:215], a[92:95], v[44:47]\n"
"v_mfma_f32_16x16x32_bf16 v[44:47], a[208:211], a[88:91], v[44:47]\n"
"v_mfma_f32_16x16x32_bf16 v[48:51], a[208:211], a[96:99], v[48:51]\n"
"v_mfma_f32_16x16x32_bf16 v[48:51], a[212:215], a[100:103], v[48:51]\n"
"v_mfma_f32_16x16x32_bf16 v[52:55], a[212:215], a[108:111], v[52:55]\n"
"v_mfma_f32_16x16x32_bf16 v[52:55], a[208:211], a[104:107], v[52:55]\n"
"v_mfma_f32_16x16x32_bf16 v[56:59], a[208:211], a[112:115], v[56:59]\n"
"v_mfma_f32_16x16x32_bf16 v[56:59], a[212:215], a[116:119], v[56:59]\n"
"v_mfma_f32_16x16x32_bf16 v[60:63], a[212:215], a[124:127], v[60:63]\n"
"v_mfma_f32_16x16x32_bf16 v[60:63], a[208:211], a[120:123], v[60:63]\n"
"v_mfma_f32_16x16x32_bf16 a[32:35], a[208:211], a[128:131], a[32:35]\n"
"v_mfma_f32_16x16x32_bf16 a[32:35], a[212:215], a[132:135], a[32:35]\n"
"s_waitcnt lgkmcnt(2)\n"
"v_mfma_f32_16x16x32_bf16 a[36:39], a[216:219], a[128:131], a[36:39]\n"
"ds_read_b128 a[236:239], v7 offset:6144\n"
"v_mfma_f32_16x16x32_bf16 a[36:39], a[220:223], a[132:135], a[36:39]\n"
"ds_read_b128 a[232:235], v6 offset:6144\n"
"v_mfma_f32_16x16x32_bf16 v[92:95], a[220:223], a[124:127], v[92:95]\n"
"v_mfma_f32_16x16x32_bf16 v[92:95], a[216:219], a[120:123], v[92:95]\n"
"v_mfma_f32_16x16x32_bf16 v[88:91], a[216:219], a[112:115], v[88:91]\n"
"v_mfma_f32_16x16x32_bf16 v[88:91], a[220:223], a[116:119], v[88:91]\n"
"v_mfma_f32_16x16x32_bf16 v[84:87], a[220:223], a[108:111], v[84:87]\n"
"v_mfma_f32_16x16x32_bf16 v[84:87], a[216:219], a[104:107], v[84:87]\n"
"v_mfma_f32_16x16x32_bf16 v[80:83], a[216:219], a[96:99], v[80:83]\n"
"v_mfma_f32_16x16x32_bf16 v[80:83], a[220:223], a[100:103], v[80:83]\n"
"v_mfma_f32_16x16x32_bf16 v[76:79], a[220:223], a[92:95], v[76:79]\n"
"v_mfma_f32_16x16x32_bf16 v[76:79], a[216:219], a[88:91], v[76:79]\n"
"v_mfma_f32_16x16x32_bf16 v[72:75], a[216:219], a[80:83], v[72:75]\n"
"v_mfma_f32_16x16x32_bf16 v[72:75], a[220:223], a[84:87], v[72:75]\n"
"v_mfma_f32_16x16x32_bf16 v[68:71], a[220:223], a[76:79], v[68:71]\n"
"v_mfma_f32_16x16x32_bf16 v[68:71], a[216:219], a[72:75], v[68:71]\n"
"v_mfma_f32_16x16x32_bf16 v[64:67], a[216:219], a[64:67], v[64:67]\n"
"v_mfma_f32_16x16x32_bf16 v[64:67], a[220:223], a[68:71], v[64:67]\n"
"s_waitcnt vmcnt(11) lgkmcnt(0)\n"
"s_barrier\n"
"v_mfma_f32_16x16x32_bf16 v[96:99], a[224:227], a[64:67], v[96:99]\n"
"ds_read_b128 a[212:215], v7 offset:8192\n"
"v_mfma_f32_16x16x32_bf16 v[96:99], a[228:231], a[68:71], v[96:99]\n"
"ds_read_b128 a[208:211], v6 offset:8192\n"
"v_mfma_f32_16x16x32_bf16 v[100:103], a[228:231], a[76:79], v[100:103]\n"
"ds_read_b128 a[136:139], v8 offset:36864\n"
"v_mfma_f32_16x16x32_bf16 v[100:103], a[224:227], a[72:75], v[100:103]\n"
"v_mfma_f32_16x16x32_bf16 v[104:107], a[224:227], a[80:83], v[104:107]\n"
"v_mfma_f32_16x16x32_bf16 v[104:107], a[228:231], a[84:87], v[104:107]\n"
"ds_read_b128 a[140:143], v9 offset:36864\n"
"v_mfma_f32_16x16x32_bf16 v[108:111], a[228:231], a[92:95], v[108:111]\n"
"v_mfma_f32_16x16x32_bf16 v[108:111], a[224:227], a[88:91], v[108:111]\n"
"v_mfma_f32_16x16x32_bf16 v[112:115], a[224:227], a[96:99], v[112:115]\n"
"ds_read_b128 a[144:147], v8 offset:38912\n"
"v_mfma_f32_16x16x32_bf16 v[112:115], a[228:231], a[100:103], v[112:115]\n"
"v_mfma_f32_16x16x32_bf16 v[116:119], a[228:231], a[108:111], v[116:119]\n"
"v_mfma_f32_16x16x32_bf16 v[116:119], a[224:227], a[104:107], v[116:119]\n"
"v_mfma_f32_16x16x32_bf16 v[120:123], a[224:227], a[112:115], v[120:123]\n"
"ds_read_b128 a[148:151], v9 offset:38912\n"
"v_mfma_f32_16x16x32_bf16 v[120:123], a[228:231], a[116:119], v[120:123]\n"
"v_mfma_f32_16x16x32_bf16 v[124:127], a[228:231], a[124:127], v[124:127]\n"
"v_mfma_f32_16x16x32_bf16 v[124:127], a[224:227], a[120:123], v[124:127]\n"
"ds_read_b128 a[152:155], v8 offset:40960\n"
"v_mfma_f32_16x16x32_bf16 a[40:43], a[224:227], a[128:131], a[40:43]\n"
"v_mfma_f32_16x16x32_bf16 a[40:43], a[228:231], a[132:135], a[40:43]\n"
"v_mfma_f32_16x16x32_bf16 a[44:47], a[232:235], a[128:131], a[44:47]\n"
"ds_read_b128 a[220:223], v7 offset:10240\n"
"v_mfma_f32_16x16x32_bf16 a[44:47], a[236:239], a[132:135], a[44:47]\n"
"ds_read_b128 a[156:159], v9 offset:40960\n"
"v_mfma_f32_16x16x32_bf16 v[156:159], a[236:239], a[124:127], v[156:159]\n"
"ds_read_b128 a[216:219], v6 offset:10240\n"
"v_mfma_f32_16x16x32_bf16 v[156:159], a[232:235], a[120:123], v[156:159]\n"
"v_mfma_f32_16x16x32_bf16 v[152:155], a[232:235], a[112:115], v[152:155]\n"
"ds_read_b128 a[160:163], v8 offset:43008\n"
"v_mfma_f32_16x16x32_bf16 v[152:155], a[236:239], a[116:119], v[152:155]\n"
"v_mfma_f32_16x16x32_bf16 v[148:151], a[236:239], a[108:111], v[148:151]\n"
"v_mfma_f32_16x16x32_bf16 v[148:151], a[232:235], a[104:107], v[148:151]\n"
"ds_read_b128 a[164:167], v9 offset:43008\n"
"v_mfma_f32_16x16x32_bf16 v[144:147], a[232:235], a[96:99], v[144:147]\n"
"v_mfma_f32_16x16x32_bf16 v[144:147], a[236:239], a[100:103], v[144:147]\n"
"v_mfma_f32_16x16x32_bf16 v[140:143], a[236:239], a[92:95], v[140:143]\n"
"ds_read_b128 a[168:171], v8 offset:45056\n"
"v_mfma_f32_16x16x32_bf16 v[140:143], a[232:235], a[88:91], v[140:143]\n"
"v_mfma_f32_16x16x32_bf16 v[136:139], a[232:235], a[80:83], v[136:139]\n"
"v_mfma_f32_16x16x32_bf16 v[136:139], a[236:239], a[84:87], v[136:139]\n"
"v_mfma_f32_16x16x32_bf16 v[132:135], a[236:239], a[76:79], v[132:135]\n"
"ds_read_b128 a[172:175], v9 offset:45056\n"
"v_mfma_f32_16x16x32_bf16 v[132:135], a[232:235], a[72:75], v[132:135]\n"
"v_mfma_f32_16x16x32_bf16 v[128:131], a[232:235], a[64:67], v[128:131]\n"
"v_mfma_f32_16x16x32_bf16 v[128:131], a[236:239], a[68:71], v[128:131]\n"
"ds_read_b128 a[176:179], v8 offset:47104\n"
"s_waitcnt lgkmcnt(13)\n"
"v_mfma_f32_16x16x32_bf16 v[160:163], a[208:211], a[64:67], v[160:163]\n"
"ds_read_b128 a[228:231], v7 offset:12288\n"
"v_mfma_f32_16x16x32_bf16 v[160:163], a[212:215], a[68:71], v[160:163]\n"
"ds_read_b128 a[224:227], v6 offset:12288\n"
"v_mfma_f32_16x16x32_bf16 v[164:167], a[212:215], a[76:79], v[164:167]\n"
"ds_read_b128 a[180:183], v9 offset:47104\n"
"v_mfma_f32_16x16x32_bf16 v[164:167], a[208:211], a[72:75], v[164:167]\n"
"v_mfma_f32_16x16x32_bf16 v[168:171], a[208:211], a[80:83], v[168:171]\n"
"v_mfma_f32_16x16x32_bf16 v[168:171], a[212:215], a[84:87], v[168:171]\n"
"v_mfma_f32_16x16x32_bf16 v[172:175], a[212:215], a[92:95], v[172:175]\n"
"ds_read_b128 a[184:187], v8 offset:49152\n"
"v_mfma_f32_16x16x32_bf16 v[172:175], a[208:211], a[88:91], v[172:175]\n"
"v_mfma_f32_16x16x32_bf16 v[176:179], a[208:211], a[96:99], v[176:179]\n"
"v_mfma_f32_16x16x32_bf16 v[176:179], a[212:215], a[100:103], v[176:179]\n"
"ds_read_b128 a[188:191], v9 offset:49152\n"
"v_mfma_f32_16x16x32_bf16 v[180:183], a[212:215], a[108:111], v[180:183]\n"
"v_mfma_f32_16x16x32_bf16 v[180:183], a[208:211], a[104:107], v[180:183]\n"
"v_mfma_f32_16x16x32_bf16 v[184:187], a[208:211], a[112:115], v[184:187]\n"
"ds_read_b128 a[192:195], v8 offset:51200\n"
"v_mfma_f32_16x16x32_bf16 v[184:187], a[212:215], a[116:119], v[184:187]\n"
"v_mfma_f32_16x16x32_bf16 v[188:191], a[212:215], a[124:127], v[188:191]\n"
"v_mfma_f32_16x16x32_bf16 v[188:191], a[208:211], a[120:123], v[188:191]\n"
"v_mfma_f32_16x16x32_bf16 a[48:51], a[208:211], a[128:131], a[48:51]\n"
"ds_read_b128 a[196:199], v9 offset:51200\n"
"v_mfma_f32_16x16x32_bf16 a[48:51], a[212:215], a[132:135], a[48:51]\n"
"s_waitcnt vmcnt(2)\n"
"s_waitcnt lgkmcnt(12)\n"
"v_mfma_f32_16x16x32_bf16 a[52:55], a[216:219], a[128:131], a[52:55]\n"
"ds_read_b128 a[236:239], v7 offset:14336\n"
"v_readlane_b32 s14, v23, 1\n"
"v_mfma_f32_16x16x32_bf16 a[52:55], a[220:223], a[132:135], a[52:55]\n"
"ds_read_b128 a[232:235], v6 offset:14336\n"
"v_mfma_f32_16x16x32_bf16 v[220:223], a[220:223], a[124:127], v[220:223]\n"
"ds_read_b128 a[200:203], v8 offset:53248\n"
"s_nop 3\n"
"v_mfma_f32_16x16x32_bf16 v[220:223], a[216:219], a[120:123], v[220:223]\n"
"s_cmp_lg_u32 s14, s7\n"
"s_cselect_b32 s6, 2, s6\n"
"v_mfma_f32_16x16x32_bf16 v[216:219], a[216:219], a[112:115], v[216:219]\n"
"ds_read_b128 a[204:207], v9 offset:53248\n"
"v_mfma_f32_16x16x32_bf16 v[216:219], a[220:223], a[116:119], v[216:219]\n"
"v_mfma_f32_16x16x32_bf16 v[212:215], a[220:223], a[108:111], v[212:215]\n"
"v_mfma_f32_16x16x32_bf16 v[212:215], a[216:219], a[104:107], v[212:215]\n"
"s_cmp_lt_u32 s6, 2\n"
"s_cbranch_scc1 EV4_%=\n"
"EV4r_%=:\n"
"v_mfma_f32_16x16x32_bf16 v[208:211], a[216:219], a[96:99], v[208:211]\n"
"v_mfma_f32_16x16x32_bf16 v[208:211], a[220:223], a[100:103], v[208:211]\n"
"s_lshr_b32 s69, s83, 1\n"
"s_xor_b32 s69, s69, s13\n"
"v_mfma_f32_16x16x32_bf16 v[204:207], a[220:223], a[92:95], v[204:207]\n"
"s_and_b32 s69, s69, 1\n"
"v_mfma_f32_16x16x32_bf16 v[204:207], a[216:219], a[88:91], v[204:207]\n"
"s_and_b32 s15, s13, 2\n"
"v_mfma_f32_16x16x32_bf16 v[200:203], a[216:219], a[80:83], v[200:203]\n"
"s_add_u32 s69, s69, s15\n"
"v_mfma_f32_16x16x32_bf16 v[200:203], a[220:223], a[84:87], v[200:203]\n"
"v_lshrrev_b32 v5, 4, v1\n"
"v_mfma_f32_16x16x32_bf16 v[196:199], a[220:223], a[76:79], v[196:199]\n"
"v_and_b32 v2, 7, v1\n"
"v_mfma_f32_16x16x32_bf16 v[196:199], a[216:219], a[72:75], v[196:199]\n"
"v_xor_b32 v2, v2, v5\n"
"v_mfma_f32_16x16x32_bf16 v[192:195], a[216:219], a[64:67], v[192:195]\n"
"v_lshlrev_b32 v2, 4, v2\n"
"v_mfma_f32_16x16x32_bf16 v[192:195], a[220:223], a[68:71], v[192:195]\n"
"s_waitcnt vmcnt(0) lgkmcnt(0)\n"
"s_barrier\n"
"v_lshrrev_b32 v5, 5, v1\n"
"v_mfma_f32_16x16x32_bf16 v[224:227], a[224:227], a[64:67], v[224:227]\n"
"ds_read_b128 a[212:215], v7 offset:32768\n"
"v_bfe_u32 v15, v1, 3, 2\n"
"v_mfma_f32_16x16x32_bf16 v[224:227], a[228:231], a[68:71], v[224:227]\n"
"ds_read_b128 a[208:211], v6 offset:32768\n"
"v_lshl_add_u32 v15, v5, 3, v15\n"
"v_mfma_f32_16x16x32_bf16 v[228:231], a[228:231], a[76:79], v[228:231]\n"
"s_lshl_b32 s14, s4, 8\n"
"v_mfma_f32_16x16x32_bf16 v[228:231], a[224:227], a[72:75], v[228:231]\n"
"s_lshl_b32 s15, s69, 6\n"
"v_mfma_f32_16x16x32_bf16 v[232:235], a[224:227], a[80:83], v[232:235]\n"
"buffer_load_dword a255, v14, s[24:27], 0 offen\n"
"v_add_u32 v14, 4096, v14\n"
"v_mfma_f32_16x16x32_bf16 v[232:235], a[228:231], a[84:87], v[232:235]\n"
"s_add_u32 s14, s14, s15\n"
"v_add_u32 v15, s14, v15\n"
"v_lshl_add_u32 v2, v15, 12, v2\n"
"v_mfma_f32_16x16x32_bf16 v[236:239], a[228:231], a[92:95], v[236:239]\n"
"s_and_b32 s14, s83, 1\n"
"v_mfma_f32_16x16x32_bf16 v[236:239], a[224:227], a[88:91], v[236:239]\n"
"s_lshl_b32 s15, s14, 11\n"
"v_mfma_f32_16x16x32_bf16 v[240:243], a[224:227], a[96:99], v[240:243]\n"
"v_add_u32 v2, s15, v2\n"
"v_mfma_f32_16x16x32_bf16 v[240:243], a[228:231], a[100:103], v[240:243]\n"
"v_xor_b32 v3, 64, v2\n"
"v_mfma_f32_16x16x32_bf16 v[244:247], a[228:231], a[108:111], v[244:247]\n"
"v_add_u32 v3, 64512, v3\n"
"v_mfma_f32_16x16x32_bf16 v[244:247], a[224:227], a[104:107], v[244:247]\n"
"s_lshl_b32 s64, s14, 17\n"
"v_mfma_f32_16x16x32_bf16 v[248:251], a[224:227], a[112:115], v[248:251]\n"
"s_add_u32 s65, s64, 14336\n"
"v_mfma_f32_16x16x32_bf16 v[248:251], a[228:231], a[116:119], v[248:251]\n"
"s_xor_b32 s66, s64, 131072\n"
"v_mfma_f32_16x16x32_bf16 v[252:255], a[228:231], a[124:127], v[252:255]\n"
"s_add_u32 s67, s66, 14336\n"
"v_mfma_f32_16x16x32_bf16 v[252:255], a[224:227], a[120:123], v[252:255]\n"
"s_lshl_b32 s10, s14, 12\n"
"v_mfma_f32_16x16x32_bf16 a[56:59], a[224:227], a[128:131], a[56:59]\n"
"s_xor_b32 s11, s10, 4096\n"
"v_mfma_f32_16x16x32_bf16 a[56:59], a[228:231], a[132:135], a[56:59]\n"
"s_mov_b32 s68, 14336\n"
"s_lshl_b32 s76, s13, 13\n"
"v_mfma_f32_16x16x32_bf16 a[60:63], a[232:235], a[128:131], a[60:63]\n"
"ds_read_b128 a[220:223], v7 offset:34816\n"
"s_lshl_b32 s77, s13, 12\n"
"v_mfma_f32_16x16x32_bf16 a[60:63], a[236:239], a[132:135], a[60:63]\n"
"ds_read_b128 a[216:219], v6 offset:34816\n"
"s_lshl_b32 s14, s83, 21\n"
"v_mfma_f32_16x16x32_bf16 a[28:31], a[236:239], a[124:127], a[28:31]\n"
"s_lshl_b32 s15, s5, 14\n"
"v_mfma_f32_16x16x32_bf16 a[28:31], a[232:235], a[120:123], a[28:31]\n"
"s_add_u32 s14, s14, s15\n"
"v_mfma_f32_16x16x32_bf16 a[24:27], a[232:235], a[112:115], a[24:27]\n"
"s_add_u32 s14, s14, s77\n"
"v_mfma_f32_16x16x32_bf16 a[24:27], a[236:239], a[116:119], a[24:27]\n"
"s_add_u32 s14, s14, 15073280\n"
"v_mfma_f32_16x16x32_bf16 a[20:23], a[236:239], a[108:111], a[20:23]\n"
"v_lshlrev_b32 v5, 4, v1\n"
"v_mfma_f32_16x16x32_bf16 a[20:23], a[232:235], a[104:107], a[20:23]\n"
"v_add_u32 v4, s14, v5\n"
"v_mfma_f32_16x16x32_bf16 a[16:19], a[232:235], a[96:99], a[16:19]\n"
"s_add_u32 s77, s77, 32768\n"
"v_mfma_f32_16x16x32_bf16 a[16:19], a[236:239], a[100:103], a[16:19]\n"
"s_mov_b32 s7, 0\n"
"v_mfma_f32_16x16x32_bf16 a[12:15], a[236:239], a[92:95], a[12:15]\n"
"s_mov_b32 s8, 0\n"
"v_mfma_f32_16x16x32_bf16 a[12:15], a[232:235], a[88:91], a[12:15]\n"
"s_mov_b32 s9, 49152\n"
"v_mfma_f32_16x16x32_bf16 a[8:11], a[232:235], a[80:83], a[8:11]\n"
"s_mov_b32 s6, s13\n"
"v_mfma_f32_16x16x32_bf16 a[8:11], a[236:239], a[84:87], a[8:11]\n"
"v_mfma_f32_16x16x32_bf16 a[4:7], a[236:239], a[76:79], a[4:7]\n"
"v_mfma_f32_16x16x32_bf16 a[4:7], a[232:235], a[72:75], a[4:7]\n"
"v_mfma_f32_16x16x32_bf16 a[0:3], a[232:235], a[64:67], a[0:3]\n"
"v_mfma_f32_16x16x32_bf16 a[0:3], a[236:239], a[68:71], a[0:3]\n"
"ds_read_b128 a[84:87], v7 offset:36864\n"
"s_waitcnt lgkmcnt(3)\n"
"v_mfma_f32_16x16x32_bf16 v[32:35], a[208:211], a[136:139], v[32:35]\n"
"ds_read_b128 a[80:83], v6 offset:36864\n"
"v_mfma_f32_16x16x32_bf16 v[32:35], a[212:215], a[140:143], v[32:35]\n"
"ds_read_b128 a[92:95], v7 offset:38912\n"
"s_waitcnt lgkmcnt(4)\n"
"v_mfma_f32_16x16x32_bf16 v[64:67], a[220:223], a[140:143], v[64:67]\n"
"ds_read_b128 a[88:91], v6 offset:38912\n"
"s_waitcnt lgkmcnt(4)\n"
"v_mfma_f32_16x16x32_bf16 v[64:67], a[216:219], a[136:139], v[64:67]\n"
"ds_read_b128 a[100:103], v7 offset:40960\n"
"v_mfma_f32_16x16x32_bf16 v[36:39], a[208:211], a[144:147], v[36:39]\n"
"ds_read_b128 a[96:99], v6 offset:40960\n"
"v_mfma_f32_16x16x32_bf16 v[36:39], a[212:215], a[148:151], v[36:39]\n"
"ds_read_b128 a[108:111], v7 offset:43008\n"
"v_mfma_f32_16x16x32_bf16 v[68:71], a[220:223], a[148:151], v[68:71]\n"
"ds_read_b128 a[104:107], v6 offset:43008\n"
"v_mfma_f32_16x16x32_bf16 v[68:71], a[216:219], a[144:147], v[68:71]\n"
"ds_read_b128 a[116:119], v7 offset:45056\n"
"v_mfma_f32_16x16x32_bf16 v[40:43], a[208:211], a[152:155], v[40:43]\n"
"ds_read_b128 a[112:115], v6 offset:45056\n"
"v_mfma_f32_16x16x32_bf16 v[40:43], a[212:215], a[156:159], v[40:43]\n"
"ds_read_b128 a[124:127], v7 offset:47104\n"
"v_mfma_f32_16x16x32_bf16 v[72:75], a[220:223], a[156:159], v[72:75]\n"
"ds_read_b128 a[120:123], v6 offset:47104\n"
"v_mfma_f32_16x16x32_bf16 v[72:75], a[216:219], a[152:155], v[72:75]\n"
"s_lshl_b32 s14, s82, 5\n"
"s_lshl_b32 s15, s79, 2\n"
"s_add_u32 s14, s14, s15\n"
"v_mfma_f32_16x16x32_bf16 v[44:47], a[208:211], a[160:163], v[44:47]\n"
"s_lshl_b32 s14, s14, 6\n"
"s_lshr_b32 s15, s81, 4\n"
"s_add_u32 s14, s14, s15\n"
"v_mfma_f32_16x16x32_bf16 v[44:47], a[212:215], a[164:167], v[44:47]\n"
"s_and_b32 s15, s13, 1\n"
"s_mul_i32 s15, s15, 9\n"
"v_mfma_f32_16x16x32_bf16 v[76:79], a[220:223], a[164:167], v[76:79]\n"
"s_add_u32 s14, s14, s15\n"
"s_lshl_b32 s14, s14, 11\n"
"s_and_b32 s15, s13, 2\n"
"v_mfma_f32_16x16x32_bf16 v[76:79], a[216:219], a[160:163], v[76:79]\n"
"s_lshl_b32 s15, s15, 9\n"
"s_add_u32 s14, s14, s15\n"
"s_add_u32 s14, s14, 15073280\n"
"v_mfma_f32_16x16x32_bf16 v[48:51], a[208:211], a[168:171], v[48:51]\n"
"v_lshlrev_b32 v7, 4, v1\n"
"v_add_u32 v6, s14, v7\n"
"v_mfma_f32_16x16x32_bf16 v[48:51], a[212:215], a[172:175], v[48:51]\n"
"s_lshl_b32 s14, s5, 2\n"
"v_and_b32 v12, 3, v1\n"
"v_add_u32 v12, s14, v12\n"
"v_mfma_f32_16x16x32_bf16 v[80:83], a[220:223], a[172:175], v[80:83]\n"
"s_lshr_b32 s15, s83, 1\n"
"s_mul_i32 s14, s15, 14\n"
"v_add_u32 v13, s14, v12\n"
"v_mfma_f32_16x16x32_bf16 v[80:83], a[216:219], a[168:171], v[80:83]\n"
"s_lshl_b32 s15, s15, 1\n"
"s_add_u32 s15, s15, 28\n"
"v_mfma_f32_16x16x32_bf16 v[52:55], a[208:211], a[176:179], v[52:55]\n"
"v_mov_b32 v10, s15\n"
"v_cmp_lt_u32 vcc, 22, v12\n"
"v_mfma_f32_16x16x32_bf16 v[52:55], a[212:215], a[180:183], v[52:55]\n"
"s_nop 1\n"
"v_addc_co_u32 v10, vcc, 0, v10, vcc\n"
"v_cmp_gt_u32 vcc, 14, v12\n"
"v_mfma_f32_16x16x32_bf16 v[84:87], a[220:223], a[180:183], v[84:87]\n"
"s_nop 1\n"
"v_cndmask_b32 v10, v10, v13, vcc\n"
"s_and_b32 s14, s83, 1\n"
"v_mfma_f32_16x16x32_bf16 v[84:87], a[216:219], a[176:179], v[84:87]\n"
"s_lshl_b32 s14, s14, 2\n"
"v_bfe_u32 v12, v1, 2, 2\n"
"v_add_u32 v12, s14, v12\n"
"v_mfma_f32_16x16x32_bf16 v[56:59], a[208:211], a[184:187], v[56:59]\n"
"v_lshl_add_u32 v10, v12, 5, v10\n"
"v_lshlrev_b32 v10, 7, v10\n"
"v_mfma_f32_16x16x32_bf16 v[56:59], a[212:215], a[188:191], v[56:59]\n"
"v_cvt_pk_bf16_f32 v16, v32, v33\n"
"v_cvt_pk_bf16_f32 v17, v34, v35\n"
"v_mfma_f32_16x16x32_bf16 v[88:91], a[220:223], a[188:191], v[88:91]\n"
"v_cvt_pk_bf16_f32 v18, v64, v65\n"
"v_cvt_pk_bf16_f32 v19, v66, v67\n"
"v_mfma_f32_16x16x32_bf16 v[88:91], a[216:219], a[184:187], v[88:91]\n"
"s_mov_b32 s82, 0\n"
"buffer_store_dwordx4 v[16:19], v6, s[56:59], s82 offen sc1\n"
"v_mfma_f32_16x16x32_bf16 v[60:63], a[208:211], a[192:195], v[60:63]\n"
"v_cvt_pk_bf16_f32 v24, v36, v37\n"
"v_cvt_pk_bf16_f32 v25, v38, v39\n"
"v_mfma_f32_16x16x32_bf16 v[60:63], a[212:215], a[196:199], v[60:63]\n"
"v_cvt_pk_bf16_f32 v26, v68, v69\n"
"v_cvt_pk_bf16_f32 v27, v70, v71\n"
"v_mfma_f32_16x16x32_bf16 v[92:95], a[220:223], a[196:199], v[92:95]\n"
"s_mov_b32 s82, 2048\n"
"buffer_store_dwordx4 v[24:27], v6, s[56:59], s82 offen sc1\n"
"v_mfma_f32_16x16x32_bf16 v[92:95], a[216:219], a[192:195], v[92:95]\n"
"v_cvt_pk_bf16_f32 v16, v40, v41\n"
"v_cvt_pk_bf16_f32 v17, v42, v43\n"
"v_mfma_f32_16x16x32_bf16 a[32:35], a[208:211], a[200:203], a[32:35]\n"
"v_cvt_pk_bf16_f32 v18, v72, v73\n"
"v_cvt_pk_bf16_f32 v19, v74, v75\n"
"v_mfma_f32_16x16x32_bf16 a[32:35], a[212:215], a[204:207], a[32:35]\n"
"s_mov_b32 s82, 4096\n"
"buffer_store_dwordx4 v[16:19], v6, s[56:59], s82 offen sc1\n"
"v_mfma_f32_16x16x32_bf16 a[36:39], a[220:223], a[204:207], a[36:39]\n"
"v_cvt_pk_bf16_f32 v24, v44, v45\n"
"v_cvt_pk_bf16_f32 v25, v46, v47\n"
"v_mfma_f32_16x16x32_bf16 a[36:39], a[216:219], a[200:203], a[36:39]\n"
"v_cvt_pk_bf16_f32 v26, v76, v77\n"
"v_cvt_pk_bf16_f32 v27, v78, v79\n"
"s_waitcnt lgkmcnt(10)\n"
"v_mfma_f32_16x16x32_bf16 v[96:99], a[80:83], a[136:139], v[96:99]\n"
"s_mov_b32 s82, 6144\n"
"buffer_store_dwordx4 v[24:27], v6, s[56:59], s82 offen sc1\n"
"v_mfma_f32_16x16x32_bf16 v[96:99], a[84:87], a[140:143], v[96:99]\n"
"v_cvt_pk_bf16_f32 v16, v48, v49\n"
"v_cvt_pk_bf16_f32 v17, v50, v51\n"
"s_waitcnt lgkmcnt(9)\n"
"v_mfma_f32_16x16x32_bf16 v[128:131], a[92:95], a[140:143], v[128:131]\n"
"v_cvt_pk_bf16_f32 v18, v80, v81\n"
"v_cvt_pk_bf16_f32 v19, v82, v83\n"
"s_waitcnt lgkmcnt(8)\n"
"v_mfma_f32_16x16x32_bf16 v[128:131], a[88:91], a[136:139], v[128:131]\n"
"s_mov_b32 s82, 8192\n"
"buffer_store_dwordx4 v[16:19], v6, s[56:59], s82 offen sc1\n"
"v_mfma_f32_16x16x32_bf16 v[100:103], a[80:83], a[144:147], v[100:103]\n"
"v_cvt_pk_bf16_f32 v24, v52, v53\n"
"v_cvt_pk_bf16_f32 v25, v54, v55\n"
"v_mfma_f32_16x16x32_bf16 v[100:103], a[84:87], a[148:151], v[100:103]\n"
"v_cvt_pk_bf16_f32 v26, v84, v85\n"
"v_cvt_pk_bf16_f32 v27, v86, v87\n"
"v_mfma_f32_16x16x32_bf16 v[132:135], a[92:95], a[148:151], v[132:135]\n"
"s_mov_b32 s82, 10240\n"
"buffer_store_dwordx4 v[24:27], v6, s[56:59], s82 offen sc1\n"
"v_mfma_f32_16x16x32_bf16 v[132:135], a[88:91], a[144:147], v[132:135]\n"
"v_cvt_pk_bf16_f32 v16, v56, v57\n"
"v_cvt_pk_bf16_f32 v17, v58, v59\n"
"v_mfma_f32_16x16x32_bf16 v[104:107], a[80:83], a[152:155], v[104:107]\n"
"v_cvt_pk_bf16_f32 v18, v88, v89\n"
"v_cvt_pk_bf16_f32 v19, v90, v91\n"
"v_mfma_f32_16x16x32_bf16 v[104:107], a[84:87], a[156:159], v[104:107]\n"
"s_mov_b32 s82, 12288\n"
"buffer_store_dwordx4 v[16:19], v6, s[56:59], s82 offen sc1\n"
"v_mfma_f32_16x16x32_bf16 v[136:139], a[92:95], a[156:159], v[136:139]\n"
"v_cvt_pk_bf16_f32 v24, v60, v61\n"
"v_cvt_pk_bf16_f32 v25, v62, v63\n"
"v_mfma_f32_16x16x32_bf16 v[136:139], a[88:91], a[152:155], v[136:139]\n"
"v_cvt_pk_bf16_f32 v26, v92, v93\n"
"v_cvt_pk_bf16_f32 v27, v94, v95\n"
"v_mfma_f32_16x16x32_bf16 v[108:111], a[80:83], a[160:163], v[108:111]\n"
"s_mov_b32 s82, 14336\n"
"buffer_store_dwordx4 v[24:27], v6, s[56:59], s82 offen sc1\n"
"v_mfma_f32_16x16x32_bf16 v[108:111], a[84:87], a[164:167], v[108:111]\n"
"v_accvgpr_read_b32 v16, a32\n"
"v_accvgpr_read_b32 v17, a33\n"
"v_mfma_f32_16x16x32_bf16 v[140:143], a[92:95], a[164:167], v[140:143]\n"
"v_accvgpr_read_b32 v18, a34\n"
"v_accvgpr_read_b32 v19, a35\n"
"v_mfma_f32_16x16x32_bf16 v[140:143], a[88:91], a[160:163], v[140:143]\n"
"v_accvgpr_read_b32 v20, a36\n"
"v_accvgpr_read_b32 v21, a37\n"
"v_mfma_f32_16x16x32_bf16 v[112:115], a[80:83], a[168:171], v[112:115]\n"
"v_accvgpr_read_b32 v22, a38\n"
"v_accvgpr_read_b32 v23, a39\n"
"v_mfma_f32_16x16x32_bf16 v[112:115], a[84:87], a[172:175], v[112:115]\n"
"v_cvt_pk_bf16_f32 v16, v16, v17\n"
"v_cvt_pk_bf16_f32 v17, v18, v19\n"
"v_mfma_f32_16x16x32_bf16 v[144:147], a[92:95], a[172:175], v[144:147]\n"
"v_cvt_pk_bf16_f32 v18, v20, v21\n"
"v_cvt_pk_bf16_f32 v19, v22, v23\n"
"v_mfma_f32_16x16x32_bf16 v[144:147], a[88:91], a[168:171], v[144:147]\n"
"s_mov_b32 s82, 16384\n"
"buffer_store_dwordx4 v[16:19], v6, s[56:59], s82 offen sc1\n"
"v_cvt_pk_bf16_f32 v24, v96, v97\n"
"v_cvt_pk_bf16_f32 v25, v98, v99\n"
"v_cvt_pk_bf16_f32 v26, v128, v129\n"
"v_mfma_f32_16x16x32_bf16 v[116:119], a[80:83], a[176:179], v[116:119]\n"
"v_cvt_pk_bf16_f32 v27, v130, v131\n"
"s_mov_b32 s82, 131072\n"
"v_mfma_f32_16x16x32_bf16 v[116:119], a[84:87], a[180:183], v[116:119]\n"
"buffer_store_dwordx4 v[24:27], v6, s[56:59], s82 offen sc1\n"
"v_cvt_pk_bf16_f32 v16, v100, v101\n"
"v_cvt_pk_bf16_f32 v17, v102, v103\n"
"v_cvt_pk_bf16_f32 v18, v132, v133\n"
"v_mfma_f32_16x16x32_bf16 v[148:151], a[92:95], a[180:183], v[148:151]\n"
"v_cvt_pk_bf16_f32 v19, v134, v135\n"
"s_mov_b32 s82, 133120\n"
"v_mfma_f32_16x16x32_bf16 v[148:151], a[88:91], a[176:179], v[148:151]\n"
"buffer_store_dwordx4 v[16:19], v6, s[56:59], s82 offen sc1\n"
"v_cvt_pk_bf16_f32 v24, v104, v105\n"
"v_cvt_pk_bf16_f32 v25, v106, v107\n"
"v_cvt_pk_bf16_f32 v26, v136, v137\n"
"v_mfma_f32_16x16x32_bf16 v[120:123], a[80:83], a[184:187], v[120:123]\n"
"v_cvt_pk_bf16_f32 v27, v138, v139\n"
"s_mov_b32 s82, 135168\n"
"v_mfma_f32_16x16x32_bf16 v[120:123], a[84:87], a[188:191], v[120:123]\n"
"buffer_store_dwordx4 v[24:27], v6, s[56:59], s82 offen sc1\n"
"v_cvt_pk_bf16_f32 v16, v108, v109\n"
"v_cvt_pk_bf16_f32 v17, v110, v111\n"
"v_mfma_f32_16x16x32_bf16 v[152:155], a[92:95], a[188:191], v[152:155]\n"
"v_cvt_pk_bf16_f32 v18, v140, v141\n"
"v_cvt_pk_bf16_f32 v19, v142, v143\n"
"v_mfma_f32_16x16x32_bf16 v[152:155], a[88:91], a[184:187], v[152:155]\n"
"s_mov_b32 s82, 137216\n"
"buffer_store_dwordx4 v[16:19], v6, s[56:59], s82 offen sc1\n"
"v_mfma_f32_16x16x32_bf16 v[124:127], a[80:83], a[192:195], v[124:127]\n"
"v_cvt_pk_bf16_f32 v24, v112, v113\n"
"v_cvt_pk_bf16_f32 v25, v114, v115\n"
"v_mfma_f32_16x16x32_bf16 v[124:127], a[84:87], a[196:199], v[124:127]\n"
"v_cvt_pk_bf16_f32 v26, v144, v145\n"
"v_cvt_pk_bf16_f32 v27, v146, v147\n"
"v_mfma_f32_16x16x32_bf16 v[156:159], a[92:95], a[196:199], v[156:159]\n"
"s_mov_b32 s82, 139264\n"
"buffer_store_dwordx4 v[24:27], v6, s[56:59], s82 offen sc1\n"
"v_mfma_f32_16x16x32_bf16 v[156:159], a[88:91], a[192:195], v[156:159]\n"
"v_cvt_pk_bf16_f32 v16, v116, v117\n"
"v_cvt_pk_bf16_f32 v17, v118, v119\n"
"v_mfma_f32_16x16x32_bf16 a[40:43], a[80:83], a[200:203], a[40:43]\n"
"v_cvt_pk_bf16_f32 v18, v148, v149\n"
"v_cvt_pk_bf16_f32 v19, v150, v151\n"
"v_mfma_f32_16x16x32_bf16 a[40:43], a[84:87], a[204:207], a[40:43]\n"
"s_mov_b32 s82, 141312\n"
"buffer_store_dwordx4 v[16:19], v6, s[56:59], s82 offen sc1\n"
"v_mfma_f32_16x16x32_bf16 a[44:47], a[92:95], a[204:207], a[44:47]\n"
"v_cvt_pk_bf16_f32 v24, v120, v121\n"
"v_cvt_pk_bf16_f32 v25, v122, v123\n"
"v_mfma_f32_16x16x32_bf16 a[44:47], a[88:91], a[200:203], a[44:47]\n"
"v_cvt_pk_bf16_f32 v26, v152, v153\n"
"v_cvt_pk_bf16_f32 v27, v154, v155\n"
"s_waitcnt lgkmcnt(6)\n"
"v_mfma_f32_16x16x32_bf16 v[160:163], a[96:99], a[136:139], v[160:163]\n"
"s_mov_b32 s82, 143360\n"
"buffer_store_dwordx4 v[24:27], v6, s[56:59], s82 offen sc1\n"
"v_mfma_f32_16x16x32_bf16 v[160:163], a[100:103], a[140:143], v[160:163]\n"
"v_cvt_pk_bf16_f32 v16, v124, v125\n"
"v_cvt_pk_bf16_f32 v17, v126, v127\n"
"s_waitcnt lgkmcnt(5)\n"
"v_mfma_f32_16x16x32_bf16 v[192:195], a[108:111], a[140:143], v[192:195]\n"
"v_cvt_pk_bf16_f32 v18, v156, v157\n"
"v_cvt_pk_bf16_f32 v19, v158, v159\n"
"s_waitcnt lgkmcnt(4)\n"
"v_mfma_f32_16x16x32_bf16 v[192:195], a[104:107], a[136:139], v[192:195]\n"
"s_mov_b32 s82, 145408\n"
"buffer_store_dwordx4 v[16:19], v6, s[56:59], s82 offen sc1\n"
"s_waitcnt vmcnt(8) lgkmcnt(0)\n"
"v_mfma_f32_16x16x32_bf16 v[164:167], a[96:99], a[144:147], v[164:167]\n"
"s_barrier\n"
"v_mfma_f32_16x16x32_bf16 v[164:167], a[100:103], a[148:151], v[164:167]\n"
"s_cmp_eq_u32 s13, 0\n"
"s_cbranch_scc1 EV5_%=\n"
"EV5r_%=:\n"
"v_mfma_f32_16x16x32_bf16 v[196:199], a[108:111], a[148:151], v[196:199]\n"
"v_accvgpr_read_b32 v24, a40\n"
"v_accvgpr_read_b32 v25, a41\n"
"v_mfma_f32_16x16x32_bf16 v[196:199], a[104:107], a[144:147], v[196:199]\n"
"v_accvgpr_read_b32 v26, a42\n"
"v_accvgpr_read_b32 v27, a43\n"
"v_mfma_f32_16x16x32_bf16 v[168:171], a[96:99], a[152:155], v[168:171]\n"
"v_accvgpr_read_b32 v28, a44\n"
"v_accvgpr_read_b32 v29, a45\n"
"v_mfma_f32_16x16x32_bf16 v[168:171], a[100:103], a[156:159], v[168:171]\n"
"v_accvgpr_read_b32 v30, a46\n"
"v_accvgpr_read_b32 v31, a47\n"
"v_mfma_f32_16x16x32_bf16 v[200:203], a[108:111], a[156:159], v[200:203]\n"
"v_cvt_pk_bf16_f32 v24, v24, v25\n"
"v_cvt_pk_bf16_f32 v25, v26, v27\n"
"v_mfma_f32_16x16x32_bf16 v[200:203], a[104:107], a[152:155], v[200:203]\n"
"v_cvt_pk_bf16_f32 v26, v28, v29\n"
"v_cvt_pk_bf16_f32 v27, v30, v31\n"
"v_mfma_f32_16x16x32_bf16 v[172:175], a[96:99], a[160:163], v[172:175]\n"
"s_mov_b32 s82, 147456\n"
"buffer_store_dwordx4 v[24:27], v6, s[56:59], s82 offen sc1\n"
"v_cvt_pk_bf16_f32 v160, v160, v161\n"
"v_cvt_pk_bf16_f32 v161, v162, v163\n"
"v_cvt_pk_bf16_f32 v162, v192, v193\n"
"v_mfma_f32_16x16x32_bf16 v[172:175], a[100:103], a[164:167], v[172:175]\n"
"v_cvt_pk_bf16_f32 v163, v194, v195\n"
"v_cvt_pk_bf16_f32 v164, v164, v165\n"
"v_mfma_f32_16x16x32_bf16 v[204:207], a[108:111], a[164:167], v[204:207]\n"
"v_cvt_pk_bf16_f32 v165, v166, v167\n"
"v_cvt_pk_bf16_f32 v166, v196, v197\n"
"v_mfma_f32_16x16x32_bf16 v[204:207], a[104:107], a[160:163], v[204:207]\n"
"s_add_u32 m0, s76, 0\n"
"v_cvt_pk_bf16_f32 v167, v198, v199\n"
"v_mfma_f32_16x16x32_bf16 v[176:179], a[96:99], a[168:171], v[176:179]\n"
"buffer_load_dwordx4 v2, s[24:27], s64 offen offset:0 lds\n"
"v_cvt_pk_bf16_f32 v168, v168, v169\n"
"v_mfma_f32_16x16x32_bf16 v[176:179], a[100:103], a[172:175], v[176:179]\n"
"v_cvt_pk_bf16_f32 v169, v170, v171\n"
"v_cvt_pk_bf16_f32 v170, v200, v201\n"
"v_mfma_f32_16x16x32_bf16 v[208:211], a[108:111], a[172:175], v[208:211]\n"
"v_cvt_pk_bf16_f32 v171, v202, v203\n"
"v_mfma_f32_16x16x32_bf16 v[208:211], a[104:107], a[168:171], v[208:211]\n"
"buffer_load_dwordx4 v3, s[24:27], s64 offen offset:1024 lds\n"
"v_cvt_pk_bf16_f32 v172, v172, v173\n"
"v_mfma_f32_16x16x32_bf16 v[180:183], a[96:99], a[176:179], v[180:183]\n"
"v_cvt_pk_bf16_f32 v173, v174, v175\n"
"v_cvt_pk_bf16_f32 v174, v204, v205\n"
"v_mfma_f32_16x16x32_bf16 v[180:183], a[100:103], a[180:183], v[180:183]\n"
"buffer_load_dwordx4 v2, s[24:27], s65 offen offset:2048 lds\n"
"v_cvt_pk_bf16_f32 v175, v206, v207\n"
"v_mfma_f32_16x16x32_bf16 v[212:215], a[108:111], a[180:183], v[212:215]\n"
"v_cvt_pk_bf16_f32 v176, v176, v177\n"
"v_cvt_pk_bf16_f32 v177, v178, v179\n"
"v_mfma_f32_16x16x32_bf16 v[212:215], a[104:107], a[176:179], v[212:215]\n"
"buffer_load_dwordx4 v3, s[24:27], s65 offen offset:3072 lds\n"
"v_cvt_pk_bf16_f32 v178, v208, v209\n"
"v_mfma_f32_16x16x32_bf16 v[184:187], a[96:99], a[184:187], v[184:187]\n"
"v_cvt_pk_bf16_f32 v179, v210, v211\n"
"s_add_u32 m0, s76, 4096\n"
"v_mfma_f32_16x16x32_bf16 v[184:187], a[100:103], a[188:191], v[184:187]\n"
"buffer_load_dwordx4 v2, s[24:27], s66 offen offset:0 lds\n"
"v_cvt_pk_bf16_f32 v180, v180, v181\n"
"v_mfma_f32_16x16x32_bf16 v[216:219], a[108:111], a[188:191], v[216:219]\n"
"v_cvt_pk_bf16_f32 v181, v182, v183\n"
"v_cvt_pk_bf16_f32 v182, v212, v213\n"
"v_mfma_f32_16x16x32_bf16 v[216:219], a[104:107], a[184:187], v[216:219]\n"
"v_cvt_pk_bf16_f32 v183, v214, v215\n"
"v_mfma_f32_16x16x32_bf16 v[188:191], a[96:99], a[192:195], v[188:191]\n"
"buffer_load_dwordx4 v3, s[24:27], s66 offen offset:1024 lds\n"
"v_mfma_f32_16x16x32_bf16 v[188:191], a[100:103], a[196:199], v[188:191]\n"
"v_cvt_pk_bf16_f32 v184, v184, v185\n"
"v_cvt_pk_bf16_f32 v185, v186, v187\n"
"v_mfma_f32_16x16x32_bf16 v[220:223], a[108:111], a[196:199], v[220:223]\n"
"v_cvt_pk_bf16_f32 v186, v216, v217\n"
"v_mfma_f32_16x16x32_bf16 v[220:223], a[104:107], a[192:195], v[220:223]\n"
"buffer_load_dwordx4 v2, s[24:27], s67 offen offset:2048 lds\n"
"v_cvt_pk_bf16_f32 v187, v218, v219\n"
"v_mfma_f32_16x16x32_bf16 a[48:51], a[96:99], a[200:203], a[48:51]\n"
"v_add_u32 v2, 512, v2\n"
"v_mfma_f32_16x16x32_bf16 a[48:51], a[100:103], a[204:207], a[48:51]\n"
"v_cvt_pk_bf16_f32 v188, v188, v189\n"
"v_cvt_pk_bf16_f32 v189, v190, v191\n"
"v_mfma_f32_16x16x32_bf16 a[52:55], a[108:111], a[204:207], a[52:55]\n"
"buffer_load_dwordx4 v3, s[24:27], s67 offen offset:3072 lds\n"
"v_cvt_pk_bf16_f32 v190, v220, v221\n"
"v_mfma_f32_16x16x32_bf16 a[52:55], a[104:107], a[200:203], a[52:55]\n"
"v_add_u32 v3, 512, v3\n"
"v_cvt_pk_bf16_f32 v191, v222, v223\n"
"v_mfma_f32_16x16x32_bf16 v[224:227], a[112:115], a[136:139], v[224:227]\n"
"s_add_u32 m0, s76, 49152\n"
"v_mfma_f32_16x16x32_bf16 v[224:227], a[116:119], a[140:143], v[224:227]\n"
"buffer_load_dwordx4 v2, s[24:27], s64 offen offset:0 lds\n"
"v_accvgpr_read_b32 v16, a48\n"
"v_mfma_f32_16x16x32_bf16 a[0:3], a[124:127], a[140:143], a[0:3]\n"
"v_accvgpr_read_b32 v17, a49\n"
"v_accvgpr_read_b32 v18, a50\n"
"v_mfma_f32_16x16x32_bf16 a[0:3], a[120:123], a[136:139], a[0:3]\n"
"v_accvgpr_read_b32 v19, a51\n"
"v_accvgpr_read_b32 v20, a52\n"
"v_mfma_f32_16x16x32_bf16 v[228:231], a[112:115], a[144:147], v[228:231]\n"
"v_accvgpr_read_b32 v21, a53\n"
"v_accvgpr_read_b32 v22, a54\n"
"v_mfma_f32_16x16x32_bf16 v[228:231], a[116:119], a[148:151], v[228:231]\n"
"v_accvgpr_read_b32 v23, a55\n"
"v_cvt_pk_bf16_f32 v192, v16, v17\n"
"v_mfma_f32_16x16x32_bf16 a[4:7], a[124:127], a[148:151], a[4:7]\n"
"v_cvt_pk_bf16_f32 v193, v18, v19\n"
"v_cvt_pk_bf16_f32 v194, v20, v21\n"
"v_mfma_f32_16x16x32_bf16 a[4:7], a[120:123], a[144:147], a[4:7]\n"
"v_cvt_pk_bf16_f32 v195, v22, v23\n"
"v_mfma_f32_16x16x32_bf16 v[232:235], a[112:115], a[152:155], v[232:235]\n"
"buffer_load_dwordx4 v3, s[24:27], s64 offen offset:1024 lds\n"
"v_accvgpr_read_b32 v28, a0\n"
"v_mfma_f32_16x16x32_bf16 v[232:235], a[116:119], a[156:159], v[232:235]\n"
"v_accvgpr_read_b32 v29, a1\n"
"v_accvgpr_read_b32 v30, a2\n"
"v_mfma_f32_16x16x32_bf16 a[8:11], a[124:127], a[156:159], a[8:11]\n"
"v_accvgpr_read_b32 v31, a3\n"
"v_cvt_pk_bf16_f32 v224, v224, v225\n"
"v_mfma_f32_16x16x32_bf16 a[8:11], a[120:123], a[152:155], a[8:11]\n"
"v_cvt_pk_bf16_f32 v225, v226, v227\n"
"v_mfma_f32_16x16x32_bf16 v[236:239], a[112:115], a[160:163], v[236:239]\n"
"buffer_load_dwordx4 v2, s[24:27], s65 offen offset:2048 lds\n"
"v_cvt_pk_bf16_f32 v226, v28, v29\n"
"v_mfma_f32_16x16x32_bf16 v[236:239], a[116:119], a[164:167], v[236:239]\n"
"v_cvt_pk_bf16_f32 v227, v30, v31\n"
"v_accvgpr_read_b32 v20, a4\n"
"v_mfma_f32_16x16x32_bf16 a[12:15], a[124:127], a[164:167], a[12:15]\n"
"v_accvgpr_read_b32 v21, a5\n"
"v_accvgpr_read_b32 v22, a6\n"
"v_mfma_f32_16x16x32_bf16 a[12:15], a[120:123], a[160:163], a[12:15]\n"
"buffer_load_dwordx4 v3, s[24:27], s65 offen offset:3072 lds\n"
"v_accvgpr_read_b32 v23, a7\n"
"v_mfma_f32_16x16x32_bf16 v[240:243], a[112:115], a[168:171], v[240:243]\n"
"v_cvt_pk_bf16_f32 v228, v228, v229\n"
"v_cvt_pk_bf16_f32 v229, v230, v231\n"
"v_mfma_f32_16x16x32_bf16 v[240:243], a[116:119], a[172:175], v[240:243]\n"
"v_cvt_pk_bf16_f32 v230, v20, v21\n"
"v_cvt_pk_bf16_f32 v231, v22, v23\n"
"s_add_u32 m0, s76, 53248\n"
"v_mfma_f32_16x16x32_bf16 a[16:19], a[124:127], a[172:175], a[16:19]\n"
"buffer_load_dwordx4 v2, s[24:27], s66 offen offset:0 lds\n"
"v_accvgpr_read_b32 v28, a8\n"
"v_mfma_f32_16x16x32_bf16 a[16:19], a[120:123], a[168:171], a[16:19]\n"
"v_accvgpr_read_b32 v29, a9\n"
"v_accvgpr_read_b32 v30, a10\n"
"v_mfma_f32_16x16x32_bf16 v[244:247], a[112:115], a[176:179], v[244:247]\n"
"v_accvgpr_read_b32 v31, a11\n"
"v_cvt_pk_bf16_f32 v232, v232, v233\n"
"v_cvt_pk_bf16_f32 v233, v234, v235\n"
"v_mfma_f32_16x16x32_bf16 v[244:247], a[116:119], a[180:183], v[244:247]\n"
"v_cvt_pk_bf16_f32 v234, v28, v29\n"
"v_cvt_pk_bf16_f32 v235, v30, v31\n"
"v_mfma_f32_16x16x32_bf16 a[20:23], a[124:127], a[180:183], a[20:23]\n"
"buffer_load_dwordx4 v3, s[24:27], s66 offen offset:1024 lds\n"
"v_accvgpr_read_b32 v20, a12\n"
"v_mfma_f32_16x16x32_bf16 a[20:23], a[120:123], a[176:179], a[20:23]\n"
"v_accvgpr_read_b32 v21, a13\n"
"v_accvgpr_read_b32 v22, a14\n"
"v_mfma_f32_16x16x32_bf16 v[248:251], a[112:115], a[184:187], v[248:251]\n"
"v_accvgpr_read_b32 v23, a15\n"
"v_cvt_pk_bf16_f32 v236, v236, v237\n"
"v_cvt_pk_bf16_f32 v237, v238, v239\n"
"v_mfma_f32_16x16x32_bf16 v[248:251], a[116:119], a[188:191], v[248:251]\n"
"buffer_load_dwordx4 v2, s[24:27], s67 offen offset:2048 lds\n"
"v_cvt_pk_bf16_f32 v238, v20, v21\n"
"v_cvt_pk_bf16_f32 v239, v22, v23\n"
"v_add_u32 v2, 512, v2\n"
"v_mfma_f32_16x16x32_bf16 a[24:27], a[124:127], a[188:191], a[24:27]\n"
"v_accvgpr_read_b32 v28, a16\n"
"v_accvgpr_read_b32 v29, a17\n"
"v_mfma_f32_16x16x32_bf16 a[24:27], a[120:123], a[184:187], a[24:27]\n"
"v_accvgpr_read_b32 v30, a18\n"
"v_mfma_f32_16x16x32_bf16 v[252:255], a[112:115], a[192:195], v[252:255]\n"
"buffer_load_dwordx4 v3, s[24:27], s67 offen offset:3072 lds\n"
"v_accvgpr_read_b32 v31, a19\n"
"v_cvt_pk_bf16_f32 v240, v240, v241\n"
"v_cvt_pk_bf16_f32 v241, v242, v243\n"
"v_add_u32 v3, 512, v3\n"
"v_mfma_f32_16x16x32_bf16 v[252:255], a[116:119], a[196:199], v[252:255]\n"
"v_cvt_pk_bf16_f32 v242, v28, v29\n"
"v_cvt_pk_bf16_f32 v243, v30, v31\n"
"v_mfma_f32_16x16x32_bf16 a[28:31], a[124:127], a[196:199], a[28:31]\n"
"v_accvgpr_read_b32 v20, a20\n"
"v_accvgpr_read_b32 v21, a21\n"
"v_mfma_f32_16x16x32_bf16 a[28:31], a[120:123], a[192:195], a[28:31]\n"
"v_accvgpr_read_b32 v22, a22\n"
"v_accvgpr_read_b32 v23, a23\n"
"v_mfma_f32_16x16x32_bf16 a[56:59], a[112:115], a[200:203], a[56:59]\n"
"v_cvt_pk_bf16_f32 v244, v244, v245\n"
"v_cvt_pk_bf16_f32 v245, v246, v247\n"
"v_mfma_f32_16x16x32_bf16 a[56:59], a[116:119], a[204:207], a[56:59]\n"
"v_cvt_pk_bf16_f32 v246, v20, v21\n"
"v_cvt_pk_bf16_f32 v247, v22, v23\n"
"v_mfma_f32_16x16x32_bf16 a[60:63], a[124:127], a[204:207], a[60:63]\n"
"v_accvgpr_read_b32 v28, a24\n"
"v_accvgpr_read_b32 v29, a25\n"
"v_mfma_f32_16x16x32_bf16 a[60:63], a[120:123], a[200:203], a[60:63]\n"
"v_accvgpr_read_b32 v30, a26\n"
"v_accvgpr_read_b32 v31, a27\n"
"v_cvt_pk_bf16_f32 v248, v248, v249\n"
"v_cvt_pk_bf16_f32 v249, v250, v251\n"
"v_cvt_pk_bf16_f32 v250, v28, v29\n"
"v_cvt_pk_bf16_f32 v251, v30, v31\n"
"v_accvgpr_read_b32 v20, a28\n"
"v_accvgpr_read_b32 v21, a29\n"
"v_accvgpr_read_b32 v22, a30\n"
"v_accvgpr_read_b32 v23, a31\n"
"v_cvt_pk_bf16_f32 v252, v252, v253\n"
"v_cvt_pk_bf16_f32 v253, v254, v255\n"
"v_cvt_pk_bf16_f32 v254, v20, v21\n"
"v_cvt_pk_bf16_f32 v255, v22, v23\n"
"s_nop 7\n"
"v_accvgpr_read_b32 v24, a56\n"
"v_accvgpr_read_b32 v25, a57\n"
"v_accvgpr_read_b32 v26, a58\n"
"v_accvgpr_read_b32 v27, a59\n"
"v_accvgpr_read_b32 v28, a60\n"
"v_accvgpr_read_b32 v29, a61\n"
"v_accvgpr_read_b32 v30, a62\n"
"v_accvgpr_read_b32 v31, a63\n"
"v_cvt_pk_bf16_f32 v196, v24, v25\n"
"v_cvt_pk_bf16_f32 v197, v26, v27\n"
"v_cvt_pk_bf16_f32 v198, v28, v29\n"
"v_cvt_pk_bf16_f32 v199, v30, v31\n"
"s_waitcnt vmcnt(16)\n"
"s_barrier\n"
"s_cmp_eq_u32 s13, 0\n"
"s_cbranch_scc1 EV6_%=\n"
"EV6r_%=:\n"
"buffer_load_dword v11, v10, s[56:59], 0 offen offset:0 sc1\n"
"s_add_u32 m0, s76, 98304\n"
"s_nop 0\n"
"buffer_load_dwordx4 v2, s[24:27], s64 offen offset:0 lds\n"
"buffer_load_dwordx4 v3, s[24:27], s64 offen offset:1024 lds\n"
"buffer_load_dwordx4 v2, s[24:27], s65 offen offset:2048 lds\n"
"buffer_load_dwordx4 v3, s[24:27], s65 offen offset:3072 lds\n"
"s_add_u32 m0, s76, 102400\n"
"s_nop 0\n"
"buffer_load_dwordx4 v2, s[24:27], s66 offen offset:0 lds\n"
"buffer_load_dwordx4 v3, s[24:27], s66 offen offset:1024 lds\n"
"buffer_load_dwordx4 v2, s[24:27], s67 offen offset:2048 lds\n"
"v_add_u32 v2, 512, v2\n"
"buffer_load_dwordx4 v3, s[24:27], s67 offen offset:3072 lds\n"
"v_add_u32 v3, 512, v3\n"
"s_waitcnt vmcnt(8)\n"
"v_cmp_ne_u32 vcc, 1, v11\n"
"s_nop 4\n"
"s_cbranch_vccnz PL7_%=\n"
"PL7d_%=:\n"
"s_add_u32 m0, s77, 0\n"
"s_nop 0\n"
"buffer_load_dwordx4 v4, s[56:59], 0 offen offset:0 sc1 lds\n"
"buffer_load_dwordx4 v4, s[56:59], 0 offen offset:1024 sc1 lds\n"
"buffer_load_dwordx4 v4, s[56:59], 0 offen offset:2048 sc1 lds\n"
"buffer_load_dwordx4 v4, s[56:59], 0 offen offset:3072 sc1 lds\n"
"v_add_u32 v4, 524288, v4\n"
"s_add_u32 m0, s77, 49152\n"
"s_nop 0\n"
"buffer_load_dwordx4 v4, s[56:59], 0 offen offset:0 sc1 lds\n"
"buffer_load_dwordx4 v4, s[56:59], 0 offen offset:1024 sc1 lds\n"
"buffer_load_dwordx4 v4, s[56:59], 0 offen offset:2048 sc1 lds\n"
"buffer_load_dwordx4 v4, s[56:59], 0 offen offset:3072 sc1 lds\n"
"v_add_u32 v4, 524288, v4\n"
"s_add_u32 m0, s77, 98304\n"
"s_nop 0\n"
"buffer_load_dwordx4 v4, s[56:59], 0 offen offset:0 sc1 lds\n"
"buffer_load_dwordx4 v4, s[56:59], 0 offen offset:1024 sc1 lds\n"
"buffer_load_dwordx4 v4, s[56:59], 0 offen offset:2048 sc1 lds\n"
"buffer_load_dwordx4 v4, s[56:59], 0 offen offset:3072 sc1 lds\n"
"v_add_u32 v4, 524288, v4\n"
"v_lshrrev_b32 v13, 4, v1\n"
"v_bfe_u32 v14, v1, 1, 3\n"
"v_xor_b32 v13, v13, v14\n"
"v_lshlrev_b32 v13, 4, v13\n"
"v_and_b32 v14, 15, v1\n"
"v_lshl_add_u32 v12, v14, 7, v13\n"
"s_lshr_b32 s14, s13, 1\n"
"s_lshl_b32 s14, s14, 14\n"
"v_add_u32 v12, s14, v12\n"
"v_xor_b32 v13, 64, v12\n"
"s_and_b32 s14, s13, 1\n"
"s_lshl_b32 s14, s14, 13\n"
"s_add_u32 s14, s14, 32768\n"
"v_lshlrev_b32 v14, 4, v1\n"
"v_add_u32 v14, s14, v14\n"
"s_waitcnt vmcnt(8)\n"
"s_barrier\n"
"ds_read_b128 a[32:35], v12 offset:0\n"
"ds_read_b128 a[36:39], v13 offset:0\n"
"ds_read_b128 a[96:99], v14 offset:0\n"
"ds_read_b128 a[100:103], v14 offset:1024\n"
"ds_read_b128 a[104:107], v14 offset:2048\n"
"ds_read_b128 a[108:111], v14 offset:3072\n"
"ds_read_b128 a[112:115], v14 offset:4096\n"
"ds_read_b128 a[116:119], v14 offset:5120\n"
"ds_read_b128 a[120:123], v14 offset:6144\n"
"ds_read_b128 a[124:127], v14 offset:7168\n"
"ds_read_b128 a[40:43], v12 offset:2048\n"
"ds_read_b128 a[44:47], v13 offset:2048\n"
"ds_read_b128 a[48:51], v12 offset:4096\n"
"ds_read_b128 a[52:55], v13 offset:4096\n"
"ds_read_b128 a[56:59], v12 offset:6144\n"
"ds_read_b128 a[60:63], v13 offset:6144\n"
"ds_read_b128 a[64:67], v12 offset:8192\n"
"ds_read_b128 a[68:71], v13 offset:8192\n"
"ds_read_b128 a[72:75], v12 offset:10240\n"
"ds_read_b128 a[76:79], v13 offset:10240\n"
"ds_read_b128 a[80:83], v12 offset:12288\n"
"ds_read_b128 a[84:87], v13 offset:12288\n"
"ds_read_b128 a[88:91], v12 offset:14336\n"
"ds_read_b128 a[92:95], v13 offset:14336\n"
"buffer_load_dword v11, v10, s[56:59], 0 offen offset:4 sc1\n"
"s_waitcnt lgkmcnt(14)\n"
"v_mfma_f32_16x16x32_bf16 a[224:227], a[96:99], a[32:35], 0\n"
"s_add_u32 s76, s76, s8\n"
"s_add_u32 s77, s77, s8\n"
"s_cmp_eq_u32 s7, 98304\n"
"v_mfma_f32_16x16x32_bf16 a[224:227], a[100:103], a[36:39], a[224:227]\n"
"s_cselect_b32 s8, 4294868992, s9\n"
"s_add_u32 s7, s7, s8\n"
"v_add_u32 v12, s8, v12\n"
"v_mfma_f32_16x16x32_bf16 a[228:231], a[108:111], a[36:39], 0\n"
"v_add_u32 v13, s8, v13\n"
"v_add_u32 v14, s8, v14\n"
"v_mfma_f32_16x16x32_bf16 a[228:231], a[104:107], a[32:35], a[228:231]\n"
"v_mfma_f32_16x16x32_bf16 a[232:235], a[112:115], a[32:35], 0\n"
"v_mfma_f32_16x16x32_bf16 a[232:235], a[116:119], a[36:39], a[232:235]\n"
"v_mfma_f32_16x16x32_bf16 a[236:239], a[124:127], a[36:39], 0\n"
"v_mfma_f32_16x16x32_bf16 a[236:239], a[120:123], a[32:35], a[236:239]\n"
"s_waitcnt lgkmcnt(13)\n"
"v_mfma_f32_16x16x32_bf16 a[252:255], a[120:123], a[40:43], 0\n"
"s_waitcnt lgkmcnt(12)\n"
"v_mfma_f32_16x16x32_bf16 a[252:255], a[124:127], a[44:47], a[252:255]\n"
"v_mfma_f32_16x16x32_bf16 a[248:251], a[116:119], a[44:47], 0\n"
"v_mfma_f32_16x16x32_bf16 a[248:251], a[112:115], a[40:43], a[248:251]\n"
"v_mfma_f32_16x16x32_bf16 a[244:247], a[104:107], a[40:43], 0\n"
"v_mfma_f32_16x16x32_bf16 a[244:247], a[108:111], a[44:47], a[244:247]\n"
"v_mfma_f32_16x16x32_bf16 a[240:243], a[100:103], a[44:47], 0\n"
"v_mfma_f32_16x16x32_bf16 a[240:243], a[96:99], a[40:43], a[240:243]\n"
"s_waitcnt vmcnt(5) lgkmcnt(0)\n"
"s_barrier\n"
"v_mfma_f32_16x16x32_bf16 v[40:43], a[96:99], a[48:51], 0\n"
"ds_read_b128 a[128:131], v12 offset:0\n"
"v_mfma_f32_16x16x32_bf16 v[40:43], a[100:103], a[52:55], v[40:43]\n"
"ds_read_b128 a[132:135], v13 offset:0\n"
"s_add_u32 m0, s76, 0\n"
"v_mfma_f32_16x16x32_bf16 v[44:47], a[108:111], a[52:55], 0\n"
"s_mov_b32 s82, 262144\n"
"buffer_store_dwordx4 v[160:163], v6, s[56:59], s82 offen sc1\n"
"v_mfma_f32_16x16x32_bf16 v[44:47], a[104:107], a[48:51], v[44:47]\n"
"ds_read_b128 a[192:195], v14 offset:0\n"
"v_mfma_f32_16x16x32_bf16 v[48:51], a[112:115], a[48:51], 0\n"
"buffer_load_dwordx4 v2, s[24:27], s64 offen offset:0 lds\n"
"v_mfma_f32_16x16x32_bf16 v[48:51], a[116:119], a[52:55], v[48:51]\n"
"ds_read_b128 a[196:199], v14 offset:1024\n"
"v_mfma_f32_16x16x32_bf16 v[52:55], a[124:127], a[52:55], 0\n"
"ds_read_b128 a[200:203], v14 offset:2048\n"
"v_mfma_f32_16x16x32_bf16 v[52:55], a[120:123], a[48:51], v[52:55]\n"
"buffer_load_dwordx4 v3, s[24:27], s64 offen offset:1024 lds\n"
"v_mfma_f32_16x16x32_bf16 v[76:79], a[120:123], a[56:59], 0\n"
"ds_read_b128 a[204:207], v14 offset:3072\n"
"s_mov_b32 s82, 264192\n"
"v_mfma_f32_16x16x32_bf16 v[76:79], a[124:127], a[60:63], v[76:79]\n"
"buffer_store_dwordx4 v[164:167], v6, s[56:59], s82 offen sc1\n"
"v_mfma_f32_16x16x32_bf16 v[72:75], a[116:119], a[60:63], 0\n"
"ds_read_b128 a[208:211], v14 offset:4096\n"
"v_mfma_f32_16x16x32_bf16 v[72:75], a[112:115], a[56:59], v[72:75]\n"
"buffer_load_dwordx4 v2, s[24:27], s65 offen offset:2048 lds\n"
"v_mfma_f32_16x16x32_bf16 v[60:63], a[104:107], a[56:59], 0\n"
"ds_read_b128 a[212:215], v14 offset:5120\n"
"v_mfma_f32_16x16x32_bf16 v[60:63], a[108:111], a[60:63], v[60:63]\n"
"ds_read_b128 a[216:219], v14 offset:6144\n"
"s_mov_b32 s82, 266240\n"
"v_mfma_f32_16x16x32_bf16 v[56:59], a[100:103], a[60:63], 0\n"
"buffer_store_dwordx4 v[168:171], v6, s[56:59], s82 offen sc1\n"
"v_mfma_f32_16x16x32_bf16 v[56:59], a[96:99], a[56:59], v[56:59]\n"
"ds_read_b128 a[220:223], v14 offset:7168\n"
"v_mfma_f32_16x16x32_bf16 v[80:83], a[96:99], a[64:67], 0\n"
"buffer_load_dwordx4 v3, s[24:27], s65 offen offset:3072 lds\n"
"v_mfma_f32_16x16x32_bf16 v[80:83], a[100:103], a[68:71], v[80:83]\n"
"ds_read_b128 a[136:139], v12 offset:2048\n"
"s_add_u32 m0, s76, 4096\n"
"v_mfma_f32_16x16x32_bf16 v[84:87], a[108:111], a[68:71], 0\n"
"ds_read_b128 a[140:143], v13 offset:2048\n"
"v_mfma_f32_16x16x32_bf16 v[84:87], a[104:107], a[64:67], v[84:87]\n"
"buffer_load_dwordx4 v2, s[24:27], s66 offen offset:0 lds\n"
"v_mfma_f32_16x16x32_bf16 v[88:91], a[112:115], a[64:67], 0\n"
"ds_read_b128 a[144:147], v12 offset:4096\n"
"s_mov_b32 s82, 268288\n"
"v_mfma_f32_16x16x32_bf16 v[88:91], a[116:119], a[68:71], v[88:91]\n"
"buffer_store_dwordx4 v[172:175], v6, s[56:59], s82 offen sc1\n"
"v_mfma_f32_16x16x32_bf16 v[92:95], a[124:127], a[68:71], 0\n"
"ds_read_b128 a[148:151], v13 offset:4096\n"
"v_mfma_f32_16x16x32_bf16 v[92:95], a[120:123], a[64:67], v[92:95]\n"
"buffer_load_dwordx4 v3, s[24:27], s66 offen offset:1024 lds\n"
"v_mfma_f32_16x16x32_bf16 v[116:119], a[120:123], a[72:75], 0\n"
"ds_read_b128 a[152:155], v12 offset:6144\n"
"v_mfma_f32_16x16x32_bf16 v[116:119], a[124:127], a[76:79], v[116:119]\n"
"ds_read_b128 a[156:159], v13 offset:6144\n"
"s_mov_b32 s82, 270336\n"
"v_mfma_f32_16x16x32_bf16 v[112:115], a[116:119], a[76:79], 0\n"
"buffer_store_dwordx4 v[176:179], v6, s[56:59], s82 offen sc1\n"
"v_mfma_f32_16x16x32_bf16 v[112:115], a[112:115], a[72:75], v[112:115]\n"
"ds_read_b128 a[160:163], v12 offset:8192\n"
"v_mfma_f32_16x16x32_bf16 v[108:111], a[104:107], a[72:75], 0\n"
"buffer_load_dwordx4 v2, s[24:27], s67 offen offset:2048 lds\n"
"v_mfma_f32_16x16x32_bf16 v[108:111], a[108:111], a[76:79], v[108:111]\n"
"ds_read_b128 a[164:167], v13 offset:8192\n"
"v_subrev_u32 v2, 1408, v2\n"
"v_mfma_f32_16x16x32_bf16 v[104:107], a[100:103], a[76:79], 0\n"
"ds_read_b128 a[168:171], v12 offset:10240\n"
"v_mfma_f32_16x16x32_bf16 v[104:107], a[96:99], a[72:75], v[104:107]\n"
"buffer_load_dwordx4 v3, s[24:27], s67 offen offset:3072 lds\n"
"v_mfma_f32_16x16x32_bf16 v[120:123], a[96:99], a[80:83], 0\n"
"ds_read_b128 a[172:175], v13 offset:10240\n"
"v_subrev_u32 v3, 1408, v3\n"
"v_mfma_f32_16x16x32_bf16 v[120:123], a[100:103], a[84:87], v[120:123]\n"
"ds_read_b128 a[176:179], v12 offset:12288\n"
"s_add_u32 m0, s77, 0\n"
"v_mfma_f32_16x16x32_bf16 v[124:127], a[108:111], a[84:87], 0\n"
"ds_read_b128 a[180:183], v13 offset:12288\n"
"v_mfma_f32_16x16x32_bf16 v[124:127], a[104:107], a[80:83], v[124:127]\n"
"buffer_load_dwordx4 v4, s[56:59], 0 offen offset:0 sc1 lds\n"
"v_mfma_f32_16x16x32_bf16 v[136:139], a[112:115], a[80:83], 0\n"
"ds_read_b128 a[184:187], v12 offset:14336\n"
"v_mfma_f32_16x16x32_bf16 v[136:139], a[116:119], a[84:87], v[136:139]\n"
"ds_read_b128 a[188:191], v13 offset:14336\n"
"v_mfma_f32_16x16x32_bf16 v[140:143], a[124:127], a[84:87], 0\n"
"buffer_load_dwordx4 v4, s[56:59], 0 offen offset:1024 sc1 lds\n"
"v_mfma_f32_16x16x32_bf16 v[140:143], a[120:123], a[80:83], v[140:143]\n"
"buffer_load_dwordx4 v4, s[56:59], 0 offen offset:2048 sc1 lds\n"
"v_mfma_f32_16x16x32_bf16 v[156:159], a[120:123], a[88:91], 0\n"
"buffer_load_dwordx4 v4, s[56:59], 0 offen offset:3072 sc1 lds\n"
"v_subrev_u32 v4, 1441792, v4\n"
"v_mfma_f32_16x16x32_bf16 v[156:159], a[124:127], a[92:95], v[156:159]\n"
"s_add_u32 s76, s76, s8\n"
"s_add_u32 s77, s77, s8\n"
"s_cmp_eq_u32 s7, 98304\n"
"v_mfma_f32_16x16x32_bf16 v[152:155], a[116:119], a[92:95], 0\n"
"s_cselect_b32 s8, 4294868992, s9\n"
"s_add_u32 s7, s7, s8\n"
"v_add_u32 v12, s8, v12\n"
"v_mfma_f32_16x16x32_bf16 v[152:155], a[112:115], a[88:91], v[152:155]\n"
"v_add_u32 v13, s8, v13\n"
"v_mfma_f32_16x16x32_bf16 v[148:151], a[104:107], a[88:91], 0\n"
"v_add_u32 v14, s8, v14\n"
"v_mfma_f32_16x16x32_bf16 v[148:151], a[108:111], a[92:95], v[148:151]\n"
"v_mfma_f32_16x16x32_bf16 v[144:147], a[100:103], a[92:95], 0\n"
"v_mfma_f32_16x16x32_bf16 v[144:147], a[96:99], a[88:91], v[144:147]\n"
"s_waitcnt lgkmcnt(14)\n"
"v_mfma_f32_16x16x32_bf16 a[224:227], a[192:195], a[128:131], a[224:227]\n"
"s_waitcnt vmcnt(18) lgkmcnt(0)\n"
"s_barrier\n"
"s_waitcnt vmcnt(17)\n"
"v_mfma_f32_16x16x32_bf16 a[224:227], a[196:199], a[132:135], a[224:227]\n"
"v_cmp_ne_u32 vcc, 1, v11\n"
"v_mfma_f32_16x16x32_bf16 a[228:231], a[204:207], a[132:135], a[228:231]\n"
"s_nop 4\n"
"s_cbranch_vccnz PL10_%=\n"
"PL10d_%=:\n"
"v_mfma_f32_16x16x32_bf16 a[228:231], a[200:203], a[128:131], a[228:231]\n"
"ds_read_b128 a[32:35], v12 offset:0\n"
"s_add_u32 m0, s76, 0\n"
"v_mfma_f32_16x16x32_bf16 a[232:235], a[208:211], a[128:131], a[232:235]\n"
"s_mov_b32 s82, 272384\n"
"buffer_store_dwordx4 v[180:183], v6, s[56:59], s82 offen sc1\n"
"v_mfma_f32_16x16x32_bf16 a[232:235], a[212:215], a[132:135], a[232:235]\n"
"ds_read_b128 a[36:39], v13 offset:0\n"
"v_mfma_f32_16x16x32_bf16 a[236:239], a[220:223], a[132:135], a[236:239]\n"
"buffer_load_dwordx4 v2, s[24:27], s64 offen offset:0 lds\n"
"v_mfma_f32_16x16x32_bf16 a[236:239], a[216:219], a[128:131], a[236:239]\n"
"ds_read_b128 a[96:99], v14 offset:0\n"
"v_mfma_f32_16x16x32_bf16 a[252:255], a[216:219], a[136:139], a[252:255]\n"
"ds_read_b128 a[100:103], v14 offset:1024\n"
"v_mfma_f32_16x16x32_bf16 a[252:255], a[220:223], a[140:143], a[252:255]\n"
"buffer_load_dwordx4 v3, s[24:27], s64 offen offset:1024 lds\n"
"v_mfma_f32_16x16x32_bf16 a[248:251], a[212:215], a[140:143], a[248:251]\n"
"ds_read_b128 a[104:107], v14 offset:2048\n"
"v_mfma_f32_16x16x32_bf16 a[248:251], a[208:211], a[136:139], a[248:251]\n"
"ds_read_b128 a[108:111], v14 offset:3072\n"
"v_mfma_f32_16x16x32_bf16 a[244:247], a[200:203], a[136:139], a[244:247]\n"
"buffer_load_dwordx4 v2, s[24:27], s65 offen offset:2048 lds\n"
"v_mfma_f32_16x16x32_bf16 a[244:247], a[204:207], a[140:143], a[244:247]\n"
"s_mov_b32 s82, 274432\n"
"buffer_store_dwordx4 v[184:187], v6, s[56:59], s82 offen sc1\n"
"v_mfma_f32_16x16x32_bf16 a[240:243], a[196:199], a[140:143], a[240:243]\n"
"ds_read_b128 a[112:115], v14 offset:4096\n"
"v_mfma_f32_16x16x32_bf16 a[240:243], a[192:195], a[136:139], a[240:243]\n"
"ds_read_b128 a[116:119], v14 offset:5120\n"
"v_mfma_f32_16x16x32_bf16 v[40:43], a[192:195], a[144:147], v[40:43]\n"
"v_mfma_f32_16x16x32_bf16 v[40:43], a[196:199], a[148:151], v[40:43]\n"
"ds_read_b128 a[120:123], v14 offset:6144\n"
"v_mfma_f32_16x16x32_bf16 v[44:47], a[204:207], a[148:151], v[44:47]\n"
"buffer_load_dwordx4 v3, s[24:27], s65 offen offset:3072 lds\n"
"v_mfma_f32_16x16x32_bf16 v[44:47], a[200:203], a[144:147], v[44:47]\n"
"ds_read_b128 a[124:127], v14 offset:7168\n"
"v_mfma_f32_16x16x32_bf16 v[48:51], a[208:211], a[144:147], v[48:51]\n"
"s_add_u32 m0, s76, 4096\n"
"v_mfma_f32_16x16x32_bf16 v[48:51], a[212:215], a[148:151], v[48:51]\n"
"ds_read_b128 a[40:43], v12 offset:2048\n"
"v_mfma_f32_16x16x32_bf16 v[52:55], a[220:223], a[148:151], v[52:55]\n"
"buffer_load_dwordx4 v2, s[24:27], s66 offen offset:0 lds\n"
"v_mfma_f32_16x16x32_bf16 v[52:55], a[216:219], a[144:147], v[52:55]\n"
"s_mov_b32 s82, 276480\n"
"buffer_store_dwordx4 v[188:191], v6, s[56:59], s82 offen sc1\n"
"v_mfma_f32_16x16x32_bf16 v[76:79], a[216:219], a[152:155], v[76:79]\n"
"ds_read_b128 a[44:47], v13 offset:2048\n"
"v_mfma_f32_16x16x32_bf16 v[76:79], a[220:223], a[156:159], v[76:79]\n"
"ds_read_b128 a[48:51], v12 offset:4096\n"
"v_mfma_f32_16x16x32_bf16 v[72:75], a[212:215], a[156:159], v[72:75]\n"
"buffer_load_dwordx4 v3, s[24:27], s66 offen offset:1024 lds\n"
"v_mfma_f32_16x16x32_bf16 v[72:75], a[208:211], a[152:155], v[72:75]\n"
"ds_read_b128 a[52:55], v13 offset:4096\n"
"v_mfma_f32_16x16x32_bf16 v[60:63], a[200:203], a[152:155], v[60:63]\n"
"v_mfma_f32_16x16x32_bf16 v[60:63], a[204:207], a[156:159], v[60:63]\n"
"ds_read_b128 a[56:59], v12 offset:6144\n"
"v_mfma_f32_16x16x32_bf16 v[56:59], a[196:199], a[156:159], v[56:59]\n"
"v_mfma_f32_16x16x32_bf16 v[56:59], a[192:195], a[152:155], v[56:59]\n"
"ds_read_b128 a[60:63], v13 offset:6144\n"
"v_mfma_f32_16x16x32_bf16 v[80:83], a[192:195], a[160:163], v[80:83]\n"
"buffer_load_dwordx4 v2, s[24:27], s67 offen offset:2048 lds\n"
"v_add_u32 v2, 512, v2\n"
"v_mfma_f32_16x16x32_bf16 v[80:83], a[196:199], a[164:167], v[80:83]\n"
"s_mov_b32 s82, 278528\n"
"buffer_store_dwordx4 v[192:195], v6, s[56:59], s82 offen sc1\n"
"v_mfma_f32_16x16x32_bf16 v[84:87], a[204:207], a[164:167], v[84:87]\n"
"ds_read_b128 a[64:67], v12 offset:8192\n"
"v_mfma_f32_16x16x32_bf16 v[84:87], a[200:203], a[160:163], v[84:87]\n"
"ds_read_b128 a[68:71], v13 offset:8192\n"
"v_mfma_f32_16x16x32_bf16 v[88:91], a[208:211], a[160:163], v[88:91]\n"
"buffer_load_dwordx4 v3, s[24:27], s67 offen offset:3072 lds\n"
"v_mfma_f32_16x16x32_bf16 v[88:91], a[212:215], a[164:167], v[88:91]\n"
"ds_read_b128 a[72:75], v12 offset:10240\n"
"v_add_u32 v3, 512, v3\n"
"v_mfma_f32_16x16x32_bf16 v[92:95], a[220:223], a[164:167], v[92:95]\n"
"v_mfma_f32_16x16x32_bf16 v[92:95], a[216:219], a[160:163], v[92:95]\n"
"ds_read_b128 a[76:79], v13 offset:10240\n"
"s_add_u32 m0, s77, 0\n"
"v_mfma_f32_16x16x32_bf16 v[116:119], a[216:219], a[168:171], v[116:119]\n"
"buffer_load_dwordx4 v4, s[56:59], 0 offen offset:0 sc1 lds\n"
"v_mfma_f32_16x16x32_bf16 v[116:119], a[220:223], a[172:175], v[116:119]\n"
"ds_read_b128 a[80:83], v12 offset:12288\n"
"v_mfma_f32_16x16x32_bf16 v[112:115], a[212:215], a[172:175], v[112:115]\n"
"v_mfma_f32_16x16x32_bf16 v[112:115], a[208:211], a[168:171], v[112:115]\n"
"ds_read_b128 a[84:87], v13 offset:12288\n"
"v_mfma_f32_16x16x32_bf16 v[108:111], a[200:203], a[168:171], v[108:111]\n"
"v_mfma_f32_16x16x32_bf16 v[108:111], a[204:207], a[172:175], v[108:111]\n"
"ds_read_b128 a[88:91], v12 offset:14336\n"
"v_mfma_f32_16x16x32_bf16 v[104:107], a[196:199], a[172:175], v[104:107]\n"
"buffer_load_dwordx4 v4, s[56:59], 0 offen offset:1024 sc1 lds\n"
"v_mfma_f32_16x16x32_bf16 v[104:107], a[192:195], a[168:171], v[104:107]\n"
"ds_read_b128 a[92:95], v13 offset:14336\n"
"v_mfma_f32_16x16x32_bf16 v[120:123], a[192:195], a[176:179], v[120:123]\n"
"v_mfma_f32_16x16x32_bf16 v[120:123], a[196:199], a[180:183], v[120:123]\n"
"v_mfma_f32_16x16x32_bf16 v[124:127], a[204:207], a[180:183], v[124:127]\n"
"buffer_load_dwordx4 v4, s[56:59], 0 offen offset:2048 sc1 lds\n"
"v_mfma_f32_16x16x32_bf16 v[124:127], a[200:203], a[176:179], v[124:127]\n"
"v_mfma_f32_16x16x32_bf16 v[136:139], a[208:211], a[176:179], v[136:139]\n"
"v_mfma_f32_16x16x32_bf16 v[136:139], a[212:215], a[180:183], v[136:139]\n"
"v_mfma_f32_16x16x32_bf16 v[140:143], a[220:223], a[180:183], v[140:143]\n"
"v_mfma_f32_16x16x32_bf16 v[140:143], a[216:219], a[176:179], v[140:143]\n"
"buffer_load_dwordx4 v4, s[56:59], 0 offen offset:3072 sc1 lds\n"
"v_mfma_f32_16x16x32_bf16 v[156:159], a[216:219], a[184:187], v[156:159]\n"
"v_add_u32 v4, 524288, v4\n"
"s_add_u32 s76, s76, s8\n"
"v_mfma_f32_16x16x32_bf16 v[156:159], a[220:223], a[188:191], v[156:159]\n"
"s_add_u32 s77, s77, s8\n"
"s_cmp_eq_u32 s7, 98304\n"
"s_cselect_b32 s8, 4294868992, s9\n"
"v_mfma_f32_16x16x32_bf16 v[152:155], a[212:215], a[188:191], v[152:155]\n"
"s_add_u32 s7, s7, s8\n"
"v_add_u32 v12, s8, v12\n"
"v_mfma_f32_16x16x32_bf16 v[152:155], a[208:211], a[184:187], v[152:155]\n"
"v_add_u32 v13, s8, v13\n"
"v_mfma_f32_16x16x32_bf16 v[148:151], a[200:203], a[184:187], v[148:151]\n"
"v_add_u32 v14, s8, v14\n"
"v_mfma_f32_16x16x32_bf16 v[148:151], a[204:207], a[188:191], v[148:151]\n"
"v_mfma_f32_16x16x32_bf16 v[144:147], a[196:199], a[188:191], v[144:147]\n"
"v_mfma_f32_16x16x32_bf16 v[144:147], a[192:195], a[184:187], v[144:147]\n"
"s_waitcnt lgkmcnt(14)\n"
"v_mfma_f32_16x16x32_bf16 a[224:227], a[96:99], a[32:35], a[224:227]\n"
"s_waitcnt vmcnt(16) lgkmcnt(0)\n"
"s_barrier\n"
"v_mfma_f32_16x16x32_bf16 a[224:227], a[100:103], a[36:39], a[224:227]\n"
"ds_read_b128 a[128:131], v12 offset:0\n"
"v_mfma_f32_16x16x32_bf16 a[228:231], a[108:111], a[36:39], a[228:231]\n"
"s_add_u32 m0, s76, 0\n"
"v_mfma_f32_16x16x32_bf16 a[228:231], a[104:107], a[32:35], a[228:231]\n"
"s_mov_b32 s82, 393216\n"
"buffer_store_dwordx4 v[224:227], v6, s[56:59], s82 offen sc1\n"
"v_mfma_f32_16x16x32_bf16 a[232:235], a[112:115], a[32:35], a[232:235]\n"
"ds_read_b128 a[132:135], v13 offset:0\n"
"v_mfma_f32_16x16x32_bf16 a[232:235], a[116:119], a[36:39], a[232:235]\n"
"buffer_load_dwordx4 v2, s[24:27], s64 offen offset:0 lds\n"
"v_mfma_f32_16x16x32_bf16 a[236:239], a[124:127], a[36:39], a[236:239]\n"
"ds_read_b128 a[192:195], v14 offset:0\n"
"v_mfma_f32_16x16x32_bf16 a[236:239], a[120:123], a[32:35], a[236:239]\n"
"ds_read_b128 a[196:199], v14 offset:1024\n"
"v_mfma_f32_16x16x32_bf16 a[252:255], a[120:123], a[40:43], a[252:255]\n"
"buffer_load_dwordx4 v3, s[24:27], s64 offen offset:1024 lds\n"
"v_mfma_f32_16x16x32_bf16 a[252:255], a[124:127], a[44:47], a[252:255]\n"
"ds_read_b128 a[200:203], v14 offset:2048\n"
"v_mfma_f32_16x16x32_bf16 a[248:251], a[116:119], a[44:47], a[248:251]\n"
"v_mfma_f32_16x16x32_bf16 a[248:251], a[112:115], a[40:43], a[248:251]\n"
"s_mov_b32 s82, 395264\n"
"buffer_store_dwordx4 v[228:231], v6, s[56:59], s82 offen sc1\n"
"v_mfma_f32_16x16x32_bf16 a[244:247], a[104:107], a[40:43], a[244:247]\n"
"ds_read_b128 a[204:207], v14 offset:3072\n"
"v_mfma_f32_16x16x32_bf16 a[244:247], a[108:111], a[44:47], a[244:247]\n"
"buffer_load_dwordx4 v2, s[24:27], s65 offen offset:2048 lds\n"
"v_mfma_f32_16x16x32_bf16 a[240:243], a[100:103], a[44:47], a[240:243]\n"
"ds_read_b128 a[208:211], v14 offset:4096\n"
"v_mfma_f32_16x16x32_bf16 a[240:243], a[96:99], a[40:43], a[240:243]\n"
"ds_read_b128 a[212:215], v14 offset:5120\n"
"v_mfma_f32_16x16x32_bf16 v[40:43], a[96:99], a[48:51], v[40:43]\n"
"v_mfma_f32_16x16x32_bf16 v[40:43], a[100:103], a[52:55], v[40:43]\n"
"ds_read_b128 a[216:219], v14 offset:6144\n"
"v_mfma_f32_16x16x32_bf16 v[44:47], a[108:111], a[52:55], v[44:47]\n"
"buffer_load_dwordx4 v3, s[24:27], s65 offen offset:3072 lds\n"
"v_mfma_f32_16x16x32_bf16 v[44:47], a[104:107], a[48:51], v[44:47]\n"
"s_mov_b32 s82, 397312\n"
"buffer_store_dwordx4 v[232:235], v6, s[56:59], s82 offen sc1\n"
"v_mfma_f32_16x16x32_bf16 v[48:51], a[112:115], a[48:51], v[48:51]\n"
"ds_read_b128 a[220:223], v14 offset:7168\n"
"s_add_u32 m0, s76, 4096\n"
"v_mfma_f32_16x16x32_bf16 v[48:51], a[116:119], a[52:55], v[48:51]\n"
"ds_read_b128 a[136:139], v12 offset:2048\n"
"v_mfma_f32_16x16x32_bf16 v[52:55], a[124:127], a[52:55], v[52:55]\n"
"buffer_load_dwordx4 v2, s[24:27], s66 offen offset:0 lds\n"
"v_mfma_f32_16x16x32_bf16 v[52:55], a[120:123], a[48:51], v[52:55]\n"
"ds_read_b128 a[140:143], v13 offset:2048\n"
"v_mfma_f32_16x16x32_bf16 v[76:79], a[120:123], a[56:59], v[76:79]\n"
"v_mfma_f32_16x16x32_bf16 v[76:79], a[124:127], a[60:63], v[76:79]\n"
"ds_read_b128 a[144:147], v12 offset:4096\n"
"v_mfma_f32_16x16x32_bf16 v[72:75], a[116:119], a[60:63], v[72:75]\n"
"buffer_load_dwordx4 v3, s[24:27], s66 offen offset:1024 lds\n"
"v_mfma_f32_16x16x32_bf16 v[72:75], a[112:115], a[56:59], v[72:75]\n"
"s_mov_b32 s82, 399360\n"
"buffer_store_dwordx4 v[236:239], v6, s[56:59], s82 offen sc1\n"
"v_mfma_f32_16x16x32_bf16 v[60:63], a[104:107], a[56:59], v[60:63]\n"
"ds_read_b128 a[148:151], v13 offset:4096\n"
"v_mfma_f32_16x16x32_bf16 v[60:63], a[108:111], a[60:63], v[60:63]\n"
"ds_read_b128 a[152:155], v12 offset:6144\n"
"v_mfma_f32_16x16x32_bf16 v[56:59], a[100:103], a[60:63], v[56:59]\n"
"v_mfma_f32_16x16x32_bf16 v[56:59], a[96:99], a[56:59], v[56:59]\n"
"ds_read_b128 a[156:159], v13 offset:6144\n"
"v_mfma_f32_16x16x32_bf16 v[80:83], a[96:99], a[64:67], v[80:83]\n"
"buffer_load_dwordx4 v2, s[24:27], s67 offen offset:2048 lds\n"
"v_add_u32 v2, 512, v2\n"
"v_mfma_f32_16x16x32_bf16 v[80:83], a[100:103], a[68:71], v[80:83]\n"
"ds_read_b128 a[160:163], v12 offset:8192\n"
"v_mfma_f32_16x16x32_bf16 v[84:87], a[108:111], a[68:71], v[84:87]\n"
"v_mfma_f32_16x16x32_bf16 v[84:87], a[104:107], a[64:67], v[84:87]\n"
"s_mov_b32 s82, 401408\n"
"buffer_store_dwordx4 v[240:243], v6, s[56:59], s82 offen sc1\n"
"v_mfma_f32_16x16x32_bf16 v[88:91], a[112:115], a[64:67], v[88:91]\n"
"ds_read_b128 a[164:167], v13 offset:8192\n"
"v_mfma_f32_16x16x32_bf16 v[88:91], a[116:119], a[68:71], v[88:91]\n"
"buffer_load_dwordx4 v3, s[24:27], s67 offen offset:3072 lds\n"
"v_mfma_f32_16x16x32_bf16 v[92:95], a[124:127], a[68:71], v[92:95]\n"
"ds_read_b128 a[168:171], v12 offset:10240\n"
"v_add_u32 v3, 512, v3\n"
"v_mfma_f32_16x16x32_bf16 v[92:95], a[120:123], a[64:67], v[92:95]\n"
"ds_read_b128 a[172:175], v13 offset:10240\n"
"s_add_u32 m0, s77, 0\n"
"v_mfma_f32_16x16x32_bf16 v[116:119], a[120:123], a[72:75], v[116:119]\n"
"buffer_load_dwordx4 v4, s[56:59], 0 offen offset:0 sc1 lds\n"
"v_mfma_f32_16x16x32_bf16 v[116:119], a[124:127], a[76:79], v[116:119]\n"
"ds_read_b128 a[176:179], v12 offset:12288\n"
"v_mfma_f32_16x16x32_bf16 v[112:115], a[116:119], a[76:79], v[112:115]\n"
"v_mfma_f32_16x16x32_bf16 v[112:115], a[112:115], a[72:75], v[112:115]\n"
"ds_read_b128 a[180:183], v13 offset:12288\n"
"v_mfma_f32_16x16x32_bf16 v[108:111], a[104:107], a[72:75], v[108:111]\n"
"v_mfma_f32_16x16x32_bf16 v[108:111], a[108:111], a[76:79], v[108:111]\n"
"ds_read_b128 a[184:187], v12 offset:14336\n"
"v_mfma_f32_16x16x32_bf16 v[104:107], a[100:103], a[76:79], v[104:107]\n"
"buffer_load_dwordx4 v4, s[56:59], 0 offen offset:1024 sc1 lds\n"
"v_mfma_f32_16x16x32_bf16 v[104:107], a[96:99], a[72:75], v[104:107]\n"
"ds_read_b128 a[188:191], v13 offset:14336\n"
"v_mfma_f32_16x16x32_bf16 v[120:123], a[96:99], a[80:83], v[120:123]\n"
"v_mfma_f32_16x16x32_bf16 v[120:123], a[100:103], a[84:87], v[120:123]\n"
"v_mfma_f32_16x16x32_bf16 v[124:127], a[108:111], a[84:87], v[124:127]\n"
"buffer_load_dwordx4 v4, s[56:59], 0 offen offset:2048 sc1 lds\n"
"v_mfma_f32_16x16x32_bf16 v[124:127], a[104:107], a[80:83], v[124:127]\n"
"v_mfma_f32_16x16x32_bf16 v[136:139], a[112:115], a[80:83], v[136:139]\n"
"v_mfma_f32_16x16x32_bf16 v[136:139], a[116:119], a[84:87], v[136:139]\n"
"v_mfma_f32_16x16x32_bf16 v[140:143], a[124:127], a[84:87], v[140:143]\n"
"v_mfma_f32_16x16x32_bf16 v[140:143], a[120:123], a[80:83], v[140:143]\n"
"buffer_load_dwordx4 v4, s[56:59], 0 offen offset:3072 sc1 lds\n"
"v_mfma_f32_16x16x32_bf16 v[156:159], a[120:123], a[88:91], v[156:159]\n"
"v_add_u32 v4, 524288, v4\n"
"s_add_u32 s76, s76, s8\n"
"v_mfma_f32_16x16x32_bf16 v[156:159], a[124:127], a[92:95], v[156:159]\n"
"s_add_u32 s77, s77, s8\n"
"s_cmp_eq_u32 s7, 98304\n"
"s_cselect_b32 s8, 4294868992, s9\n"
"v_mfma_f32_16x16x32_bf16 v[152:155], a[116:119], a[92:95], v[152:155]\n"
"s_add_u32 s7, s7, s8\n"
"v_add_u32 v12, s8, v12\n"
"v_mfma_f32_16x16x32_bf16 v[152:155], a[112:115], a[88:91], v[152:155]\n"
"v_add_u32 v13, s8, v13\n"
"v_mfma_f32_16x16x32_bf16 v[148:151], a[104:107], a[88:91], v[148:151]\n"
"v_add_u32 v14, s8, v14\n"
"v_mfma_f32_16x16x32_bf16 v[148:151], a[108:111], a[92:95], v[148:151]\n"
"v_mfma_f32_16x16x32_bf16 v[144:147], a[100:103], a[92:95], v[144:147]\n"
"v_mfma_f32_16x16x32_bf16 v[144:147], a[96:99], a[88:91], v[144:147]\n"
"s_waitcnt lgkmcnt(14)\n"
"v_mfma_f32_16x16x32_bf16 a[224:227], a[192:195], a[128:131], a[224:227]\n"
"s_waitcnt vmcnt(17) lgkmcnt(0)\n"
"s_barrier\n"
"v_mfma_f32_16x16x32_bf16 a[224:227], a[196:199], a[132:135], a[224:227]\n"
"s_cmp_eq_u32 s13, 0\n"
"s_cbranch_scc1 EV8_%=\n"
"EV8r_%=:\n"
"v_mfma_f32_16x16x32_bf16 a[228:231], a[204:207], a[132:135], a[228:231]\n"
"ds_read_b128 a[32:35], v12 offset:0\n"
"s_add_u32 m0, s76, 0\n"
"v_mfma_f32_16x16x32_bf16 a[228:231], a[200:203], a[128:131], a[228:231]\n"
"s_mov_b32 s82, 403456\n"
"buffer_store_dwordx4 v[244:247], v6, s[56:59], s82 offen sc1\n"
"v_mfma_f32_16x16x32_bf16 a[232:235], a[208:211], a[128:131], a[232:235]\n"
"ds_read_b128 a[36:39], v13 offset:0\n"
"v_mfma_f32_16x16x32_bf16 a[232:235], a[212:215], a[132:135], a[232:235]\n"
"buffer_load_dwordx4 v2, s[24:27], s64 offen offset:0 lds\n"
"v_mfma_f32_16x16x32_bf16 a[236:239], a[220:223], a[132:135], a[236:239]\n"
"ds_read_b128 a[96:99], v14 offset:0\n"
"v_mfma_f32_16x16x32_bf16 a[236:239], a[216:219], a[128:131], a[236:239]\n"
"ds_read_b128 a[100:103], v14 offset:1024\n"
"v_mfma_f32_16x16x32_bf16 a[252:255], a[216:219], a[136:139], a[252:255]\n"
"buffer_load_dwordx4 v3, s[24:27], s64 offen offset:1024 lds\n"
"v_mfma_f32_16x16x32_bf16 a[252:255], a[220:223], a[140:143], a[252:255]\n"
"ds_read_b128 a[104:107], v14 offset:2048\n"
"v_mfma_f32_16x16x32_bf16 a[248:251], a[212:215], a[140:143], a[248:251]\n"
"v_mfma_f32_16x16x32_bf16 a[248:251], a[208:211], a[136:139], a[248:251]\n"
"ds_read_b128 a[108:111], v14 offset:3072\n"
"v_mfma_f32_16x16x32_bf16 a[244:247], a[200:203], a[136:139], a[244:247]\n"
"buffer_load_dwordx4 v2, s[24:27], s65 offen offset:2048 lds\n"
"v_mfma_f32_16x16x32_bf16 a[244:247], a[204:207], a[140:143], a[244:247]\n"
"s_mov_b32 s82, 405504\n"
"buffer_store_dwordx4 v[248:251], v6, s[56:59], s82 offen sc1\n"
"v_mfma_f32_16x16x32_bf16 a[240:243], a[196:199], a[140:143], a[240:243]\n"
"ds_read_b128 a[112:115], v14 offset:4096\n"
"v_mfma_f32_16x16x32_bf16 a[240:243], a[192:195], a[136:139], a[240:243]\n"
"ds_read_b128 a[116:119], v14 offset:5120\n"
"v_mfma_f32_16x16x32_bf16 v[40:43], a[192:195], a[144:147], v[40:43]\n"
"v_mfma_f32_16x16x32_bf16 v[40:43], a[196:199], a[148:151], v[40:43]\n"
"ds_read_b128 a[120:123], v14 offset:6144\n"
"v_mfma_f32_16x16x32_bf16 v[44:47], a[204:207], a[148:151], v[44:47]\n"
"buffer_load_dwordx4 v3, s[24:27], s65 offen offset:3072 lds\n"
"v_mfma_f32_16x16x32_bf16 v[44:47], a[200:203], a[144:147], v[44:47]\n"
"ds_read_b128 a[124:127], v14 offset:7168\n"
"v_mfma_f32_16x16x32_bf16 v[48:51], a[208:211], a[144:147], v[48:51]\n"
"s_add_u32 m0, s76, 4096\n"
"v_mfma_f32_16x16x32_bf16 v[48:51], a[212:215], a[148:151], v[48:51]\n"
"ds_read_b128 a[40:43], v12 offset:2048\n"
"v_mfma_f32_16x16x32_bf16 v[52:55], a[220:223], a[148:151], v[52:55]\n"
"buffer_load_dwordx4 v2, s[24:27], s66 offen offset:0 lds\n"
"v_mfma_f32_16x16x32_bf16 v[52:55], a[216:219], a[144:147], v[52:55]\n"
"s_mov_b32 s82, 407552\n"
"buffer_store_dwordx4 v[252:255], v6, s[56:59], s82 offen sc1\n"
"v_mfma_f32_16x16x32_bf16 v[76:79], a[216:219], a[152:155], v[76:79]\n"
"ds_read_b128 a[44:47], v13 offset:2048\n"
"v_mfma_f32_16x16x32_bf16 v[76:79], a[220:223], a[156:159], v[76:79]\n"
"ds_read_b128 a[48:51], v12 offset:4096\n"
"v_mfma_f32_16x16x32_bf16 v[72:75], a[212:215], a[156:159], v[72:75]\n"
"buffer_load_dwordx4 v3, s[24:27], s66 offen offset:1024 lds\n"
"v_mfma_f32_16x16x32_bf16 v[72:75], a[208:211], a[152:155], v[72:75]\n"
"ds_read_b128 a[52:55], v13 offset:4096\n"
"v_mfma_f32_16x16x32_bf16 v[60:63], a[200:203], a[152:155], v[60:63]\n"
"v_mfma_f32_16x16x32_bf16 v[60:63], a[204:207], a[156:159], v[60:63]\n"
"ds_read_b128 a[56:59], v12 offset:6144\n"
"v_mfma_f32_16x16x32_bf16 v[56:59], a[196:199], a[156:159], v[56:59]\n"
"v_mfma_f32_16x16x32_bf16 v[56:59], a[192:195], a[152:155], v[56:59]\n"
"ds_read_b128 a[60:63], v13 offset:6144\n"
"v_mfma_f32_16x16x32_bf16 v[80:83], a[192:195], a[160:163], v[80:83]\n"
"buffer_load_dwordx4 v2, s[24:27], s67 offen offset:2048 lds\n"
"v_add_u32 v2, 512, v2\n"
"v_mfma_f32_16x16x32_bf16 v[80:83], a[196:199], a[164:167], v[80:83]\n"
"s_mov_b32 s82, 409600\n"
"buffer_store_dwordx4 v[196:199], v6, s[56:59], s82 offen sc1\n"
"v_mfma_f32_16x16x32_bf16 v[84:87], a[204:207], a[164:167], v[84:87]\n"
"ds_read_b128 a[64:67], v12 offset:8192\n"
"v_mfma_f32_16x16x32_bf16 v[84:87], a[200:203], a[160:163], v[84:87]\n"
"ds_read_b128 a[68:71], v13 offset:8192\n"
"v_mfma_f32_16x16x32_bf16 v[88:91], a[208:211], a[160:163], v[88:91]\n"
"buffer_load_dwordx4 v3, s[24:27], s67 offen offset:3072 lds\n"
"v_mfma_f32_16x16x32_bf16 v[88:91], a[212:215], a[164:167], v[88:91]\n"
"ds_read_b128 a[72:75], v12 offset:10240\n"
"v_add_u32 v3, 512, v3\n"
"v_mfma_f32_16x16x32_bf16 v[92:95], a[220:223], a[164:167], v[92:95]\n"
"v_mfma_f32_16x16x32_bf16 v[92:95], a[216:219], a[160:163], v[92:95]\n"
"ds_read_b128 a[76:79], v13 offset:10240\n"
"s_add_u32 m0, s77, 0\n"
"v_mfma_f32_16x16x32_bf16 v[116:119], a[216:219], a[168:171], v[116:119]\n"
"buffer_load_dwordx4 v4, s[56:59], 0 offen offset:0 sc1 lds\n"
"v_mfma_f32_16x16x32_bf16 v[116:119], a[220:223], a[172:175], v[116:119]\n"
"ds_read_b128 a[80:83], v12 offset:12288\n"
"v_mfma_f32_16x16x32_bf16 v[112:115], a[212:215], a[172:175], v[112:115]\n"
"v_mfma_f32_16x16x32_bf16 v[112:115], a[208:211], a[168:171], v[112:115]\n"
"ds_read_b128 a[84:87], v13 offset:12288\n"
"v_mfma_f32_16x16x32_bf16 v[108:111], a[200:203], a[168:171], v[108:111]\n"
"v_mfma_f32_16x16x32_bf16 v[108:111], a[204:207], a[172:175], v[108:111]\n"
"ds_read_b128 a[88:91], v12 offset:14336\n"
"v_mfma_f32_16x16x32_bf16 v[104:107], a[196:199], a[172:175], v[104:107]\n"
"buffer_load_dwordx4 v4, s[56:59], 0 offen offset:1024 sc1 lds\n"
"v_mfma_f32_16x16x32_bf16 v[104:107], a[192:195], a[168:171], v[104:107]\n"
"ds_read_b128 a[92:95], v13 offset:14336\n"
"v_mfma_f32_16x16x32_bf16 v[120:123], a[192:195], a[176:179], v[120:123]\n"
"v_mfma_f32_16x16x32_bf16 v[120:123], a[196:199], a[180:183], v[120:123]\n"
"v_mfma_f32_16x16x32_bf16 v[124:127], a[204:207], a[180:183], v[124:127]\n"
"buffer_load_dwordx4 v4, s[56:59], 0 offen offset:2048 sc1 lds\n"
"v_mfma_f32_16x16x32_bf16 v[124:127], a[200:203], a[176:179], v[124:127]\n"
"v_mfma_f32_16x16x32_bf16 v[136:139], a[208:211], a[176:179], v[136:139]\n"
"v_mfma_f32_16x16x32_bf16 v[136:139], a[212:215], a[180:183], v[136:139]\n"
"v_mfma_f32_16x16x32_bf16 v[140:143], a[220:223], a[180:183], v[140:143]\n"
"v_mfma_f32_16x16x32_bf16 v[140:143], a[216:219], a[176:179], v[140:143]\n"
"buffer_load_dwordx4 v4, s[56:59], 0 offen offset:3072 sc1 lds\n"
"v_mfma_f32_16x16x32_bf16 v[156:159], a[216:219], a[184:187], v[156:159]\n"
"v_add_u32 v4, 524288, v4\n"
"s_add_u32 s76, s76, s8\n"
"v_mfma_f32_16x16x32_bf16 v[156:159], a[220:223], a[188:191], v[156:159]\n"
"s_add_u32 s77, s77, s8\n"
"s_cmp_eq_u32 s7, 98304\n"
"s_cselect_b32 s8, 4294868992, s9\n"
"v_mfma_f32_16x16x32_bf16 v[152:155], a[212:215], a[188:191], v[152:155]\n"
"s_add_u32 s7, s7, s8\n"
"v_add_u32 v12, s8, v12\n"
"v_mfma_f32_16x16x32_bf16 v[152:155], a[208:211], a[184:187], v[152:155]\n"
"v_add_u32 v13, s8, v13\n"
"v_mfma_f32_16x16x32_bf16 v[148:151], a[200:203], a[184:187], v[148:151]\n"
"v_add_u32 v14, s8, v14\n"
"v_mfma_f32_16x16x32_bf16 v[148:151], a[204:207], a[188:191], v[148:151]\n"
"v_mfma_f32_16x16x32_bf16 v[144:147], a[196:199], a[188:191], v[144:147]\n"
"v_mfma_f32_16x16x32_bf16 v[144:147], a[192:195], a[184:187], v[144:147]\n"
"s_waitcnt lgkmcnt(14)\n"
"v_mfma_f32_16x16x32_bf16 a[224:227], a[96:99], a[32:35], a[224:227]\n"
"s_waitcnt vmcnt(16) lgkmcnt(0)\n"
"s_barrier\n"
"v_mfma_f32_16x16x32_bf16 a[224:227], a[100:103], a[36:39], a[224:227]\n"
"buffer_load_dword v11, v10, s[56:59], 0 offen offset:8 sc1\n"
"v_mfma_f32_16x16x32_bf16 a[228:231], a[108:111], a[36:39], a[228:231]\n"
"ds_read_b128 a[128:131], v12 offset:0\n"
"s_add_u32 m0, s76, 0\n"
"v_mfma_f32_16x16x32_bf16 a[228:231], a[104:107], a[32:35], a[228:231]\n"
"ds_read_b128 a[132:135], v13 offset:0\n"
"v_mfma_f32_16x16x32_bf16 a[232:235], a[112:115], a[32:35], a[232:235]\n"
"buffer_load_dwordx4 v2, s[24:27], s64 offen offset:0 lds\n"
"v_mfma_f32_16x16x32_bf16 a[232:235], a[116:119], a[36:39], a[232:235]\n"
"ds_read_b128 a[192:195], v14 offset:0\n"
"v_mfma_f32_16x16x32_bf16 a[236:239], a[124:127], a[36:39], a[236:239]\n"
"v_mfma_f32_16x16x32_bf16 a[236:239], a[120:123], a[32:35], a[236:239]\n"
"ds_read_b128 a[196:199], v14 offset:1024\n"
"v_mfma_f32_16x16x32_bf16 a[252:255], a[120:123], a[40:43], a[252:255]\n"
"buffer_load_dwordx4 v3, s[24:27], s64 offen offset:1024 lds\n"
"v_mfma_f32_16x16x32_bf16 a[252:255], a[124:127], a[44:47], a[252:255]\n"
"ds_read_b128 a[200:203], v14 offset:2048\n"
"v_mfma_f32_16x16x32_bf16 a[248:251], a[116:119], a[44:47], a[248:251]\n"
"v_mfma_f32_16x16x32_bf16 a[248:251], a[112:115], a[40:43], a[248:251]\n"
"ds_read_b128 a[204:207], v14 offset:3072\n"
"v_mfma_f32_16x16x32_bf16 a[244:247], a[104:107], a[40:43], a[244:247]\n"
"buffer_load_dwordx4 v2, s[24:27], s65 offen offset:2048 lds\n"
"v_mfma_f32_16x16x32_bf16 a[244:247], a[108:111], a[44:47], a[244:247]\n"
"ds_read_b128 a[208:211], v14 offset:4096\n"
"v_mfma_f32_16x16x32_bf16 a[240:243], a[100:103], a[44:47], a[240:243]\n"
"v_mfma_f32_16x16x32_bf16 a[240:243], a[96:99], a[40:43], a[240:243]\n"
"ds_read_b128 a[212:215], v14 offset:5120\n"
"v_mfma_f32_16x16x32_bf16 v[40:43], a[96:99], a[48:51], v[40:43]\n"
"v_mfma_f32_16x16x32_bf16 v[40:43], a[100:103], a[52:55], v[40:43]\n"
"ds_read_b128 a[216:219], v14 offset:6144\n"
"v_mfma_f32_16x16x32_bf16 v[44:47], a[108:111], a[52:55], v[44:47]\n"
"buffer_load_dwordx4 v3, s[24:27], s65 offen offset:3072 lds\n"
"v_mfma_f32_16x16x32_bf16 v[44:47], a[104:107], a[48:51], v[44:47]\n"
"ds_read_b128 a[220:223], v14 offset:7168\n"
"v_mfma_f32_16x16x32_bf16 v[48:51], a[112:115], a[48:51], v[48:51]\n"
"s_add_u32 m0, s76, 4096\n"
"v_mfma_f32_16x16x32_bf16 v[48:51], a[116:119], a[52:55], v[48:51]\n"
"ds_read_b128 a[136:139], v12 offset:2048\n"
"v_mfma_f32_16x16x32_bf16 v[52:55], a[124:127], a[52:55], v[52:55]\n"
"buffer_load_dwordx4 v2, s[24:27], s66 offen offset:0 lds\n"
"v_mfma_f32_16x16x32_bf16 v[52:55], a[120:123], a[48:51], v[52:55]\n"
"ds_read_b128 a[140:143], v13 offset:2048\n"
"v_mfma_f32_16x16x32_bf16 v[76:79], a[120:123], a[56:59], v[76:79]\n"
"v_mfma_f32_16x16x32_bf16 v[76:79], a[124:127], a[60:63], v[76:79]\n"
"ds_read_b128 a[144:147], v12 offset:4096\n"
"v_mfma_f32_16x16x32_bf16 v[72:75], a[116:119], a[60:63], v[72:75]\n"
"buffer_load_dwordx4 v3, s[24:27], s66 offen offset:1024 lds\n"
"v_mfma_f32_16x16x32_bf16 v[72:75], a[112:115], a[56:59], v[72:75]\n"
"ds_read_b128 a[148:151], v13 offset:4096\n"
"v_mfma_f32_16x16x32_bf16 v[60:63], a[104:107], a[56:59], v[60:63]\n"
"v_mfma_f32_16x16x32_bf16 v[60:63], a[108:111], a[60:63], v[60:63]\n"
"ds_read_b128 a[152:155], v12 offset:6144\n"
"v_mfma_f32_16x16x32_bf16 v[56:59], a[100:103], a[60:63], v[56:59]\n"
"v_mfma_f32_16x16x32_bf16 v[56:59], a[96:99], a[56:59], v[56:59]\n"
"ds_read_b128 a[156:159], v13 offset:6144\n"
"v_mfma_f32_16x16x32_bf16 v[80:83], a[96:99], a[64:67], v[80:83]\n"
"buffer_load_dwordx4 v2, s[24:27], s67 offen offset:2048 lds\n"
"v_subrev_u32 v2, 1408, v2\n"
"v_mfma_f32_16x16x32_bf16 v[80:83], a[100:103], a[68:71], v[80:83]\n"
"ds_read_b128 a[160:163], v12 offset:8192\n"
"v_mfma_f32_16x16x32_bf16 v[84:87], a[108:111], a[68:71], v[84:87]\n"
"v_mfma_f32_16x16x32_bf16 v[84:87], a[104:107], a[64:67], v[84:87]\n"
"ds_read_b128 a[164:167], v13 offset:8192\n"
"v_mfma_f32_16x16x32_bf16 v[88:91], a[112:115], a[64:67], v[88:91]\n"
"buffer_load_dwordx4 v3, s[24:27], s67 offen offset:3072 lds\n"
"v_mfma_f32_16x16x32_bf16 v[88:91], a[116:119], a[68:71], v[88:91]\n"
"ds_read_b128 a[168:171], v12 offset:10240\n"
"v_subrev_u32 v3, 1408, v3\n"
"v_mfma_f32_16x16x32_bf16 v[92:95], a[124:127], a[68:71], v[92:95]\n"
"v_mfma_f32_16x16x32_bf16 v[92:95], a[120:123], a[64:67], v[92:95]\n"
"ds_read_b128 a[172:175], v13 offset:10240\n"
"s_add_u32 m0, s77, 0\n"
"v_mfma_f32_16x16x32_bf16 v[116:119], a[120:123], a[72:75], v[116:119]\n"
"buffer_load_dwordx4 v4, s[56:59], 0 offen offset:0 sc1 lds\n"
"v_mfma_f32_16x16x32_bf16 v[116:119], a[124:127], a[76:79], v[116:119]\n"
"ds_read_b128 a[176:179], v12 offset:12288\n"
"v_mfma_f32_16x16x32_bf16 v[112:115], a[116:119], a[76:79], v[112:115]\n"
"v_mfma_f32_16x16x32_bf16 v[112:115], a[112:115], a[72:75], v[112:115]\n"
"ds_read_b128 a[180:183], v13 offset:12288\n"
"v_mfma_f32_16x16x32_bf16 v[108:111], a[104:107], a[72:75], v[108:111]\n"
"v_mfma_f32_16x16x32_bf16 v[108:111], a[108:111], a[76:79], v[108:111]\n"
"ds_read_b128 a[184:187], v12 offset:14336\n"
"v_mfma_f32_16x16x32_bf16 v[104:107], a[100:103], a[76:79], v[104:107]\n"
"buffer_load_dwordx4 v4, s[56:59], 0 offen offset:1024 sc1 lds\n"
"v_mfma_f32_16x16x32_bf16 v[104:107], a[96:99], a[72:75], v[104:107]\n"
"ds_read_b128 a[188:191], v13 offset:14336\n"
"v_mfma_f32_16x16x32_bf16 v[120:123], a[96:99], a[80:83], v[120:123]\n"
"v_mfma_f32_16x16x32_bf16 v[120:123], a[100:103], a[84:87], v[120:123]\n"
"v_mfma_f32_16x16x32_bf16 v[124:127], a[108:111], a[84:87], v[124:127]\n"
"buffer_load_dwordx4 v4, s[56:59], 0 offen offset:2048 sc1 lds\n"
"v_mfma_f32_16x16x32_bf16 v[124:127], a[104:107], a[80:83], v[124:127]\n"
"v_mfma_f32_16x16x32_bf16 v[136:139], a[112:115], a[80:83], v[136:139]\n"
"v_mfma_f32_16x16x32_bf16 v[136:139], a[116:119], a[84:87], v[136:139]\n"
"v_mfma_f32_16x16x32_bf16 v[140:143], a[124:127], a[84:87], v[140:143]\n"
"v_mfma_f32_16x16x32_bf16 v[140:143], a[120:123], a[80:83], v[140:143]\n"
"buffer_load_dwordx4 v4, s[56:59], 0 offen offset:3072 sc1 lds\n"
"v_mfma_f32_16x16x32_bf16 v[156:159], a[120:123], a[88:91], v[156:159]\n"
"v_subrev_u32 v4, 1441792, v4\n"
"s_add_u32 s76, s76, s8\n"
"v_mfma_f32_16x16x32_bf16 v[156:159], a[124:127], a[92:95], v[156:159]\n"
"s_add_u32 s77, s77, s8\n"
"s_cmp_eq_u32 s7, 98304\n"
"s_cselect_b32 s8, 4294868992, s9\n"
"v_mfma_f32_16x16x32_bf16 v[152:155], a[116:119], a[92:95], v[152:155]\n"
"s_add_u32 s7, s7, s8\n"
"v_add_u32 v12, s8, v12\n"
"v_mfma_f32_16x16x32_bf16 v[152:155], a[112:115], a[88:91], v[152:155]\n"
"v_add_u32 v13, s8, v13\n"
"v_mfma_f32_16x16x32_bf16 v[148:151], a[104:107], a[88:91], v[148:151]\n"
"v_add_u32 v14, s8, v14\n"
"v_mfma_f32_16x16x32_bf16 v[148:151], a[108:111], a[92:95], v[148:151]\n"
"v_mfma_f32_16x16x32_bf16 v[144:147], a[100:103], a[92:95], v[144:147]\n"
"v_mfma_f32_16x16x32_bf16 v[144:147], a[96:99], a[88:91], v[144:147]\n"
"s_waitcnt lgkmcnt(14)\n"
"v_mfma_f32_16x16x32_bf16 a[224:227], a[192:195], a[128:131], a[224:227]\n"
"s_waitcnt vmcnt(13) lgkmcnt(0)\n"
"s_barrier\n"
"v_mfma_f32_16x16x32_bf16 a[224:227], a[196:199], a[132:135], a[224:227]\n"
"s_cmp_eq_u32 s13, 0\n"
"s_cbranch_scc1 EV9_%=\n"
"EV9r_%=:\n"
"v_mfma_f32_16x16x32_bf16 a[228:231], a[204:207], a[132:135], a[228:231]\n"
"s_waitcnt vmcnt(12)\n"
"v_cmp_ne_u32 vcc, 1, v11\n"
"v_mfma_f32_16x16x32_bf16 a[228:231], a[200:203], a[128:131], a[228:231]\n"
"s_nop 4\n"
"s_cbranch_vccnz PL11_%=\n"
"PL11d_%=:\n"
"v_mfma_f32_16x16x32_bf16 a[232:235], a[208:211], a[128:131], a[232:235]\n"
"ds_read_b128 a[32:35], v12 offset:0\n"
"s_add_u32 m0, s76, 0\n"
"v_mfma_f32_16x16x32_bf16 a[232:235], a[212:215], a[132:135], a[232:235]\n"
"ds_read_b128 a[36:39], v13 offset:0\n"
"v_mfma_f32_16x16x32_bf16 a[236:239], a[220:223], a[132:135], a[236:239]\n"
"buffer_load_dwordx4 v2, s[24:27], s64 offen offset:0 lds\n"
"v_mfma_f32_16x16x32_bf16 a[236:239], a[216:219], a[128:131], a[236:239]\n"
"ds_read_b128 a[96:99], v14 offset:0\n"
"v_mfma_f32_16x16x32_bf16 a[252:255], a[216:219], a[136:139], a[252:255]\n"
"ds_read_b128 a[100:103], v14 offset:1024\n"
"v_mfma_f32_16x16x32_bf16 a[252:255], a[220:223], a[140:143], a[252:255]\n"
"buffer_load_dwordx4 v3, s[24:27], s64 offen offset:1024 lds\n"
"v_mfma_f32_16x16x32_bf16 a[248:251], a[212:215], a[140:143], a[248:251]\n"
"ds_read_b128 a[104:107], v14 offset:2048\n"
"v_mfma_f32_16x16x32_bf16 a[248:251], a[208:211], a[136:139], a[248:251]\n"
"ds_read_b128 a[108:111], v14 offset:3072\n"
"v_mfma_f32_16x16x32_bf16 a[244:247], a[200:203], a[136:139], a[244:247]\n"
"buffer_load_dwordx4 v2, s[24:27], s65 offen offset:2048 lds\n"
"v_mfma_f32_16x16x32_bf16 a[244:247], a[204:207], a[140:143], a[244:247]\n"
"ds_read_b128 a[112:115], v14 offset:4096\n"
"v_mfma_f32_16x16x32_bf16 a[240:243], a[196:199], a[140:143], a[240:243]\n"
"v_mfma_f32_16x16x32_bf16 a[240:243], a[192:195], a[136:139], a[240:243]\n"
"ds_read_b128 a[116:119], v14 offset:5120\n"
"v_mfma_f32_16x16x32_bf16 v[40:43], a[192:195], a[144:147], v[40:43]\n"
"v_mfma_f32_16x16x32_bf16 v[40:43], a[196:199], a[148:151], v[40:43]\n"
"ds_read_b128 a[120:123], v14 offset:6144\n"
"v_mfma_f32_16x16x32_bf16 v[44:47], a[204:207], a[148:151], v[44:47]\n"
"buffer_load_dwordx4 v3, s[24:27], s65 offen offset:3072 lds\n"
"v_mfma_f32_16x16x32_bf16 v[44:47], a[200:203], a[144:147], v[44:47]\n"
"ds_read_b128 a[124:127], v14 offset:7168\n"
"v_mfma_f32_16x16x32_bf16 v[48:51], a[208:211], a[144:147], v[48:51]\n"
"s_add_u32 m0, s76, 4096\n"
"v_mfma_f32_16x16x32_bf16 v[48:51], a[212:215], a[148:151], v[48:51]\n"
"ds_read_b128 a[40:43], v12 offset:2048\n"
"v_mfma_f32_16x16x32_bf16 v[52:55], a[220:223], a[148:151], v[52:55]\n"
"buffer_load_dwordx4 v2, s[24:27], s66 offen offset:0 lds\n"
"v_mfma_f32_16x16x32_bf16 v[52:55], a[216:219], a[144:147], v[52:55]\n"
"ds_read_b128 a[44:47], v13 offset:2048\n"
"v_mfma_f32_16x16x32_bf16 v[76:79], a[216:219], a[152:155], v[76:79]\n"
"v_mfma_f32_16x16x32_bf16 v[76:79], a[220:223], a[156:159], v[76:79]\n"
"ds_read_b128 a[48:51], v12 offset:4096\n"
"v_mfma_f32_16x16x32_bf16 v[72:75], a[212:215], a[156:159], v[72:75]\n"
"buffer_load_dwordx4 v3, s[24:27], s66 offen offset:1024 lds\n"
"v_mfma_f32_16x16x32_bf16 v[72:75], a[208:211], a[152:155], v[72:75]\n"
"ds_read_b128 a[52:55], v13 offset:4096\n"
"v_mfma_f32_16x16x32_bf16 v[60:63], a[200:203], a[152:155], v[60:63]\n"
"v_mfma_f32_16x16x32_bf16 v[60:63], a[204:207], a[156:159], v[60:63]\n"
"ds_read_b128 a[56:59], v12 offset:6144\n"
"v_mfma_f32_16x16x32_bf16 v[56:59], a[196:199], a[156:159], v[56:59]\n"
"v_mfma_f32_16x16x32_bf16 v[56:59], a[192:195], a[152:155], v[56:59]\n"
"ds_read_b128 a[60:63], v13 offset:6144\n"
"v_mfma_f32_16x16x32_bf16 v[80:83], a[192:195], a[160:163], v[80:83]\n"
"buffer_load_dwordx4 v2, s[24:27], s67 offen offset:2048 lds\n"
"v_add_u32 v2, 512, v2\n"
"v_mfma_f32_16x16x32_bf16 v[80:83], a[196:199], a[164:167], v[80:83]\n"
"ds_read_b128 a[64:67], v12 offset:8192\n"
"v_mfma_f32_16x16x32_bf16 v[84:87], a[204:207], a[164:167], v[84:87]\n"
"v_mfma_f32_16x16x32_bf16 v[84:87], a[200:203], a[160:163], v[84:87]\n"
"ds_read_b128 a[68:71], v13 offset:8192\n"
"v_mfma_f32_16x16x32_bf16 v[88:91], a[208:211], a[160:163], v[88:91]\n"
"buffer_load_dwordx4 v3, s[24:27], s67 offen offset:3072 lds\n"
"v_mfma_f32_16x16x32_bf16 v[88:91], a[212:215], a[164:167], v[88:91]\n"
"ds_read_b128 a[72:75], v12 offset:10240\n"
"v_add_u32 v3, 512, v3\n"
"v_mfma_f32_16x16x32_bf16 v[92:95], a[220:223], a[164:167], v[92:95]\n"
"v_mfma_f32_16x16x32_bf16 v[92:95], a[216:219], a[160:163], v[92:95]\n"
"ds_read_b128 a[76:79], v13 offset:10240\n"
"s_add_u32 m0, s77, 0\n"
"v_mfma_f32_16x16x32_bf16 v[116:119], a[216:219], a[168:171], v[116:119]\n"
"buffer_load_dwordx4 v4, s[56:59], 0 offen offset:0 sc1 lds\n"
"v_mfma_f32_16x16x32_bf16 v[116:119], a[220:223], a[172:175], v[116:119]\n"
"ds_read_b128 a[80:83], v12 offset:12288\n"
"v_mfma_f32_16x16x32_bf16 v[112:115], a[212:215], a[172:175], v[112:115]\n"
"v_mfma_f32_16x16x32_bf16 v[112:115], a[208:211], a[168:171], v[112:115]\n"
"ds_read_b128 a[84:87], v13 offset:12288\n"
"v_mfma_f32_16x16x32_bf16 v[108:111], a[200:203], a[168:171], v[108:111]\n"
"v_mfma_f32_16x16x32_bf16 v[108:111], a[204:207], a[172:175], v[108:111]\n"
"ds_read_b128 a[88:91], v12 offset:14336\n"
"v_mfma_f32_16x16x32_bf16 v[104:107], a[196:199], a[172:175], v[104:107]\n"
"buffer_load_dwordx4 v4, s[56:59], 0 offen offset:1024 sc1 lds\n"
"v_mfma_f32_16x16x32_bf16 v[104:107], a[192:195], a[168:171], v[104:107]\n"
"ds_read_b128 a[92:95], v13 offset:14336\n"
"v_mfma_f32_16x16x32_bf16 v[120:123], a[192:195], a[176:179], v[120:123]\n"
"v_mfma_f32_16x16x32_bf16 v[120:123], a[196:199], a[180:183], v[120:123]\n"
"v_mfma_f32_16x16x32_bf16 v[124:127], a[204:207], a[180:183], v[124:127]\n"
"buffer_load_dwordx4 v4, s[56:59], 0 offen offset:2048 sc1 lds\n"
"v_mfma_f32_16x16x32_bf16 v[124:127], a[200:203], a[176:179], v[124:127]\n"
"v_mfma_f32_16x16x32_bf16 v[136:139], a[208:211], a[176:179], v[136:139]\n"
"v_mfma_f32_16x16x32_bf16 v[136:139], a[212:215], a[180:183], v[136:139]\n"
"v_mfma_f32_16x16x32_bf16 v[140:143], a[220:223], a[180:183], v[140:143]\n"
"v_mfma_f32_16x16x32_bf16 v[140:143], a[216:219], a[176:179], v[140:143]\n"
"buffer_load_dwordx4 v4, s[56:59], 0 offen offset:3072 sc1 lds\n"
"v_mfma_f32_16x16x32_bf16 v[156:159], a[216:219], a[184:187], v[156:159]\n"
"v_add_u32 v4, 524288, v4\n"
"s_add_u32 s76, s76, s8\n"
"v_mfma_f32_16x16x32_bf16 v[156:159], a[220:223], a[188:191], v[156:159]\n"
"s_add_u32 s77, s77, s8\n"
"s_cmp_eq_u32 s7, 98304\n"
"s_cselect_b32 s8, 4294868992, s9\n"
"v_mfma_f32_16x16x32_bf16 v[152:155], a[212:215], a[188:191], v[152:155]\n"
"s_add_u32 s7, s7, s8\n"
"v_add_u32 v12, s8, v12\n"
"v_mfma_f32_16x16x32_bf16 v[152:155], a[208:211], a[184:187], v[152:155]\n"
"v_add_u32 v13, s8, v13\n"
"v_mfma_f32_16x16x32_bf16 v[148:151], a[200:203], a[184:187], v[148:151]\n"
"v_add_u32 v14, s8, v14\n"
"v_mfma_f32_16x16x32_bf16 v[148:151], a[204:207], a[188:191], v[148:151]\n"
"v_mfma_f32_16x16x32_bf16 v[144:147], a[196:199], a[188:191], v[144:147]\n"
"v_mfma_f32_16x16x32_bf16 v[144:147], a[192:195], a[184:187], v[144:147]\n"
"s_waitcnt lgkmcnt(14)\n"
"v_mfma_f32_16x16x32_bf16 a[224:227], a[96:99], a[32:35], a[224:227]\n"
"s_waitcnt vmcnt(12) lgkmcnt(0)\n"
"s_barrier\n"
"v_mfma_f32_16x16x32_bf16 a[224:227], a[100:103], a[36:39], a[224:227]\n"
"ds_read_b128 a[128:131], v12 offset:0\n"
"v_mfma_f32_16x16x32_bf16 a[228:231], a[108:111], a[36:39], a[228:231]\n"
"s_add_u32 m0, s76, 0\n"
"v_mfma_f32_16x16x32_bf16 a[228:231], a[104:107], a[32:35], a[228:231]\n"
"ds_read_b128 a[132:135], v13 offset:0\n"
"v_mfma_f32_16x16x32_bf16 a[232:235], a[112:115], a[32:35], a[232:235]\n"
"buffer_load_dwordx4 v2, s[24:27], s64 offen offset:0 lds\n"
"v_mfma_f32_16x16x32_bf16 a[232:235], a[116:119], a[36:39], a[232:235]\n"
"ds_read_b128 a[192:195], v14 offset:0\n"
"v_mfma_f32_16x16x32_bf16 a[236:239], a[124:127], a[36:39], a[236:239]\n"
"v_mfma_f32_16x16x32_bf16 a[236:239], a[120:123], a[32:35], a[236:239]\n"
"ds_read_b128 a[196:199], v14 offset:1024\n"
"v_mfma_f32_16x16x32_bf16 a[252:255], a[120:123], a[40:43], a[252:255]\n"
"buffer_load_dwordx4 v3, s[24:27], s64 offen offset:1024 lds\n"
"v_mfma_f32_16x16x32_bf16 a[252:255], a[124:127], a[44:47], a[252:255]\n"
"ds_read_b128 a[200:203], v14 offset:2048\n"
"v_mfma_f32_16x16x32_bf16 a[248:251], a[116:119], a[44:47], a[248:251]\n"
"v_mfma_f32_16x16x32_bf16 a[248:251], a[112:115], a[40:43], a[248:251]\n"
"ds_read_b128 a[204:207], v14 offset:3072\n"
"v_mfma_f32_16x16x32_bf16 a[244:247], a[104:107], a[40:43], a[244:247]\n"
"buffer_load_dwordx4 v2, s[24:27], s65 offen offset:2048 lds\n"
"v_mfma_f32_16x16x32_bf16 a[244:247], a[108:111], a[44:47], a[244:247]\n"
"ds_read_b128 a[208:211], v14 offset:4096\n"
"v_mfma_f32_16x16x32_bf16 a[240:243], a[100:103], a[44:47], a[240:243]\n"
"v_mfma_f32_16x16x32_bf16 a[240:243], a[96:99], a[40:43], a[240:243]\n"
"ds_read_b128 a[212:215], v14 offset:5120\n"
"v_mfma_f32_16x16x32_bf16 v[40:43], a[96:99], a[48:51], v[40:43]\n"
"v_mfma_f32_16x16x32_bf16 v[40:43], a[100:103], a[52:55], v[40:43]\n"
"ds_read_b128 a[216:219], v14 offset:6144\n"
"v_mfma_f32_16x16x32_bf16 v[44:47], a[108:111], a[52:55], v[44:47]\n"
"buffer_load_dwordx4 v3, s[24:27], s65 offen offset:3072 lds\n"
"v_mfma_f32_16x16x32_bf16 v[44:47], a[104:107], a[48:51], v[44:47]\n"
"ds_read_b128 a[220:223], v14 offset:7168\n"
"v_mfma_f32_16x16x32_bf16 v[48:51], a[112:115], a[48:51], v[48:51]\n"
"s_add_u32 m0, s76, 4096\n"
"v_mfma_f32_16x16x32_bf16 v[48:51], a[116:119], a[52:55], v[48:51]\n"
"ds_read_b128 a[136:139], v12 offset:2048\n"
"v_mfma_f32_16x16x32_bf16 v[52:55], a[124:127], a[52:55], v[52:55]\n"
"buffer_load_dwordx4 v2, s[24:27], s66 offen offset:0 lds\n"
"v_mfma_f32_16x16x32_bf16 v[52:55], a[120:123], a[48:51], v[52:55]\n"
"ds_read_b128 a[140:143], v13 offset:2048\n"
"v_mfma_f32_16x16x32_bf16 v[76:79], a[120:123], a[56:59], v[76:79]\n"
"v_mfma_f32_16x16x32_bf16 v[76:79], a[124:127], a[60:63], v[76:79]\n"
"ds_read_b128 a[144:147], v12 offset:4096\n"
"v_mfma_f32_16x16x32_bf16 v[72:75], a[116:119], a[60:63], v[72:75]\n"
"buffer_load_dwordx4 v3, s[24:27], s66 offen offset:1024 lds\n"
"v_mfma_f32_16x16x32_bf16 v[72:75], a[112:115], a[56:59], v[72:75]\n"
"ds_read_b128 a[148:151], v13 offset:4096\n"
"v_mfma_f32_16x16x32_bf16 v[60:63], a[104:107], a[56:59], v[60:63]\n"
"v_mfma_f32_16x16x32_bf16 v[60:63], a[108:111], a[60:63], v[60:63]\n"
"ds_read_b128 a[152:155], v12 offset:6144\n"
"v_mfma_f32_16x16x32_bf16 v[56:59], a[100:103], a[60:63], v[56:59]\n"
"v_mfma_f32_16x16x32_bf16 v[56:59], a[96:99], a[56:59], v[56:59]\n"
"ds_read_b128 a[156:159], v13 offset:6144\n"
"v_mfma_f32_16x16x32_bf16 v[80:83], a[96:99], a[64:67], v[80:83]\n"
"buffer_load_dwordx4 v2, s[24:27], s67 offen offset:2048 lds\n"
"v_add_u32 v2, 512, v2\n"
"v_mfma_f32_16x16x32_bf16 v[80:83], a[100:103], a[68:71], v[80:83]\n"
"ds_read_b128 a[160:163], v12 offset:8192\n"
"v_mfma_f32_16x16x32_bf16 v[84:87], a[108:111], a[68:71], v[84:87]\n"
"v_mfma_f32_16x16x32_bf16 v[84:87], a[104:107], a[64:67], v[84:87]\n"
"ds_read_b128 a[164:167], v13 offset:8192\n"
"v_mfma_f32_16x16x32_bf16 v[88:91], a[112:115], a[64:67], v[88:91]\n"
"buffer_load_dwordx4 v3, s[24:27], s67 offen offset:3072 lds\n"
"v_mfma_f32_16x16x32_bf16 v[88:91], a[116:119], a[68:71], v[88:91]\n"
"ds_read_b128 a[168:171], v12 offset:10240\n"
"v_add_u32 v3, 512, v3\n"
"v_mfma_f32_16x16x32_bf16 v[92:95], a[124:127], a[68:71], v[92:95]\n"
"v_mfma_f32_16x16x32_bf16 v[92:95], a[120:123], a[64:67], v[92:95]\n"
"ds_read_b128 a[172:175], v13 offset:10240\n"
"s_add_u32 m0, s77, 0\n"
"v_mfma_f32_16x16x32_bf16 v[116:119], a[120:123], a[72:75], v[116:119]\n"
"buffer_load_dwordx4 v4, s[56:59], 0 offen offset:0 sc1 lds\n"
"v_mfma_f32_16x16x32_bf16 v[116:119], a[124:127], a[76:79], v[116:119]\n"
"ds_read_b128 a[176:179], v12 offset:12288\n"
"v_mfma_f32_16x16x32_bf16 v[112:115], a[116:119], a[76:79], v[112:115]\n"
"v_mfma_f32_16x16x32_bf16 v[112:115], a[112:115], a[72:75], v[112:115]\n"
"ds_read_b128 a[180:183], v13 offset:12288\n"
"v_mfma_f32_16x16x32_bf16 v[108:111], a[104:107], a[72:75], v[108:111]\n"
"v_mfma_f32_16x16x32_bf16 v[108:111], a[108:111], a[76:79], v[108:111]\n"
"ds_read_b128 a[184:187], v12 offset:14336\n"
"v_mfma_f32_16x16x32_bf16 v[104:107], a[100:103], a[76:79], v[104:107]\n"
"buffer_load_dwordx4 v4, s[56:59], 0 offen offset:1024 sc1 lds\n"
"v_mfma_f32_16x16x32_bf16 v[104:107], a[96:99], a[72:75], v[104:107]\n"
"ds_read_b128 a[188:191], v13 offset:14336\n"
"v_mfma_f32_16x16x32_bf16 v[120:123], a[96:99], a[80:83], v[120:123]\n"
"v_mfma_f32_16x16x32_bf16 v[120:123], a[100:103], a[84:87], v[120:123]\n"
"v_mfma_f32_16x16x32_bf16 v[124:127], a[108:111], a[84:87], v[124:127]\n"
"buffer_load_dwordx4 v4, s[56:59], 0 offen offset:2048 sc1 lds\n"
"v_mfma_f32_16x16x32_bf16 v[124:127], a[104:107], a[80:83], v[124:127]\n"
"v_mfma_f32_16x16x32_bf16 v[136:139], a[112:115], a[80:83], v[136:139]\n"
"v_mfma_f32_16x16x32_bf16 v[136:139], a[116:119], a[84:87], v[136:139]\n"
"v_mfma_f32_16x16x32_bf16 v[140:143], a[124:127], a[84:87], v[140:143]\n"
"v_mfma_f32_16x16x32_bf16 v[140:143], a[120:123], a[80:83], v[140:143]\n"
"buffer_load_dwordx4 v4, s[56:59], 0 offen offset:3072 sc1 lds\n"
"v_mfma_f32_16x16x32_bf16 v[156:159], a[120:123], a[88:91], v[156:159]\n"
"v_add_u32 v4, 524288, v4\n"
"s_add_u32 s76, s76, s8\n"
"v_mfma_f32_16x16x32_bf16 v[156:159], a[124:127], a[92:95], v[156:159]\n"
"s_add_u32 s77, s77, s8\n"
"s_cmp_eq_u32 s7, 98304\n"
"s_cselect_b32 s8, 4294868992, s9\n"
"v_mfma_f32_16x16x32_bf16 v[152:155], a[116:119], a[92:95], v[152:155]\n"
"s_add_u32 s7, s7, s8\n"
"v_add_u32 v12, s8, v12\n"
"v_mfma_f32_16x16x32_bf16 v[152:155], a[112:115], a[88:91], v[152:155]\n"
"v_add_u32 v13, s8, v13\n"
"v_mfma_f32_16x16x32_bf16 v[148:151], a[104:107], a[88:91], v[148:151]\n"
"v_add_u32 v14, s8, v14\n"
"v_mfma_f32_16x16x32_bf16 v[148:151], a[108:111], a[92:95], v[148:151]\n"
"v_mfma_f32_16x16x32_bf16 v[144:147], a[100:103], a[92:95], v[144:147]\n"
"v_mfma_f32_16x16x32_bf16 v[144:147], a[96:99], a[88:91], v[144:147]\n"
"s_waitcnt lgkmcnt(14)\n"
"v_mfma_f32_16x16x32_bf16 a[224:227], a[192:195], a[128:131], a[224:227]\n"
"s_waitcnt vmcnt(12) lgkmcnt(0)\n"
"s_barrier\n"
"v_mfma_f32_16x16x32_bf16 a[224:227], a[196:199], a[132:135], a[224:227]\n"
"ds_read_b128 a[32:35], v12 offset:0\n"
"v_mfma_f32_16x16x32_bf16 a[228:231], a[204:207], a[132:135], a[228:231]\n"
"s_add_u32 m0, s76, 0\n"
"v_mfma_f32_16x16x32_bf16 a[228:231], a[200:203], a[128:131], a[228:231]\n"
"ds_read_b128 a[36:39], v13 offset:0\n"
"v_mfma_f32_16x16x32_bf16 a[232:235], a[208:211], a[128:131], a[232:235]\n"
"buffer_load_dwordx4 v2, s[24:27], s64 offen offset:0 lds\n"
"v_mfma_f32_16x16x32_bf16 a[232:235], a[212:215], a[132:135], a[232:235]\n"
"ds_read_b128 a[96:99], v14 offset:0\n"
"v_mfma_f32_16x16x32_bf16 a[236:239], a[220:223], a[132:135], a[236:239]\n"
"v_mfma_f32_16x16x32_bf16 a[236:239], a[216:219], a[128:131], a[236:239]\n"
"ds_read_b128 a[100:103], v14 offset:1024\n"
"v_mfma_f32_16x16x32_bf16 a[252:255], a[216:219], a[136:139], a[252:255]\n"
"buffer_load_dwordx4 v3, s[24:27], s64 offen offset:1024 lds\n"
"v_mfma_f32_16x16x32_bf16 a[252:255], a[220:223], a[140:143], a[252:255]\n"
"ds_read_b128 a[104:107], v14 offset:2048\n"
"v_mfma_f32_16x16x32_bf16 a[248:251], a[212:215], a[140:143], a[248:251]\n"
"v_mfma_f32_16x16x32_bf16 a[248:251], a[208:211], a[136:139], a[248:251]\n"
"ds_read_b128 a[108:111], v14 offset:3072\n"
"v_mfma_f32_16x16x32_bf16 a[244:247], a[200:203], a[136:139], a[244:247]\n"
"buffer_load_dwordx4 v2, s[24:27], s65 offen offset:2048 lds\n"
"v_mfma_f32_16x16x32_bf16 a[244:247], a[204:207], a[140:143], a[244:247]\n"
"ds_read_b128 a[112:115], v14 offset:4096\n"
"v_mfma_f32_16x16x32_bf16 a[240:243], a[196:199], a[140:143], a[240:243]\n"
"v_mfma_f32_16x16x32_bf16 a[240:243], a[192:195], a[136:139], a[240:243]\n"
"ds_read_b128 a[116:119], v14 offset:5120\n"
"v_mfma_f32_16x16x32_bf16 v[40:43], a[192:195], a[144:147], v[40:43]\n"
"v_mfma_f32_16x16x32_bf16 v[40:43], a[196:199], a[148:151], v[40:43]\n"
"ds_read_b128 a[120:123], v14 offset:6144\n"
"v_mfma_f32_16x16x32_bf16 v[44:47], a[204:207], a[148:151], v[44:47]\n"
"buffer_load_dwordx4 v3, s[24:27], s65 offen offset:3072 lds\n"
"v_mfma_f32_16x16x32_bf16 v[44:47], a[200:203], a[144:147], v[44:47]\n"
"ds_read_b128 a[124:127], v14 offset:7168\n"
"v_mfma_f32_16x16x32_bf16 v[48:51], a[208:211], a[144:147], v[48:51]\n"
"s_add_u32 m0, s76, 4096\n"
"v_mfma_f32_16x16x32_bf16 v[48:51], a[212:215], a[148:151], v[48:51]\n"
"ds_read_b128 a[40:43], v12 offset:2048\n"
"v_mfma_f32_16x16x32_bf16 v[52:55], a[220:223], a[148:151], v[52:55]\n"
"buffer_load_dwordx4 v2, s[24:27], s66 offen offset:0 lds\n"
"v_mfma_f32_16x16x32_bf16 v[52:55], a[216:219], a[144:147], v[52:55]\n"
"ds_read_b128 a[44:47], v13 offset:2048\n"
"v_mfma_f32_16x16x32_bf16 v[76:79], a[216:219], a[152:155], v[76:79]\n"
"v_mfma_f32_16x16x32_bf16 v[76:79], a[220:223], a[156:159], v[76:79]\n"
"ds_read_b128 a[48:51], v12 offset:4096\n"
"v_mfma_f32_16x16x32_bf16 v[72:75], a[212:215], a[156:159], v[72:75]\n"
"buffer_load_dwordx4 v3, s[24:27], s66 offen offset:1024 lds\n"
"v_mfma_f32_16x16x32_bf16 v[72:75], a[208:211], a[152:155], v[72:75]\n"
"ds_read_b128 a[52:55], v13 offset:4096\n"
"v_mfma_f32_16x16x32_bf16 v[60:63], a[200:203], a[152:155], v[60:63]\n"
"v_mfma_f32_16x16x32_bf16 v[60:63], a[204:207], a[156:159], v[60:63]\n"
"ds_read_b128 a[56:59], v12 offset:6144\n"
"v_mfma_f32_16x16x32_bf16 v[56:59], a[196:199], a[156:159], v[56:59]\n"
"v_mfma_f32_16x16x32_bf16 v[56:59], a[192:195], a[152:155], v[56:59]\n"
"ds_read_b128 a[60:63], v13 offset:6144\n"
"v_mfma_f32_16x16x32_bf16 v[80:83], a[192:195], a[160:163], v[80:83]\n"
"buffer_load_dwordx4 v2, s[24:27], s67 offen offset:2048 lds\n"
"v_add_u32 v2, 512, v2\n"
"v_mfma_f32_16x16x32_bf16 v[80:83], a[196:199], a[164:167], v[80:83]\n"
"ds_read_b128 a[64:67], v12 offset:8192\n"
"v_mfma_f32_16x16x32_bf16 v[84:87], a[204:207], a[164:167], v[84:87]\n"
"v_mfma_f32_16x16x32_bf16 v[84:87], a[200:203], a[160:163], v[84:87]\n"
"ds_read_b128 a[68:71], v13 offset:8192\n"
"v_mfma_f32_16x16x32_bf16 v[88:91], a[208:211], a[160:163], v[88:91]\n"
"buffer_load_dwordx4 v3, s[24:27], s67 offen offset:3072 lds\n"
"v_mfma_f32_16x16x32_bf16 v[88:91], a[212:215], a[164:167], v[88:91]\n"
"ds_read_b128 a[72:75], v12 offset:10240\n"
"v_add_u32 v3, 512, v3\n"
"v_mfma_f32_16x16x32_bf16 v[92:95], a[220:223], a[164:167], v[92:95]\n"
"v_mfma_f32_16x16x32_bf16 v[92:95], a[216:219], a[160:163], v[92:95]\n"
"ds_read_b128 a[76:79], v13 offset:10240\n"
"s_add_u32 m0, s77, 0\n"
"v_mfma_f32_16x16x32_bf16 v[116:119], a[216:219], a[168:171], v[116:119]\n"
"buffer_load_dwordx4 v4, s[56:59], 0 offen offset:0 sc1 lds\n"
"v_mfma_f32_16x16x32_bf16 v[116:119], a[220:223], a[172:175], v[116:119]\n"
"ds_read_b128 a[80:83], v12 offset:12288\n"
"v_mfma_f32_16x16x32_bf16 v[112:115], a[212:215], a[172:175], v[112:115]\n"
"v_mfma_f32_16x16x32_bf16 v[112:115], a[208:211], a[168:171], v[112:115]\n"
"ds_read_b128 a[84:87], v13 offset:12288\n"
"v_mfma_f32_16x16x32_bf16 v[108:111], a[200:203], a[168:171], v[108:111]\n"
"v_mfma_f32_16x16x32_bf16 v[108:111], a[204:207], a[172:175], v[108:111]\n"
"ds_read_b128 a[88:91], v12 offset:14336\n"
"v_mfma_f32_16x16x32_bf16 v[104:107], a[196:199], a[172:175], v[104:107]\n"
"buffer_load_dwordx4 v4, s[56:59], 0 offen offset:1024 sc1 lds\n"
"v_mfma_f32_16x16x32_bf16 v[104:107], a[192:195], a[168:171], v[104:107]\n"
"ds_read_b128 a[92:95], v13 offset:14336\n"
"v_mfma_f32_16x16x32_bf16 v[120:123], a[192:195], a[176:179], v[120:123]\n"
"v_mfma_f32_16x16x32_bf16 v[120:123], a[196:199], a[180:183], v[120:123]\n"
"v_mfma_f32_16x16x32_bf16 v[124:127], a[204:207], a[180:183], v[124:127]\n"
"buffer_load_dwordx4 v4, s[56:59], 0 offen offset:2048 sc1 lds\n"
"v_mfma_f32_16x16x32_bf16 v[124:127], a[200:203], a[176:179], v[124:127]\n"
"v_mfma_f32_16x16x32_bf16 v[136:139], a[208:211], a[176:179], v[136:139]\n"
"v_mfma_f32_16x16x32_bf16 v[136:139], a[212:215], a[180:183], v[136:139]\n"
"v_mfma_f32_16x16x32_bf16 v[140:143], a[220:223], a[180:183], v[140:143]\n"
"v_mfma_f32_16x16x32_bf16 v[140:143], a[216:219], a[176:179], v[140:143]\n"
"buffer_load_dwordx4 v4, s[56:59], 0 offen offset:3072 sc1 lds\n"
"v_mfma_f32_16x16x32_bf16 v[156:159], a[216:219], a[184:187], v[156:159]\n"
"v_add_u32 v4, 524288, v4\n"
"s_add_u32 s76, s76, s8\n"
"v_mfma_f32_16x16x32_bf16 v[156:159], a[220:223], a[188:191], v[156:159]\n"
"s_add_u32 s77, s77, s8\n"
"s_cmp_eq_u32 s7, 98304\n"
"s_cselect_b32 s8, 4294868992, s9\n"
"v_mfma_f32_16x16x32_bf16 v[152:155], a[212:215], a[188:191], v[152:155]\n"
"s_add_u32 s7, s7, s8\n"
"v_add_u32 v12, s8, v12\n"
"v_mfma_f32_16x16x32_bf16 v[152:155], a[208:211], a[184:187], v[152:155]\n"
"v_add_u32 v13, s8, v13\n"
"v_mfma_f32_16x16x32_bf16 v[148:151], a[200:203], a[184:187], v[148:151]\n"
"v_add_u32 v14, s8, v14\n"
"v_mfma_f32_16x16x32_bf16 v[148:151], a[204:207], a[188:191], v[148:151]\n"
"v_mfma_f32_16x16x32_bf16 v[144:147], a[196:199], a[188:191], v[144:147]\n"
"v_mfma_f32_16x16x32_bf16 v[144:147], a[192:195], a[184:187], v[144:147]\n"
"s_waitcnt lgkmcnt(14)\n"
"v_mfma_f32_16x16x32_bf16 a[224:227], a[96:99], a[32:35], a[224:227]\n"
"s_waitcnt vmcnt(12) lgkmcnt(0)\n"
"s_barrier\n"
"v_mfma_f32_16x16x32_bf16 a[224:227], a[100:103], a[36:39], a[224:227]\n"
"buffer_load_dword v11, v10, s[56:59], 0 offen offset:12 sc1\n"
"v_mfma_f32_16x16x32_bf16 a[228:231], a[108:111], a[36:39], a[228:231]\n"
"ds_read_b128 a[128:131], v12 offset:0\n"
"s_add_u32 m0, s76, 0\n"
"v_mfma_f32_16x16x32_bf16 a[228:231], a[104:107], a[32:35], a[228:231]\n"
"ds_read_b128 a[132:135], v13 offset:0\n"
"v_mfma_f32_16x16x32_bf16 a[232:235], a[112:115], a[32:35], a[232:235]\n"
"buffer_load_dwordx4 v2, s[24:27], s64 offen offset:0 lds\n"
"v_mfma_f32_16x16x32_bf16 a[232:235], a[116:119], a[36:39], a[232:235]\n"
"ds_read_b128 a[192:195], v14 offset:0\n"
"v_mfma_f32_16x16x32_bf16 a[236:239], a[124:127], a[36:39], a[236:239]\n"
"v_mfma_f32_16x16x32_bf16 a[236:239], a[120:123], a[32:35], a[236:239]\n"
"ds_read_b128 a[196:199], v14 offset:1024\n"
"v_mfma_f32_16x16x32_bf16 a[252:255], a[120:123], a[40:43], a[252:255]\n"
"buffer_load_dwordx4 v3, s[24:27], s64 offen offset:1024 lds\n"
"v_mfma_f32_16x16x32_bf16 a[252:255], a[124:127], a[44:47], a[252:255]\n"
"ds_read_b128 a[200:203], v14 offset:2048\n"
"v_mfma_f32_16x16x32_bf16 a[248:251], a[116:119], a[44:47], a[248:251]\n"
"v_mfma_f32_16x16x32_bf16 a[248:251], a[112:115], a[40:43], a[248:251]\n"
"ds_read_b128 a[204:207], v14 offset:3072\n"
"v_mfma_f32_16x16x32_bf16 a[244:247], a[104:107], a[40:43], a[244:247]\n"
"buffer_load_dwordx4 v2, s[24:27], s65 offen offset:2048 lds\n"
"v_mfma_f32_16x16x32_bf16 a[244:247], a[108:111], a[44:47], a[244:247]\n"
"ds_read_b128 a[208:211], v14 offset:4096\n"
"v_mfma_f32_16x16x32_bf16 a[240:243], a[100:103], a[44:47], a[240:243]\n"
"v_mfma_f32_16x16x32_bf16 a[240:243], a[96:99], a[40:43], a[240:243]\n"
"ds_read_b128 a[212:215], v14 offset:5120\n"
"v_mfma_f32_16x16x32_bf16 v[40:43], a[96:99], a[48:51], v[40:43]\n"
"v_mfma_f32_16x16x32_bf16 v[40:43], a[100:103], a[52:55], v[40:43]\n"
"ds_read_b128 a[216:219], v14 offset:6144\n"
"v_mfma_f32_16x16x32_bf16 v[44:47], a[108:111], a[52:55], v[44:47]\n"
"buffer_load_dwordx4 v3, s[24:27], s65 offen offset:3072 lds\n"
"v_mfma_f32_16x16x32_bf16 v[44:47], a[104:107], a[48:51], v[44:47]\n"
"ds_read_b128 a[220:223], v14 offset:7168\n"
"v_mfma_f32_16x16x32_bf16 v[48:51], a[112:115], a[48:51], v[48:51]\n"
"s_add_u32 m0, s76, 4096\n"
"v_mfma_f32_16x16x32_bf16 v[48:51], a[116:119], a[52:55], v[48:51]\n"
"ds_read_b128 a[136:139], v12 offset:2048\n"
"v_mfma_f32_16x16x32_bf16 v[52:55], a[124:127], a[52:55], v[52:55]\n"
"buffer_load_dwordx4 v2, s[24:27], s66 offen offset:0 lds\n"
"v_mfma_f32_16x16x32_bf16 v[52:55], a[120:123], a[48:51], v[52:55]\n"
"ds_read_b128 a[140:143], v13 offset:2048\n"
"v_mfma_f32_16x16x32_bf16 v[76:79], a[120:123], a[56:59], v[76:79]\n"
"v_mfma_f32_16x16x32_bf16 v[76:79], a[124:127], a[60:63], v[76:79]\n"
"ds_read_b128 a[144:147], v12 offset:4096\n"
"v_mfma_f32_16x16x32_bf16 v[72:75], a[116:119], a[60:63], v[72:75]\n"
"buffer_load_dwordx4 v3, s[24:27], s66 offen offset:1024 lds\n"
"v_mfma_f32_16x16x32_bf16 v[72:75], a[112:115], a[56:59], v[72:75]\n"
"ds_read_b128 a[148:151], v13 offset:4096\n"
"v_mfma_f32_16x16x32_bf16 v[60:63], a[104:107], a[56:59], v[60:63]\n"
"v_mfma_f32_16x16x32_bf16 v[60:63], a[108:111], a[60:63], v[60:63]\n"
"ds_read_b128 a[152:155], v12 offset:6144\n"
"v_mfma_f32_16x16x32_bf16 v[56:59], a[100:103], a[60:63], v[56:59]\n"
"v_mfma_f32_16x16x32_bf16 v[56:59], a[96:99], a[56:59], v[56:59]\n"
"ds_read_b128 a[156:159], v13 offset:6144\n"
"v_mfma_f32_16x16x32_bf16 v[80:83], a[96:99], a[64:67], v[80:83]\n"
"buffer_load_dwordx4 v2, s[24:27], s67 offen offset:2048 lds\n"
"v_subrev_u32 v2, 1408, v2\n"
"v_mfma_f32_16x16x32_bf16 v[80:83], a[100:103], a[68:71], v[80:83]\n"
"ds_read_b128 a[160:163], v12 offset:8192\n"
"v_mfma_f32_16x16x32_bf16 v[84:87], a[108:111], a[68:71], v[84:87]\n"
"v_mfma_f32_16x16x32_bf16 v[84:87], a[104:107], a[64:67], v[84:87]\n"
"ds_read_b128 a[164:167], v13 offset:8192\n"
"v_mfma_f32_16x16x32_bf16 v[88:91], a[112:115], a[64:67], v[88:91]\n"
"buffer_load_dwordx4 v3, s[24:27], s67 offen offset:3072 lds\n"
"v_mfma_f32_16x16x32_bf16 v[88:91], a[116:119], a[68:71], v[88:91]\n"
"ds_read_b128 a[168:171], v12 offset:10240\n"
"v_subrev_u32 v3, 1408, v3\n"
"v_mfma_f32_16x16x32_bf16 v[92:95], a[124:127], a[68:71], v[92:95]\n"
"v_mfma_f32_16x16x32_bf16 v[92:95], a[120:123], a[64:67], v[92:95]\n"
"ds_read_b128 a[172:175], v13 offset:10240\n"
"s_add_u32 m0, s77, 0\n"
"v_mfma_f32_16x16x32_bf16 v[116:119], a[120:123], a[72:75], v[116:119]\n"
"buffer_load_dwordx4 v4, s[56:59], 0 offen offset:0 sc1 lds\n"
"v_mfma_f32_16x16x32_bf16 v[116:119], a[124:127], a[76:79], v[116:119]\n"
"ds_read_b128 a[176:179], v12 offset:12288\n"
"v_mfma_f32_16x16x32_bf16 v[112:115], a[116:119], a[76:79], v[112:115]\n"
"v_mfma_f32_16x16x32_bf16 v[112:115], a[112:115], a[72:75], v[112:115]\n"
"ds_read_b128 a[180:183], v13 offset:12288\n"
"v_mfma_f32_16x16x32_bf16 v[108:111], a[104:107], a[72:75], v[108:111]\n"
"v_mfma_f32_16x16x32_bf16 v[108:111], a[108:111], a[76:79], v[108:111]\n"
"ds_read_b128 a[184:187], v12 offset:14336\n"
"v_mfma_f32_16x16x32_bf16 v[104:107], a[100:103], a[76:79], v[104:107]\n"
"buffer_load_dwordx4 v4, s[56:59], 0 offen offset:1024 sc1 lds\n"
"v_mfma_f32_16x16x32_bf16 v[104:107], a[96:99], a[72:75], v[104:107]\n"
"ds_read_b128 a[188:191], v13 offset:14336\n"
"v_mfma_f32_16x16x32_bf16 v[120:123], a[96:99], a[80:83], v[120:123]\n"
"v_mfma_f32_16x16x32_bf16 v[120:123], a[100:103], a[84:87], v[120:123]\n"
"v_mfma_f32_16x16x32_bf16 v[124:127], a[108:111], a[84:87], v[124:127]\n"
"buffer_load_dwordx4 v4, s[56:59], 0 offen offset:2048 sc1 lds\n"
"v_mfma_f32_16x16x32_bf16 v[124:127], a[104:107], a[80:83], v[124:127]\n"
"v_mfma_f32_16x16x32_bf16 v[136:139], a[112:115], a[80:83], v[136:139]\n"
"v_mfma_f32_16x16x32_bf16 v[136:139], a[116:119], a[84:87], v[136:139]\n"
"v_mfma_f32_16x16x32_bf16 v[140:143], a[124:127], a[84:87], v[140:143]\n"
"v_mfma_f32_16x16x32_bf16 v[140:143], a[120:123], a[80:83], v[140:143]\n"
"buffer_load_dwordx4 v4, s[56:59], 0 offen offset:3072 sc1 lds\n"
"v_mfma_f32_16x16x32_bf16 v[156:159], a[120:123], a[88:91], v[156:159]\n"
"v_subrev_u32 v4, 1441792, v4\n"
"s_add_u32 s76, s76, s8\n"
"v_mfma_f32_16x16x32_bf16 v[156:159], a[124:127], a[92:95], v[156:159]\n"
"s_add_u32 s77, s77, s8\n"
"s_cmp_eq_u32 s7, 98304\n"
"s_cselect_b32 s8, 4294868992, s9\n"
"v_mfma_f32_16x16x32_bf16 v[152:155], a[116:119], a[92:95], v[152:155]\n"
"s_add_u32 s7, s7, s8\n"
"v_add_u32 v12, s8, v12\n"
"v_mfma_f32_16x16x32_bf16 v[152:155], a[112:115], a[88:91], v[152:155]\n"
"v_add_u32 v13, s8, v13\n"
"v_mfma_f32_16x16x32_bf16 v[148:151], a[104:107], a[88:91], v[148:151]\n"
"v_add_u32 v14, s8, v14\n"
"v_mfma_f32_16x16x32_bf16 v[148:151], a[108:111], a[92:95], v[148:151]\n"
"v_mfma_f32_16x16x32_bf16 v[144:147], a[100:103], a[92:95], v[144:147]\n"
"v_mfma_f32_16x16x32_bf16 v[144:147], a[96:99], a[88:91], v[144:147]\n"
"s_waitcnt lgkmcnt(14)\n"
"v_mfma_f32_16x16x32_bf16 a[224:227], a[192:195], a[128:131], a[224:227]\n"
"s_waitcnt vmcnt(13) lgkmcnt(0)\n"
"s_barrier\n"
"s_waitcnt vmcnt(12)\n"
"v_mfma_f32_16x16x32_bf16 a[224:227], a[196:199], a[132:135], a[224:227]\n"
"v_cmp_ne_u32 vcc, 1, v11\n"
"v_mfma_f32_16x16x32_bf16 a[228:231], a[204:207], a[132:135], a[228:231]\n"
"s_nop 4\n"
"s_cbranch_vccnz PL12_%=\n"
"PL12d_%=:\n"
"v_mfma_f32_16x16x32_bf16 a[228:231], a[200:203], a[128:131], a[228:231]\n"
"ds_read_b128 a[32:35], v12 offset:0\n"
"s_add_u32 m0, s76, 0\n"
"v_mfma_f32_16x16x32_bf16 a[232:235], a[208:211], a[128:131], a[232:235]\n"
"ds_read_b128 a[36:39], v13 offset:0\n"
"v_mfma_f32_16x16x32_bf16 a[232:235], a[212:215], a[132:135], a[232:235]\n"
"buffer_load_dwordx4 v2, s[24:27], s64 offen offset:0 lds\n"
"v_mfma_f32_16x16x32_bf16 a[236:239], a[220:223], a[132:135], a[236:239]\n"
"ds_read_b128 a[96:99], v14 offset:0\n"
"v_mfma_f32_16x16x32_bf16 a[236:239], a[216:219], a[128:131], a[236:239]\n"
"ds_read_b128 a[100:103], v14 offset:1024\n"
"v_mfma_f32_16x16x32_bf16 a[252:255], a[216:219], a[136:139], a[252:255]\n"
"buffer_load_dwordx4 v3, s[24:27], s64 offen offset:1024 lds\n"
"v_mfma_f32_16x16x32_bf16 a[252:255], a[220:223], a[140:143], a[252:255]\n"
"ds_read_b128 a[104:107], v14 offset:2048\n"
"v_mfma_f32_16x16x32_bf16 a[248:251], a[212:215], a[140:143], a[248:251]\n"
"v_mfma_f32_16x16x32_bf16 a[248:251], a[208:211], a[136:139], a[248:251]\n"
"ds_read_b128 a[108:111], v14 offset:3072\n"
"v_mfma_f32_16x16x32_bf16 a[244:247], a[200:203], a[136:139], a[244:247]\n"
"buffer_load_dwordx4 v2, s[24:27], s65 offen offset:2048 lds\n"
"v_mfma_f32_16x16x32_bf16 a[244:247], a[204:207], a[140:143], a[244:247]\n"
"ds_read_b128 a[112:115], v14 offset:4096\n"
"v_mfma_f32_16x16x32_bf16 a[240:243], a[196:199], a[140:143], a[240:243]\n"
"v_mfma_f32_16x16x32_bf16 a[240:243], a[192:195], a[136:139], a[240:243]\n"
"ds_read_b128 a[116:119], v14 offset:5120\n"
"v_mfma_f32_16x16x32_bf16 v[40:43], a[192:195], a[144:147], v[40:43]\n"
"v_mfma_f32_16x16x32_bf16 v[40:43], a[196:199], a[148:151], v[40:43]\n"
"ds_read_b128 a[120:123], v14 offset:6144\n"
"v_mfma_f32_16x16x32_bf16 v[44:47], a[204:207], a[148:151], v[44:47]\n"
"buffer_load_dwordx4 v3, s[24:27], s65 offen offset:3072 lds\n"
"v_mfma_f32_16x16x32_bf16 v[44:47], a[200:203], a[144:147], v[44:47]\n"
"ds_read_b128 a[124:127], v14 offset:7168\n"
"v_mfma_f32_16x16x32_bf16 v[48:51], a[208:211], a[144:147], v[48:51]\n"
"s_add_u32 m0, s76, 4096\n"
"v_mfma_f32_16x16x32_bf16 v[48:51], a[212:215], a[148:151], v[48:51]\n"
"ds_read_b128 a[40:43], v12 offset:2048\n"
"v_mfma_f32_16x16x32_bf16 v[52:55], a[220:223], a[148:151], v[52:55]\n"
"buffer_load_dwordx4 v2, s[24:27], s66 offen offset:0 lds\n"
"v_mfma_f32_16x16x32_bf16 v[52:55], a[216:219], a[144:147], v[52:55]\n"
"ds_read_b128 a[44:47], v13 offset:2048\n"
"v_mfma_f32_16x16x32_bf16 v[76:79], a[216:219], a[152:155], v[76:79]\n"
"v_mfma_f32_16x16x32_bf16 v[76:79], a[220:223], a[156:159], v[76:79]\n"
"ds_read_b128 a[48:51], v12 offset:4096\n"
"v_mfma_f32_16x16x32_bf16 v[72:75], a[212:215], a[156:159], v[72:75]\n"
"buffer_load_dwordx4 v3, s[24:27], s66 offen offset:1024 lds\n"
"v_mfma_f32_16x16x32_bf16 v[72:75], a[208:211], a[152:155], v[72:75]\n"
"ds_read_b128 a[52:55], v13 offset:4096\n"
"v_mfma_f32_16x16x32_bf16 v[60:63], a[200:203], a[152:155], v[60:63]\n"
"v_mfma_f32_16x16x32_bf16 v[60:63], a[204:207], a[156:159], v[60:63]\n"
"ds_read_b128 a[56:59], v12 offset:6144\n"
"v_mfma_f32_16x16x32_bf16 v[56:59], a[196:199], a[156:159], v[56:59]\n"
"v_mfma_f32_16x16x32_bf16 v[56:59], a[192:195], a[152:155], v[56:59]\n"
"ds_read_b128 a[60:63], v13 offset:6144\n"
"v_mfma_f32_16x16x32_bf16 v[80:83], a[192:195], a[160:163], v[80:83]\n"
"buffer_load_dwordx4 v2, s[24:27], s67 offen offset:2048 lds\n"
"v_add_u32 v2, 512, v2\n"
"v_mfma_f32_16x16x32_bf16 v[80:83], a[196:199], a[164:167], v[80:83]\n"
"ds_read_b128 a[64:67], v12 offset:8192\n"
"v_mfma_f32_16x16x32_bf16 v[84:87], a[204:207], a[164:167], v[84:87]\n"
"v_mfma_f32_16x16x32_bf16 v[84:87], a[200:203], a[160:163], v[84:87]\n"
"ds_read_b128 a[68:71], v13 offset:8192\n"
"v_mfma_f32_16x16x32_bf16 v[88:91], a[208:211], a[160:163], v[88:91]\n"
"buffer_load_dwordx4 v3, s[24:27], s67 offen offset:3072 lds\n"
"v_mfma_f32_16x16x32_bf16 v[88:91], a[212:215], a[164:167], v[88:91]\n"
"ds_read_b128 a[72:75], v12 offset:10240\n"
"v_add_u32 v3, 512, v3\n"
"v_mfma_f32_16x16x32_bf16 v[92:95], a[220:223], a[164:167], v[92:95]\n"
"v_mfma_f32_16x16x32_bf16 v[92:95], a[216:219], a[160:163], v[92:95]\n"
"ds_read_b128 a[76:79], v13 offset:10240\n"
"s_add_u32 m0, s77, 0\n"
"v_mfma_f32_16x16x32_bf16 v[116:119], a[216:219], a[168:171], v[116:119]\n"
"buffer_load_dwordx4 v4, s[56:59], 0 offen offset:0 sc1 lds\n"
"v_mfma_f32_16x16x32_bf16 v[116:119], a[220:223], a[172:175], v[116:119]\n"
"ds_read_b128 a[80:83], v12 offset:12288\n"
"v_mfma_f32_16x16x32_bf16 v[112:115], a[212:215], a[172:175], v[112:115]\n"
"v_mfma_f32_16x16x32_bf16 v[112:115], a[208:211], a[168:171], v[112:115]\n"
"ds_read_b128 a[84:87], v13 offset:12288\n"
"v_mfma_f32_16x16x32_bf16 v[108:111], a[200:203], a[168:171], v[108:111]\n"
"v_mfma_f32_16x16x32_bf16 v[108:111], a[204:207], a[172:175], v[108:111]\n"
"ds_read_b128 a[88:91], v12 offset:14336\n"
"v_mfma_f32_16x16x32_bf16 v[104:107], a[196:199], a[172:175], v[104:107]\n"
"buffer_load_dwordx4 v4, s[56:59], 0 offen offset:1024 sc1 lds\n"
"v_mfma_f32_16x16x32_bf16 v[104:107], a[192:195], a[168:171], v[104:107]\n"
"ds_read_b128 a[92:95], v13 offset:14336\n"
"v_mfma_f32_16x16x32_bf16 v[120:123], a[192:195], a[176:179], v[120:123]\n"
"v_mfma_f32_16x16x32_bf16 v[120:123], a[196:199], a[180:183], v[120:123]\n"
"v_mfma_f32_16x16x32_bf16 v[124:127], a[204:207], a[180:183], v[124:127]\n"
"buffer_load_dwordx4 v4, s[56:59], 0 offen offset:2048 sc1 lds\n"
"v_mfma_f32_16x16x32_bf16 v[124:127], a[200:203], a[176:179], v[124:127]\n"
"v_mfma_f32_16x16x32_bf16 v[136:139], a[208:211], a[176:179], v[136:139]\n"
"v_mfma_f32_16x16x32_bf16 v[136:139], a[212:215], a[180:183], v[136:139]\n"
"v_mfma_f32_16x16x32_bf16 v[140:143], a[220:223], a[180:183], v[140:143]\n"
"v_mfma_f32_16x16x32_bf16 v[140:143], a[216:219], a[176:179], v[140:143]\n"
"buffer_load_dwordx4 v4, s[56:59], 0 offen offset:3072 sc1 lds\n"
"v_mfma_f32_16x16x32_bf16 v[156:159], a[216:219], a[184:187], v[156:159]\n"
"v_add_u32 v4, 524288, v4\n"
"s_add_u32 s76, s76, s8\n"
"v_mfma_f32_16x16x32_bf16 v[156:159], a[220:223], a[188:191], v[156:159]\n"
"s_add_u32 s77, s77, s8\n"
"s_cmp_eq_u32 s7, 98304\n"
"s_cselect_b32 s8, 4294868992, s9\n"
"v_mfma_f32_16x16x32_bf16 v[152:155], a[212:215], a[188:191], v[152:155]\n"
"s_add_u32 s7, s7, s8\n"
"v_add_u32 v12, s8, v12\n"
"v_mfma_f32_16x16x32_bf16 v[152:155], a[208:211], a[184:187], v[152:155]\n"
"v_add_u32 v13, s8, v13\n"
"v_mfma_f32_16x16x32_bf16 v[148:151], a[200:203], a[184:187], v[148:151]\n"
"v_add_u32 v14, s8, v14\n"
"v_mfma_f32_16x16x32_bf16 v[148:151], a[204:207], a[188:191], v[148:151]\n"
"v_mfma_f32_16x16x32_bf16 v[144:147], a[196:199], a[188:191], v[144:147]\n"
"v_mfma_f32_16x16x32_bf16 v[144:147], a[192:195], a[184:187], v[144:147]\n"
"s_waitcnt lgkmcnt(14)\n"
"v_mfma_f32_16x16x32_bf16 a[224:227], a[96:99], a[32:35], a[224:227]\n"
"s_waitcnt vmcnt(12) lgkmcnt(0)\n"
"s_barrier\n"
"v_mfma_f32_16x16x32_bf16 a[224:227], a[100:103], a[36:39], a[224:227]\n"
"ds_read_b128 a[128:131], v12 offset:0\n"
"v_mfma_f32_16x16x32_bf16 a[228:231], a[108:111], a[36:39], a[228:231]\n"
"s_add_u32 m0, s76, 0\n"
"v_mfma_f32_16x16x32_bf16 a[228:231], a[104:107], a[32:35], a[228:231]\n"
"ds_read_b128 a[132:135], v13 offset:0\n"
"v_mfma_f32_16x16x32_bf16 a[232:235], a[112:115], a[32:35], a[232:235]\n"
"buffer_load_dwordx4 v2, s[24:27], s64 offen offset:0 lds\n"
"v_mfma_f32_16x16x32_bf16 a[232:235], a[116:119], a[36:39], a[232:235]\n"
"ds_read_b128 a[192:195], v14 offset:0\n"
"v_mfma_f32_16x16x32_bf16 a[236:239], a[124:127], a[36:39], a[236:239]\n"
"v_mfma_f32_16x16x32_bf16 a[236:239], a[120:123], a[32:35], a[236:239]\n"
"ds_read_b128 a[196:199], v14 offset:1024\n"
"v_mfma_f32_16x16x32_bf16 a[252:255], a[120:123], a[40:43], a[252:255]\n"
"buffer_load_dwordx4 v3, s[24:27], s64 offen offset:1024 lds\n"
"v_mfma_f32_16x16x32_bf16 a[252:255], a[124:127], a[44:47], a[252:255]\n"
"ds_read_b128 a[200:203], v14 offset:2048\n"
"v_mfma_f32_16x16x32_bf16 a[248:251], a[116:119], a[44:47], a[248:251]\n"
"v_mfma_f32_16x16x32_bf16 a[248:251], a[112:115], a[40:43], a[248:251]\n"
"ds_read_b128 a[204:207], v14 offset:3072\n"
"v_mfma_f32_16x16x32_bf16 a[244:247], a[104:107], a[40:43], a[244:247]\n"
"buffer_load_dwordx4 v2, s[24:27], s65 offen offset:2048 lds\n"
"v_mfma_f32_16x16x32_bf16 a[244:247], a[108:111], a[44:47], a[244:247]\n"
"ds_read_b128 a[208:211], v14 offset:4096\n"
"v_mfma_f32_16x16x32_bf16 a[240:243], a[100:103], a[44:47], a[240:243]\n"
"v_mfma_f32_16x16x32_bf16 a[240:243], a[96:99], a[40:43], a[240:243]\n"
"ds_read_b128 a[212:215], v14 offset:5120\n"
"v_mfma_f32_16x16x32_bf16 v[40:43], a[96:99], a[48:51], v[40:43]\n"
"v_mfma_f32_16x16x32_bf16 v[40:43], a[100:103], a[52:55], v[40:43]\n"
"ds_read_b128 a[216:219], v14 offset:6144\n"
"v_mfma_f32_16x16x32_bf16 v[44:47], a[108:111], a[52:55], v[44:47]\n"
"buffer_load_dwordx4 v3, s[24:27], s65 offen offset:3072 lds\n"
"v_mfma_f32_16x16x32_bf16 v[44:47], a[104:107], a[48:51], v[44:47]\n"
"ds_read_b128 a[220:223], v14 offset:7168\n"
"v_mfma_f32_16x16x32_bf16 v[48:51], a[112:115], a[48:51], v[48:51]\n"
"s_add_u32 m0, s76, 4096\n"
"v_mfma_f32_16x16x32_bf16 v[48:51], a[116:119], a[52:55], v[48:51]\n"
"ds_read_b128 a[136:139], v12 offset:2048\n"
"v_mfma_f32_16x16x32_bf16 v[52:55], a[124:127], a[52:55], v[52:55]\n"
"buffer_load_dwordx4 v2, s[24:27], s66 offen offset:0 lds\n"
"v_mfma_f32_16x16x32_bf16 v[52:55], a[120:123], a[48:51], v[52:55]\n"
"ds_read_b128 a[140:143], v13 offset:2048\n"
"v_mfma_f32_16x16x32_bf16 v[76:79], a[120:123], a[56:59], v[76:79]\n"
"v_mfma_f32_16x16x32_bf16 v[76:79], a[124:127], a[60:63], v[76:79]\n"
"ds_read_b128 a[144:147], v12 offset:4096\n"
"v_mfma_f32_16x16x32_bf16 v[72:75], a[116:119], a[60:63], v[72:75]\n"
"buffer_load_dwordx4 v3, s[24:27], s66 offen offset:1024 lds\n"
"v_mfma_f32_16x16x32_bf16 v[72:75], a[112:115], a[56:59], v[72:75]\n"
"ds_read_b128 a[148:151], v13 offset:4096\n"
"v_mfma_f32_16x16x32_bf16 v[60:63], a[104:107], a[56:59], v[60:63]\n"
"v_mfma_f32_16x16x32_bf16 v[60:63], a[108:111], a[60:63], v[60:63]\n"
"ds_read_b128 a[152:155], v12 offset:6144\n"
"v_mfma_f32_16x16x32_bf16 v[56:59], a[100:103], a[60:63], v[56:59]\n"
"v_mfma_f32_16x16x32_bf16 v[56:59], a[96:99], a[56:59], v[56:59]\n"
"ds_read_b128 a[156:159], v13 offset:6144\n"
"v_mfma_f32_16x16x32_bf16 v[80:83], a[96:99], a[64:67], v[80:83]\n"
"buffer_load_dwordx4 v2, s[24:27], s67 offen offset:2048 lds\n"
"v_add_u32 v2, 512, v2\n"
"v_mfma_f32_16x16x32_bf16 v[80:83], a[100:103], a[68:71], v[80:83]\n"
"ds_read_b128 a[160:163], v12 offset:8192\n"
"v_mfma_f32_16x16x32_bf16 v[84:87], a[108:111], a[68:71], v[84:87]\n"
"v_mfma_f32_16x16x32_bf16 v[84:87], a[104:107], a[64:67], v[84:87]\n"
"ds_read_b128 a[164:167], v13 offset:8192\n"
"v_mfma_f32_16x16x32_bf16 v[88:91], a[112:115], a[64:67], v[88:91]\n"
"buffer_load_dwordx4 v3, s[24:27], s67 offen offset:3072 lds\n"
"v_mfma_f32_16x16x32_bf16 v[88:91], a[116:119], a[68:71], v[88:91]\n"
"ds_read_b128 a[168:171], v12 offset:10240\n"
"v_add_u32 v3, 512, v3\n"
"v_mfma_f32_16x16x32_bf16 v[92:95], a[124:127], a[68:71], v[92:95]\n"
"v_mfma_f32_16x16x32_bf16 v[92:95], a[120:123], a[64:67], v[92:95]\n"
"ds_read_b128 a[172:175], v13 offset:10240\n"
"s_add_u32 m0, s77, 0\n"
"v_mfma_f32_16x16x32_bf16 v[116:119], a[120:123], a[72:75], v[116:119]\n"
"buffer_load_dwordx4 v4, s[56:59], 0 offen offset:0 sc1 lds\n"
"v_mfma_f32_16x16x32_bf16 v[116:119], a[124:127], a[76:79], v[116:119]\n"
"ds_read_b128 a[176:179], v12 offset:12288\n"
"v_mfma_f32_16x16x32_bf16 v[112:115], a[116:119], a[76:79], v[112:115]\n"
"v_mfma_f32_16x16x32_bf16 v[112:115], a[112:115], a[72:75], v[112:115]\n"
"ds_read_b128 a[180:183], v13 offset:12288\n"
"v_mfma_f32_16x16x32_bf16 v[108:111], a[104:107], a[72:75], v[108:111]\n"
"v_mfma_f32_16x16x32_bf16 v[108:111], a[108:111], a[76:79], v[108:111]\n"
"ds_read_b128 a[184:187], v12 offset:14336\n"
"v_mfma_f32_16x16x32_bf16 v[104:107], a[100:103], a[76:79], v[104:107]\n"
"buffer_load_dwordx4 v4, s[56:59], 0 offen offset:1024 sc1 lds\n"
"v_mfma_f32_16x16x32_bf16 v[104:107], a[96:99], a[72:75], v[104:107]\n"
"ds_read_b128 a[188:191], v13 offset:14336\n"
"v_mfma_f32_16x16x32_bf16 v[120:123], a[96:99], a[80:83], v[120:123]\n"
"v_mfma_f32_16x16x32_bf16 v[120:123], a[100:103], a[84:87], v[120:123]\n"
"v_mfma_f32_16x16x32_bf16 v[124:127], a[108:111], a[84:87], v[124:127]\n"
"buffer_load_dwordx4 v4, s[56:59], 0 offen offset:2048 sc1 lds\n"
"v_mfma_f32_16x16x32_bf16 v[124:127], a[104:107], a[80:83], v[124:127]\n"
"v_mfma_f32_16x16x32_bf16 v[136:139], a[112:115], a[80:83], v[136:139]\n"
"v_mfma_f32_16x16x32_bf16 v[136:139], a[116:119], a[84:87], v[136:139]\n"
"v_mfma_f32_16x16x32_bf16 v[140:143], a[124:127], a[84:87], v[140:143]\n"
"v_mfma_f32_16x16x32_bf16 v[140:143], a[120:123], a[80:83], v[140:143]\n"
"buffer_load_dwordx4 v4, s[56:59], 0 offen offset:3072 sc1 lds\n"
"v_mfma_f32_16x16x32_bf16 v[156:159], a[120:123], a[88:91], v[156:159]\n"
"v_add_u32 v4, 524288, v4\n"
"s_add_u32 s76, s76, s8\n"
"v_mfma_f32_16x16x32_bf16 v[156:159], a[124:127], a[92:95], v[156:159]\n"
"s_add_u32 s77, s77, s8\n"
"s_cmp_eq_u32 s7, 98304\n"
"s_cselect_b32 s8, 4294868992, s9\n"
"v_mfma_f32_16x16x32_bf16 v[152:155], a[116:119], a[92:95], v[152:155]\n"
"s_add_u32 s7, s7, s8\n"
"v_add_u32 v12, s8, v12\n"
"v_mfma_f32_16x16x32_bf16 v[152:155], a[112:115], a[88:91], v[152:155]\n"
"v_add_u32 v13, s8, v13\n"
"v_mfma_f32_16x16x32_bf16 v[148:151], a[104:107], a[88:91], v[148:151]\n"
"v_add_u32 v14, s8, v14\n"
"v_mfma_f32_16x16x32_bf16 v[148:151], a[108:111], a[92:95], v[148:151]\n"
"v_mfma_f32_16x16x32_bf16 v[144:147], a[100:103], a[92:95], v[144:147]\n"
"v_mfma_f32_16x16x32_bf16 v[144:147], a[96:99], a[88:91], v[144:147]\n"
"s_branch P1END_%=\n"
"EV13_%=:\n"
"s_lshl_b32 s14, s4, 3\n"
"s_add_u32 s14, s14, s5\n"
"s_lshl_b32 s14, s14, 2\n"
"s_add_u32 s14, s14, s83\n"
"s_mov_b64 exec, 0xfffff\n"
"v_and_b32 v16, 3, v1\n"
"v_xor_b32 v17, s14, v16\n"
"v_lshrrev_b32 v18, 2, v1\n"
"v_lshl_add_u32 v17, v17, 2, v18\n"
"v_lshlrev_b32 v17, 7, v17\n"
"v_lshl_add_u32 v17, v16, 4, v17\n"
"v_add_u32 v31, 262144, v17\n"
"s_lshl_b32 s14, s79, 5\n"
"s_add_u32 s14, s14, s80\n"
"s_lshl_b32 s15, s14, 7\n"
"s_mov_b64 exec, 0xf0000\n"
"v_lshl_add_u32 v31, v16, 2, s15\n"
"s_mov_b64 exec, -1\n"
"s_lshl_b32 s14, s14, 2\n"
"s_add_u32 s14, s14, 32768\n"
"v_writelane_b32 v31, s14, 20\n"
"s_lshl_b32 s14, s12, 2\n"
"s_add_u32 s15, s14, 36864\n"
"s_add_u32 s14, s14, 38912\n"
"v_writelane_b32 v22, s15, 0\n"
"v_writelane_b32 v21, s14, 0\n"
"s_mov_b32 s14, 40960\n"
"v_writelane_b32 v21, s14, 1\n"
"s_branch EV13r_%=\n"
"EV14_%=:\n"
"v_lshlrev_b32 v29, 4, v1\n"
"v_add_u32 v29, 36864, v29\n"
"s_mov_b32 s14, 40960\n"
"v_writelane_b32 v21, s14, 1\n"
"v_writelane_b32 v30, s14, 0\n"
"s_branch EV14r_%=\n"
"EV16_%=:\n"
"s_mov_b32 s15, 0x40000\n"
"s_waitcnt vmcnt(0)\n"
"HS15_%=:\n"
"buffer_store_dword v23, v22, s[56:59], 0 offen sc1\n"
"buffer_load_dword v23, v21, s[56:59], 0 offen sc1\n"
"v_xor_b32 v24, v24, v16\n"
"v_xor_b32 v25, v25, v17\n"
"v_xor_b32 v26, v26, v18\n"
"v_xor_b32 v27, v27, v19\n"
"v_or3_b32 v24, v24, v25, v26\n"
"v_or_b32 v24, v24, v27\n"
"v_cmp_ne_u32 vcc, 0, v24\n"
"s_nop 3\n"
"s_cmp_eq_u64 vcc, 0\n"
"s_cselect_b32 s14, 0, 1073741824\n"
"buffer_store_dword v28, v30, s[56:59], s14 offen sc1\n"
"buffer_load_dwordx4 v[24:27], v29, s[56:59], 0 offen sc1\n"
"s_waitcnt vmcnt(0)\n"
"v_readlane_b32 s14, v23, 1\n"
"s_nop 3\n"
"s_cmp_lg_u32 s14, s7\n"
"s_cselect_b32 s6, 2, s6\n"
"s_sub_u32 s15, s15, 1\n"
"s_cselect_b32 s6, 2, s6\n"
"s_cmp_lt_u32 s6, 2\n"
"s_cbranch_scc1 HS15_%=\n"
"s_branch EV16r_%=\n"
"EV17_%=:\n"
"s_lshl_b32 s14, s79, 5\n"
"s_add_u32 s14, s14, s80\n"
"s_lshl_b32 s14, s14, 7\n"
"s_add_u32 s14, s14, 0\n"
"s_mov_b64 exec, 15\n"
"v_mov_b32 v14, 1\n"
"v_lshlrev_b32 v15, 2, v1\n"
"buffer_store_dword v14, v15, s[56:59], s14 offen sc1\n"
"s_mov_b64 exec, -1\n"
"s_branch EV17r_%=\n"
"PL18_%=:\n"
"s_mov_b32 s14, 0x40000\n"
"PL18a_%=:\n"
"buffer_load_dword v11, v10, s[56:59], 0 offen offset:0 sc1\n"
"s_sub_u32 s14, s14, 1\n"
"s_waitcnt vmcnt(0)\n"
"s_cbranch_scc1 PL18d_%=\n"
"v_cmp_ne_u32 vcc, 1, v11\n"
"s_nop 4\n"
"s_cbranch_vccnz PL18a_%=\n"
"s_branch PL18d_%=\n"
"PL19_%=:\n"
"s_mov_b32 s14, 0x40000\n"
"PL19a_%=:\n"
"buffer_load_dword v11, v10, s[56:59], 0 offen offset:4 sc1\n"
"s_sub_u32 s14, s14, 1\n"
"s_waitcnt vmcnt(0)\n"
"s_cbranch_scc1 PL19d_%=\n"
"v_cmp_ne_u32 vcc, 1, v11\n"
"s_nop 4\n"
"s_cbranch_vccnz PL19a_%=\n"
"s_branch PL19d_%=\n"
"PL20_%=:\n"
"s_mov_b32 s14, 0x40000\n"
"PL20a_%=:\n"
"buffer_load_dword v11, v10, s[56:59], 0 offen offset:8 sc1\n"
"s_sub_u32 s14, s14, 1\n"
"s_waitcnt vmcnt(0)\n"
"s_cbranch_scc1 PL20d_%=\n"
"v_cmp_ne_u32 vcc, 1, v11\n"
"s_nop 4\n"
"s_cbranch_vccnz PL20a_%=\n"
"s_branch PL20d_%=\n"
"EV21_%=:\n"
"s_lshl_b32 s14, s79, 5\n"
"s_add_u32 s14, s14, s80\n"
"s_lshl_b32 s14, s14, 2\n"
"s_add_u32 s14, s14, 32768\n"
"s_mov_b64 exec, 1\n"
"v_mov_b32 v5, 1\n"
"v_lshlrev_b32 v15, 2, v1\n"
"buffer_store_dword v5, v15, s[56:59], s14 offen sc1\n"
"s_mov_b64 exec, -1\n"
"s_branch EV21r_%=\n"
"PL22_%=:\n"
"s_mov_b32 s14, 0x40000\n"
"PL22a_%=:\n"
"buffer_load_dword v11, v10, s[56:59], 0 offen offset:12 sc1\n"
"s_sub_u32 s14, s14, 1\n"
"s_waitcnt vmcnt(0)\n"
"s_cbranch_scc1 PL22d_%=\n"
"v_cmp_ne_u32 vcc, 1, v11\n"
"s_nop 4\n"
"s_cbranch_vccnz PL22a_%=\n"
"s_branch PL22d_%=\n"
"PL23_%=:\n"
"s_mov_b32 s14, 0x40000\n"
"PL23a_%=:\n"
"buffer_load_dword v31, v30, s[56:59], 0 offen offset:0 sc1\n"
"s_sub_u32 s14, s14, 1\n"
"s_waitcnt vmcnt(0)\n"
"s_cbranch_scc1 PL23d_%=\n"
"v_cmp_ne_u32 vcc, 1, v31\n"
"s_nop 4\n"
"s_cbranch_vccnz PL23a_%=\n"
"s_branch PL23d_%=\n"
"ST1_%=:\n"
"s_mov_b32 s14, 0x40000\n"
"s_mov_b64 exec, 0xe\n"
"ST1a_%=:\n"
"buffer_load_dword v13, v14, s[56:59], 0 offen sc1\n"
"s_sub_u32 s14, s14, 1\n"
"s_waitcnt vmcnt(0)\n"
"s_cbranch_scc1 ST1e_%=\n"
"v_cmp_ne_u32 vcc, 1, v13\n"
"s_nop 4\n"
"s_bitcmp1_b32 vcc_lo, 1\n"
"s_cbranch_scc1 ST1a_%=\n"
"ST1e_%=:\n"
"s_mov_b64 exec, -1\n"
"s_branch ST1d_%=\n"
"ST2_%=:\n"
"s_mov_b32 s14, 0x40000\n"
"s_mov_b64 exec, 0xe\n"
"ST2a_%=:\n"
"buffer_load_dword v13, v14, s[56:59], 0 offen sc1\n"
"s_sub_u32 s14, s14, 1\n"
"s_waitcnt vmcnt(0)\n"
"s_cbranch_scc1 ST2e_%=\n"
"v_cmp_ne_u32 vcc, 1, v13\n"
"s_nop 4\n"
"s_bitcmp1_b32 vcc_lo, 2\n"
"s_cbranch_scc1 ST2a_%=\n"
"ST2e_%=:\n"
"s_mov_b64 exec, -1\n"
"s_branch ST2d_%=\n"
"ST3_%=:\n"
"buffer_load_dword v13, v14, s[56:59], 0 offen sc1\n"
"s_waitcnt vmcnt(0)\n"
"v_cmp_ne_u32 vcc, 1, v13\n"
"s_nop 4\n"
"s_bitcmp1_b32 vcc_lo, 3\n"
"s_cbranch_scc0 ST3d_%=\n"
"v_lshlrev_b32 v16, 16, v160\n"
"v_and_b32 v17, 0xffff0000, v160\n"
"v_lshlrev_b32 v18, 16, v161\n"
"v_and_b32 v19, 0xffff0000, v161\n"
"v_lshlrev_b32 v20, 16, v162\n"
"v_and_b32 v21, 0xffff0000, v162\n"
"v_lshlrev_b32 v22, 16, v163\n"
"v_and_b32 v23, 0xffff0000, v163\n"
"v_pk_add_f32 v[32:33], v[32:33], v[16:17]\n"
"v_pk_add_f32 v[34:35], v[34:35], v[18:19]\n"
"v_pk_add_f32 v[36:37], v[36:37], v[20:21]\n"
"v_pk_add_f32 v[38:39], v[38:39], v[22:23]\n"
"v_lshlrev_b32 v24, 16, v164\n"
"v_and_b32 v25, 0xffff0000, v164\n"
"v_lshlrev_b32 v26, 16, v165\n"
"v_and_b32 v27, 0xffff0000, v165\n"
"v_lshlrev_b32 v28, 16, v166\n"
"v_and_b32 v29, 0xffff0000, v166\n"
"v_lshlrev_b32 v30, 16, v167\n"
"v_and_b32 v31, 0xffff0000, v167\n"
"v_pk_add_f32 v[64:65], v[64:65], v[24:25]\n"
"v_pk_add_f32 v[66:67], v[66:67], v[26:27]\n"
"v_pk_add_f32 v[68:69], v[68:69], v[28:29]\n"
"v_pk_add_f32 v[70:71], v[70:71], v[30:31]\n"
"v_lshlrev_b32 v16, 16, v168\n"
"v_and_b32 v17, 0xffff0000, v168\n"
"v_lshlrev_b32 v18, 16, v169\n"
"v_and_b32 v19, 0xffff0000, v169\n"
"v_lshlrev_b32 v20, 16, v170\n"
"v_and_b32 v21, 0xffff0000, v170\n"
"v_lshlrev_b32 v22, 16, v171\n"
"v_and_b32 v23, 0xffff0000, v171\n"
"v_pk_add_f32 v[96:97], v[96:97], v[16:17]\n"
"v_pk_add_f32 v[98:99], v[98:99], v[18:19]\n"
"v_pk_add_f32 v[100:101], v[100:101], v[20:21]\n"
"v_pk_add_f32 v[102:103], v[102:103], v[22:23]\n"
"v_lshlrev_b32 v24, 16, v172\n"
"v_and_b32 v25, 0xffff0000, v172\n"
"v_lshlrev_b32 v26, 16, v173\n"
"v_and_b32 v27, 0xffff0000, v173\n"
"v_lshlrev_b32 v28, 16, v174\n"
"v_and_b32 v29, 0xffff0000, v174\n"
"v_lshlrev_b32 v30, 16, v175\n"
"v_and_b32 v31, 0xffff0000, v175\n"
"v_pk_add_f32 v[128:129], v[128:129], v[24:25]\n"
"v_pk_add_f32 v[130:131], v[130:131], v[26:27]\n"
"v_pk_add_f32 v[132:133], v[132:133], v[28:29]\n"
"v_pk_add_f32 v[134:135], v[134:135], v[30:31]\n"
"v_lshlrev_b32 v16, 16, v176\n"
"v_and_b32 v17, 0xffff0000, v176\n"
"v_lshlrev_b32 v18, 16, v177\n"
"v_and_b32 v19, 0xffff0000, v177\n"
"v_lshlrev_b32 v20, 16, v178\n"
"v_and_b32 v21, 0xffff0000, v178\n"
"v_lshlrev_b32 v22, 16, v179\n"
"v_and_b32 v23, 0xffff0000, v179\n"
"v_pk_add_f32 v[32:33], v[32:33], v[16:17]\n"
"v_pk_add_f32 v[34:35], v[34:35], v[18:19]\n"
"v_pk_add_f32 v[36:37], v[36:37], v[20:21]\n"
"v_pk_add_f32 v[38:39], v[38:39], v[22:23]\n"
"v_lshlrev_b32 v24, 16, v180\n"
"v_and_b32 v25, 0xffff0000, v180\n"
"v_lshlrev_b32 v26, 16, v181\n"
"v_and_b32 v27, 0xffff0000, v181\n"
"v_lshlrev_b32 v28, 16, v182\n"
"v_and_b32 v29, 0xffff0000, v182\n"
"v_lshlrev_b32 v30, 16, v183\n"
"v_and_b32 v31, 0xffff0000, v183\n"
"v_pk_add_f32 v[64:65], v[64:65], v[24:25]\n"
"v_pk_add_f32 v[66:67], v[66:67], v[26:27]\n"
"v_pk_add_f32 v[68:69], v[68:69], v[28:29]\n"
"v_pk_add_f32 v[70:71], v[70:71], v[30:31]\n"
"v_lshlrev_b32 v16, 16, v184\n"
"v_and_b32 v17, 0xffff0000, v184\n"
"v_lshlrev_b32 v18, 16, v185\n"
"v_and_b32 v19, 0xffff0000, v185\n"
"v_lshlrev_b32 v20, 16, v186\n"
"v_and_b32 v21, 0xffff0000, v186\n"
"v_lshlrev_b32 v22, 16, v187\n"
"v_and_b32 v23, 0xffff0000, v187\n"
"v_pk_add_f32 v[96:97], v[96:97], v[16:17]\n"
"v_pk_add_f32 v[98:99], v[98:99], v[18:19]\n"
"v_pk_add_f32 v[100:101], v[100:101], v[20:21]\n"
"v_pk_add_f32 v[102:103], v[102:103], v[22:23]\n"
"v_lshlrev_b32 v24, 16, v188\n"
"v_and_b32 v25, 0xffff0000, v188\n"
"v_lshlrev_b32 v26, 16, v189\n"
"v_and_b32 v27, 0xffff0000, v189\n"
"v_lshlrev_b32 v28, 16, v190\n"
"v_and_b32 v29, 0xffff0000, v190\n"
"v_lshlrev_b32 v30, 16, v191\n"
"v_and_b32 v31, 0xffff0000, v191\n"
"v_pk_add_f32 v[128:129], v[128:129], v[24:25]\n"
"v_pk_add_f32 v[130:131], v[130:131], v[26:27]\n"
"v_pk_add_f32 v[132:133], v[132:133], v[28:29]\n"
"v_pk_add_f32 v[134:135], v[134:135], v[30:31]\n"
"s_mov_b32 s14, 0x40000\n"
"s_mov_b64 exec, 0xe\n"
"ST3a_%=:\n"
"buffer_load_dword v13, v14, s[56:59], 0 offen sc1\n"
"s_sub_u32 s14, s14, 1\n"
"s_waitcnt vmcnt(0)\n"
"s_cbranch_scc1 ST3e_%=\n"
"v_cmp_ne_u32 vcc, 1, v13\n"
"s_nop 4\n"
"s_bitcmp1_b32 vcc_lo, 3\n"
"s_cbranch_scc1 ST3a_%=\n"
"ST3e_%=:\n"
"s_mov_b64 exec, -1\n"
"buffer_load_dwordx4 v[192:195], v2, s[56:59], s71 offen offset:0 sc1\n"
"buffer_load_dwordx4 v[196:199], v2, s[56:59], s71 offen offset:1024 sc1\n"
"buffer_load_dwordx4 v[200:203], v2, s[56:59], s71 offen offset:2048 sc1\n"
"buffer_load_dwordx4 v[204:207], v2, s[56:59], s71 offen offset:3072 sc1\n"
"s_branch STX_%=\n"
"END_%=:\n"
    :: "s"(__builtin_amdgcn_kernarg_segment_ptr()), "s"(blockIdx.x), "v"(threadIdx.x * 16), "s"((unsigned)(size_t)smem)
    :
      "s4","s5","s6","s7","s8","s9","s10","s11","s12","s13","s14","s15","s16","s17","s18","s19","s20","s21","s22","s23","s24","s25","s26","s27",
      "s28","s29","s30","s31","s33","s34","s35","s36","s37","s38","s39","s40","s41","s42","s43","s44","s45","s46","s47","s48","s49","s50","s51","s52",
      "s53","s54","s55","s56","s57","s58","s59","s60","s61","s62","s63","s64","s65","s66","s67","s68","s69","s70","s71","s72","s73","s74","s75","s76",
      "s77","s78","s79","s80","s81","s82","s83","s84","s85","s86","s87","s88","s89","s90","s91","s92","s93","s94","s95","s96","s97","s98","s99","v1",
      "v2","v3","v4","v5","v6","v7","v8","v9","v10","v11","v12","v13","v14","v15","v16","v17","v18","v19","v20","v21","v22","v23","v24","v25",
      "v26","v27","v28","v29","v30","v31","v32","v33","v34","v35","v36","v37","v38","v39","v40","v41","v42","v43","v44","v45","v46","v47","v48","v49",
      "v50","v51","v52","v53","v54","v55","v56","v57","v58","v59","v60","v61","v62","v63","v64","v65","v66","v67","v68","v69","v70","v71","v72","v73",
      "v74","v75","v76","v77","v78","v79","v80","v81","v82","v83","v84","v85","v86","v87","v88","v89","v90","v91","v92","v93","v94","v95","v96","v97",
      "v98","v99","v100","v101","v102","v103","v104","v105","v106","v107","v108","v109","v110","v111","v112","v113","v114","v115","v116","v117","v118","v119","v120","v121",
      "v122","v123","v124","v125","v126","v127","v128","v129","v130","v131","v132","v133","v134","v135","v136","v137","v138","v139","v140","v141","v142","v143","v144","v145",
      "v146","v147","v148","v149","v150","v151","v152","v153","v154","v155","v156","v157","v158","v159","v160","v161","v162","v163","v164","v165","v166","v167","v168","v169",
      "v170","v171","v172","v173","v174","v175","v176","v177","v178","v179","v180","v181","v182","v183","v184","v185","v186","v187","v188","v189","v190","v191","v192","v193",
      "v194","v195","v196","v197","v198","v199","v200","v201","v202","v203","v204","v205","v206","v207","v208","v209","v210","v211","v212","v213","v214","v215","v216","v217",
      "v218","v219","v220","v221","v222","v223","v224","v225","v226","v227","v228","v229","v230","v231","v232","v233","v234","v235","v236","v237","v238","v239","v240","v241",
      "v242","v243","v244","v245","v246","v247","v248","v249","v250","v251","v252","v253","v254","v255","a0","a1","a2","a3","a4","a5","a6","a7","a8","a9",
      "a10","a11","a12","a13","a14","a15","a16","a17","a18","a19","a20","a21","a22","a23","a24","a25","a26","a27","a28","a29","a30","a31","a32","a33",
      "a34","a35","a36","a37","a38","a39","a40","a41","a42","a43","a44","a45","a46","a47","a48","a49","a50","a51","a52","a53","a54","a55","a56","a57",
      "a58","a59","a60","a61","a62","a63","a64","a65","a66","a67","a68","a69","a70","a71","a72","a73","a74","a75","a76","a77","a78","a79","a80","a81",
      "a82","a83","a84","a85","a86","a87","a88","a89","a90","a91","a92","a93","a94","a95","a96","a97","a98","a99","a100","a101","a102","a103","a104","a105",
      "a106","a107","a108","a109","a110","a111","a112","a113","a114","a115","a116","a117","a118","a119","a120","a121","a122","a123","a124","a125","a126","a127","a128","a129",
      "a130","a131","a132","a133","a134","a135","a136","a137","a138","a139","a140","a141","a142","a143","a144","a145","a146","a147","a148","a149","a150","a151","a152","a153",
      "a154","a155","a156","a157","a158","a159","a160","a161","a162","a163","a164","a165","a166","a167","a168","a169","a170","a171","a172","a173","a174","a175","a176","a177",
      "a178","a179","a180","a181","a182","a183","a184","a185","a186","a187","a188","a189","a190","a191","a192","a193","a194","a195","a196","a197","a198","a199","a200","a201",
      "a202","a203","a204","a205","a206","a207","a208","a209","a210","a211","a212","a213","a214","a215","a216","a217","a218","a219","a220","a221","a222","a223","a224","a225",
      "a226","a227","a228","a229","a230","a231","a232","a233","a234","a235","a236","a237","a238","a239","a240","a241","a242","a243","a244","a245","a246","a247","a248","a249",
      "a250","a251","a252","a253","a254","a255","vcc","scc","memory");
}
extern "C" void kernel_launch(void* const* d_in, const int* in_sizes, int n_in, void* d_out, int out_size, void* d_ws, size_t ws_size, hipStream_t stream) {
  Args a;
  a.p[0]=d_in[0]; a.p[1]=d_in[1]; a.p[2]=d_in[2]; a.p[3]=d_in[3]; a.p[4]=d_in[4]; a.p[5]=d_in[6]; a.p[6]=d_in[7]; a.p[7]=d_in[8]; a.p[8]=d_in[9];
  a.p[9]=d_out; a.p[10]=d_ws; a.p[11]=d_ws;

  k_fused<<<256,256,0,stream>>>(a);
}
```
